```python
import torch

def _softmax(x, axis=-1):
    return torch.softmax(x, dim=axis)
def _gelu(x):
    return torch.nn.functional.gelu(x, approximate="tanh")
def _silu(x):
    return torch.nn.functional.silu(x)
def _sigmoid(x):
    return torch.sigmoid(x)
def _layernorm(x, axis=-1, eps=1e-5):
    m = x.mean(dim=axis, keepdim=True)
    v = x.var(dim=axis, keepdim=True, unbiased=False)
    return (x - m) / torch.sqrt(v + eps)
def _scatter_add(tgt, idx, src, axis=0):
    return tgt.clone().index_add_(axis, idx, src)
def _segment_sum(src, seg, n):
    out = torch.zeros((n,) + src.shape[1:], dtype=src.dtype)
    return out.index_add_(0, seg, src)
def _pad(x, pw):
    flat = tuple(v for pair in reversed(pw) for v in pair)
    return torch.nn.functional.pad(x, flat)

kernel_name = 'interleave_layers__sliding_block__dense_activation_silu__prompt'

def setup_inputs():

    _w = (lambda a: (lambda t: t.to(torch.bfloat16) if t.is_floating_point() else t)(torch.as_tensor(a)))
    def _A_setup():
        B, S, D = 1, 2048, 512
        return {
            "x": _w((lambda *s: torch.randn(*s, dtype=torch.bfloat16) * 0.05)(B, S, D)),
            "wqkv": _w((lambda *s: torch.randn(*s, dtype=torch.bfloat16) * 0.05)(D, 3 * D)),
            "wo": _w((lambda *s: torch.randn(*s, dtype=torch.bfloat16) * 0.05)(D, D)),
        }
    _A_in = _A_setup()

    def _B_setup():
        B, S, D, F = 1, 2048, 512, 1792
        return {
            "x": _w((lambda *s: torch.randn(*s, dtype=torch.bfloat16) * 0.05)(B, S, D)),
            "w1": _w((lambda *s: torch.randn(*s, dtype=torch.bfloat16) * 0.8)(D, F) * (D ** -0.5)),
            "w2": _w((lambda *s: torch.randn(*s, dtype=torch.bfloat16) * 0.8)(F, D) * (F ** -0.5)),
        }
    _B_in = _B_setup()

    return {
        "A_x": _A_in["x"],
        "A_wqkv": _A_in["wqkv"],
        "A_wo": _A_in["wo"],
        "B_x": _B_in["x"],
        "B_w1": _B_in["w1"],
        "B_w2": _B_in["w2"],
    }

def reference(A_x, A_wqkv, A_wo, B_x, B_w1, B_w2):
    def _A_ref(x, wqkv, wo):
        B, S, D = x.shape
        H = 32; Dh = D // H; W = 256
        qkv = (lambda x, *d: x.permute(*d))((x @ wqkv).reshape(B, S, 3, H, Dh), 2, 0, 3, 1, 4)
        q, k, v = qkv[0], qkv[1], qkv[2]
        scores = torch.einsum("bhqd,bhkd->bhqk", q, k) * (Dh ** -0.5)
        idx = torch.arange(S)
        mask = (idx[:, None] - idx[None, :] >= 0) & (idx[:, None] - idx[None, :] < W)
        scores = torch.where(mask[None, None], scores, -1e9)
        p = _softmax(scores, axis=-1)
        o = torch.einsum("bhqk,bhkd->bhqd", p, v)
        return (lambda x, *d: x.permute(*d))(o, 0, 2, 1, 3).reshape(B, S, D) @ wo

    def _B_ref(x, w1, w2):
        h = torch.einsum("bsd,df->bsf", x, w1)
        h = _silu(h)
        out = torch.einsum("bsf,fd->bsd", h, w2)
        return out

    x = A_x
    x = x + _A_ref(_layernorm(x, axis=-1), A_wqkv, A_wo)
    x = x + _B_ref(_layernorm(x, axis=-1), B_w1, B_w2)
    x = x + _A_ref(_layernorm(x, axis=-1), A_wqkv, A_wo)
    x = x + _B_ref(_layernorm(x, axis=-1), B_w1, B_w2)
    return x
```

```cpp
#include <hip/hip_runtime.h>
#include <hip/hip_bf16.h>
#include <stdint.h>

typedef short bf16x8 __attribute__((ext_vector_type(8)));
typedef float f32x2 __attribute__((ext_vector_type(2)));
typedef float f32x4 __attribute__((ext_vector_type(4)));
typedef float f32x16 __attribute__((ext_vector_type(16)));
typedef unsigned u32x2 __attribute__((ext_vector_type(2)));
typedef unsigned u32x4 __attribute__((ext_vector_type(4)));
typedef __bf16 bf16x2_t __attribute__((ext_vector_type(2)));
#define GAS __attribute__((address_space(1)))
#define LAS __attribute__((address_space(3)))
#define DEV __device__ __forceinline__
#define SB() __builtin_amdgcn_sched_barrier(0)

constexpr int S = 2048, D = 512, H = 32, F = 1792;
constexpr int NTEAM = 16, NMEM = 16;
constexpr float EPS = 1e-5f;
constexpr float LOG2E = 1.4426950408889634f;
constexpr float QSCALE = 0.25f * LOG2E;

constexpr size_t KB = 1024, MB = 1024 * 1024;
constexpr size_t WS_FLAGS = 0;
constexpr size_t WS_CSQ   = 64 * KB;
constexpr size_t WS_CS1   = WS_CSQ + 96 * KB;
constexpr size_t WS_WQKV  = 512 * KB;
constexpr size_t WS_WO    = WS_WQKV + 1536 * KB;
constexpr size_t WS_W1    = WS_WO + 512 * KB;
constexpr size_t WS_W2    = WS_W1 + 1792 * KB;
constexpr size_t WS_XF0   = WS_W2 + 1792 * KB;
constexpr size_t WS_ST0   = WS_XF0 + 2 * MB;
constexpr size_t WS_LAYER = WS_ST0 + 256 * KB;
constexpr size_t L_QF  = 0;
constexpr size_t L_KF  = L_QF + 2 * MB;
constexpr size_t L_VF  = L_KF + 2 * MB;
constexpr size_t L_OF  = L_VF + 2 * MB;
constexpr size_t L_X1F = L_OF + 2 * MB;
constexpr size_t L_ST1 = L_X1F + 2 * MB;
constexpr size_t L_HF  = L_ST1 + 256 * KB;
constexpr size_t L_X2F = L_HF + 7 * MB;
constexpr size_t L_ST2 = L_X2F + 2 * MB;
constexpr size_t L_SIZE = L_ST2 + 256 * KB;

struct Params {
    const unsigned short* x; const unsigned short* wqkv; const unsigned short* wo; const unsigned short* w1; const unsigned short* w2;
    unsigned short* out; unsigned char* ws;
};

DEV float bf2f(unsigned short b) { return __uint_as_float((unsigned)b << 16); }
DEV float bflo(unsigned u) { return __uint_as_float(u << 16); }
DEV float bfhi(unsigned u) { return __uint_as_float(u & 0xffff0000u); }
DEV unsigned pk2(float lo, float hi) { f32x2 v = {lo, hi}; bf16x2_t r = __builtin_convertvector(v, bf16x2_t); return __builtin_bit_cast(unsigned, r); }
DEV float xsum16(float v) { auto r = __builtin_amdgcn_permlane16_swap(__float_as_uint(v), __float_as_uint(v), false, false); return __uint_as_float(r[0]) + __uint_as_float(r[1]); }
DEV float xsum32(float v) { auto r = __builtin_amdgcn_permlane32_swap(__float_as_uint(v), __float_as_uint(v), false, false); return __uint_as_float(r[0]) + __uint_as_float(r[1]); }
DEV float xmax32(float v) { auto r = __builtin_amdgcn_permlane32_swap(__float_as_uint(v), __float_as_uint(v), false, false); return __builtin_fmaxf(__uint_as_float(r[0]), __uint_as_float(r[1])); }
DEV f32x4 mfma16(bf16x8 a, bf16x8 b, f32x4 c) { return __builtin_amdgcn_mfma_f32_16x16x32_bf16(a, b, c, 0, 0, 0); }
DEV f32x16 mfma32(bf16x8 a, bf16x8 b, f32x16 c) { return __builtin_amdgcn_mfma_f32_32x32x16_bf16(a, b, c, 0, 0, 0); }
DEV void glds16(const void* g, unsigned lds_base) {
    unsigned sv; asm volatile("s_mov_b32 %0, m0\n\ts_mov_b32 m0, %2\n\ts_nop 0\n\tglobal_load_lds_dwordx4 %1, off\n\ts_mov_b32 m0, %0" : "=&s"(sv) : "v"(g), "s"(lds_base) : "memory"); }
DEV unsigned rfl(unsigned v) { return __builtin_amdgcn_readfirstlane(v); }
#define WAITV0() asm volatile("s_waitcnt vmcnt(0)" ::: "memory")

DEV void block_map(int b, int& team, int& mem) {
    const int x = b & 7, r = b >> 3;
    team = 4 * (x >> 1) + (r >> 3); mem = 8 * (x & 1) + (r & 7);
}

DEV int kC16(int g, int e) { return 16 * (e >> 2) + 4 * g + (e & 3); }
DEV int kO(int g, int e) { return 16 * (g >> 1) + 8 * (e >> 2) + 4 * (g & 1) + (e & 3); }

__global__ __launch_bounds__(256) void k_prep(Params p) {
    const int lane = threadIdx.x & 63, gw = blockIdx.x * 4 + (threadIdx.x >> 6);
    const int n16 = lane & 15, g = lane >> 4;
    int id = gw;
    if (blockIdx.x == 0 && threadIdx.x < 64) {   }
    if (id < 5632) {
        const unsigned short* W; int ldw, col, s, ord = 0; size_t dst; float* cs = nullptr;
        if (id < 1536) { const int j = id / 96, r = id % 96; s = r / 6; const int t = r % 6; W = p.wqkv; ldw = 1536; col = (t >> 1) * 512 + 32 * j + 16 * (t & 1) + n16; dst = WS_WQKV + (size_t)id * KB;
            cs = (float*)(p.ws + WS_CSQ) + (j * 16 + s) * 96 + t * 16 + n16; }
        else if (id < 2048) { const int i = id - 1536, cb = i / 64, r = i % 64; s = r / 4; const int t = r % 4; W = p.wo; ldw = 512; col = 64 * cb + 16 * t + n16; dst = WS_WO + (size_t)i * KB; ord = 1; }
        else if (id < 3840) { const int i = id - 2048, j = i / 128, r = i % 128; s = r / 8; const int t = r % 8; W = p.w1; ldw = 1792; col = 128 * j + 16 * t + n16; dst = WS_W1 + (size_t)i * KB;
            cs = (float*)(p.ws + WS_CS1) + (j * 16 + s) * 128 + t * 16 + n16; }
        else { const int i = id - 3840, cb = i / 224, r = i % 224; s = r / 4; const int t = r % 4; W = p.w2; ldw = 512; col = 64 * cb + 16 * t + n16; dst = WS_W2 + (size_t)i * KB; }
        unsigned short v[8]; float sum = 0.f;
#pragma unroll
        for (int e = 0; e < 8; ++e) { const int k = 32 * s + (ord ? kO(g, e) : kC16(g, e)); v[e] = W[(size_t)k * ldw + col]; sum += bf2f(v[e]); }
        u32x4 o = {(unsigned)v[0] | ((unsigned)v[1] << 16), (unsigned)v[2] | ((unsigned)v[3] << 16), (unsigned)v[4] | ((unsigned)v[5] << 16), (unsigned)v[6] | ((unsigned)v[7] << 16)};
        *(u32x4*)(p.ws + dst + lane * 16) = o;
        sum = xsum32(xsum16(sum));
        if (cs && g == 0) *cs = sum;
    } else if (id < 5632 + 2048) {
        const int i = id - 5632, tt = i >> 4, s = i & 15;
        const unsigned short* xr = p.x + (size_t)(16 * tt + n16) * D + 32 * s + 4 * g;
        const u32x2 a = *(const u32x2*)xr, b = *(const u32x2*)(xr + 16);
        *(u32x4*)(p.ws + WS_XF0 + (size_t)i * KB + lane * 16) = (u32x4){a.x, a.y, b.x, b.y};
        float f[8] = {bflo(a.x), bfhi(a.x), bflo(a.y), bfhi(a.y), bflo(b.x), bfhi(b.x), bflo(b.y), bfhi(b.y)};
        float sm = 0.f;
#pragma unroll
        for (int e = 0; e < 8; ++e) sm += f[e];
        const float mean = xsum32(xsum16(sm)) * (1.f / 32.f); float q = 0.f;
#pragma unroll
        for (int e = 0; e < 8; ++e) { const float d = f[e] - mean; q += d * d; }
        q = xsum32(xsum16(q));
        if (g == 0) *(f32x2*)(p.ws + WS_ST0 + ((size_t)i * 16 + n16) * 8) = (f32x2){mean, q};
    }
}

DEV void load_stats(const unsigned char* st, int tt, int lane, float& a, float& b) {
    const int tok = lane & 15, g = lane >> 4;
    f32x2 pr[4];
#pragma unroll
    for (int i = 0; i < 4; ++i) pr[i] = *(const f32x2*)(st + (((size_t)tt * 16 + 4 * g + i) * 16 + tok) * 8);
    float ms = (pr[0].x + pr[1].x) + (pr[2].x + pr[3].x);
    const float mean = xsum32(xsum16(ms)) * (1.f / 16.f);
    float q = 0.f;
#pragma unroll
    for (int i = 0; i < 4; ++i) { const float d = pr[i].x - mean; q += pr[i].y + 32.f * d * d; }
    q = xsum32(xsum16(q));
    const float rstd = __builtin_amdgcn_rsqf(q * (1.f / 512.f) + EPS);
    a = rstd; b = -mean * rstd;
}

template <int NFR> DEV void dma_frags(const unsigned char* src, unsigned lds_dst, int wid, int lane) {
#pragma unroll
    for (int i = 0; i < (NFR + 7) / 8; ++i) { const int f = i * 8 + wid; if (NFR % 8 == 0 || f < NFR) glds16(src + (size_t)f * KB + lane * 16, rfl(lds_dst + f * 1024)); }
}

template <int KS, int NT, int NTL, int SWAPV>
DEV void gemm_loop(f32x4 (&acc)[NT], const bf16x8 (&b)[KS], LAS const unsigned char* wl) {
    bf16x8 fa[2][NT];
#pragma unroll
    for (int t = 0; t < NT; ++t) fa[0][t] = *(LAS const bf16x8*)(wl + t * 1024);
    SB();
#pragma unroll
    for (int s = 0; s < KS; ++s) {
#pragma unroll
        for (int t = 0; t < NT; ++t) {
            if (s + 1 < KS) fa[(s + 1) & 1][t] = *(LAS const bf16x8*)(wl + ((s + 1) * NTL + t) * 1024);
            if (t >= SWAPV) acc[t] = mfma16(b[s], fa[s & 1][t], acc[t]); else acc[t] = mfma16(fa[s & 1][t], b[s], acc[t]);
            SB();
        }
    }
}

__global__ __launch_bounds__(512, 2) void k_qkv(Params p, int layer) {
    extern __shared__ __attribute__((aligned(16))) unsigned char lds[];
    int team, mem; block_map(blockIdx.x, team, mem);
    const int tid = threadIdx.x, lane = tid & 63, wid = rfl(tid >> 6), tok = lane & 15, g = lane >> 4;
    const unsigned lds0 = (unsigned)(uintptr_t)lds;
    unsigned char* wl = p.ws + WS_LAYER + (size_t)layer * L_SIZE;
    const unsigned char* xf = layer == 0 ? p.ws + WS_XF0 : p.ws + WS_LAYER + L_X2F;
    const unsigned char* st = layer == 0 ? p.ws + WS_ST0 : p.ws + WS_LAYER + L_ST2;
    dma_frags<96>(p.ws + WS_WQKV + (size_t)mem * 96 * KB, lds0, wid, lane);
    LAS float* csl = (LAS float*)(lds + 96 * 1024);
    LAS f32x2* stl = (LAS f32x2*)(lds + 96 * 1024 + 512) + wid * 16;
    if (tid < 96) { const float* c = (const float*)(p.ws + WS_CSQ) + mem * 16 * 96 + tid; float s = 0.f;
#pragma unroll
        for (int i = 0; i < 16; ++i) s += c[i * 96];
        csl[tid] = s; }
    const int tt = 8 * team + wid;
    bf16x8 b[16];
#pragma unroll
    for (int s = 0; s < 16; ++s) b[s] = *(const bf16x8*)(xf + ((size_t)tt * 16 + s) * KB + lane * 16);
    float la, lb; load_stats(st, tt, lane, la, lb);
    if (g == 0) stl[tok] = (f32x2){la, lb};
    WAITV0(); __syncthreads();
    f32x4 acc[6];
#pragma unroll
    for (int t = 0; t < 6; ++t) acc[t] = (f32x4){0.f, 0.f, 0.f, 0.f};
    gemm_loop<16, 6, 6, 4>(acc, b, (LAS const unsigned char*)lds + lane * 16);
    const int blk = 4 * team + (wid >> 1), half = wid & 1;
#pragma unroll
    for (int t = 0; t < 4; ++t) {
        const f32x4 cs = *(LAS const f32x4*)(csl + 16 * t + 4 * g);
        float v[4];
#pragma unroll
        for (int r = 0; r < 4; ++r) { v[r] = la * acc[t][r] + lb * cs[r]; if (t < 2) v[r] *= QSCALE; }
        const u32x2 o = {pk2(v[0], v[1]), pk2(v[2], v[3])};
        const int head = 2 * mem + (t & 1);
        unsigned char* dst = wl + (t < 2 ? L_QF : L_KF) + ((size_t)blk * 32 + head) * KB + (16 * half + tok + 32 * (g >> 1)) * 16 + 8 * (g & 1);
        *(u32x2*)dst = o;
    }
    {
        const f32x4 s01 = *(LAS const f32x4*)(stl + 4 * g), s23 = *(LAS const f32x4*)(stl + 4 * g + 2);
#pragma unroll
        for (int t = 4; t < 6; ++t) {
            const float cs = csl[16 * t + tok];
            const float v0 = s01[0] * acc[t][0] + s01[1] * cs, v1 = s01[2] * acc[t][1] + s01[3] * cs, v2 = s23[0] * acc[t][2] + s23[1] * cs, v3 = s23[2] * acc[t][3] + s23[3] * cs;
            const u32x2 o = {pk2(v0, v1), pk2(v2, v3)};
            const int head = 2 * mem + (t & 1);
            unsigned char* dst = wl + L_VF + (((size_t)blk * 32 + head) * 2 + half) * 512 + ((g & 1) * 16 + tok) * 16 + 8 * (g >> 1);
            *(u32x2*)dst = o;
        }
    }
}

constexpr int AL_K = 0, AL_V = 24 * 1024, AL_Q = 72 * 1024, AL_END = 80 * 1024;
DEV void attn_unit(LAS const unsigned char* lds, int team, int qb, int hd, int lane, f32x16& o) {
    const int q32 = lane & 31, h = lane >> 5;
    const bf16x8 qf = *(LAS const bf16x8*)(lds + AL_Q + (qb * 2 + hd) * 1024 + lane * 16);
    const int first = team >= 2 ? 0 : 8 - 4 * team - qb;
    bf16x8 kf[9];
#pragma unroll
    for (int i = 0; i < 9; ++i) if (i >= first) kf[i] = *(LAS const bf16x8*)(lds + AL_K + ((qb + i) * 2 + hd) * 1024 + lane * 16);
    const f32x16 zero = {};
    float m = -INFINITY;
#pragma unroll
    for (int i = 0; i < 9; ++i) if (i >= first) {
        f32x16 s = mfma32(kf[i], qf, zero);
        if (i == 0) {
#pragma unroll
            for (int r = 0; r < 16; ++r) { const int key = (r & 3) + 8 * (r >> 2) + 4 * h; if (key <= q32) s[r] = -INFINITY; }
        }
        if (i == 8) {
#pragma unroll
            for (int r = 0; r < 16; ++r) { const int key = (r & 3) + 8 * (r >> 2) + 4 * h; if (key > q32) s[r] = -INFINITY; }
        }
#pragma unroll
        for (int r = 0; r < 16; r += 2) m = __builtin_fmaxf(__builtin_fmaxf(m, s[r]), s[r + 1]);
    }
    m = xmax32(m);
    f32x16 nm;
#pragma unroll
    for (int r = 0; r < 16; ++r) nm[r] = -m;
    o = zero;
#pragma unroll
    for (int i = 0; i < 9; ++i) if (i >= first) {
        f32x16 s = mfma32(kf[i], qf, nm);
        if (i == 0) {
#pragma unroll
            for (int r = 0; r < 16; ++r) { const int key = (r & 3) + 8 * (r >> 2) + 4 * h; if (key <= q32) s[r] = -INFINITY; }
        }
        if (i == 8) {
#pragma unroll
            for (int r = 0; r < 16; ++r) { const int key = (r & 3) + 8 * (r >> 2) + 4 * h; if (key > q32) s[r] = -INFINITY; }
        }
        u32x4 p0, p1;
#pragma unroll
        for (int e = 0; e < 4; ++e) {
            p0[e] = pk2(__builtin_amdgcn_exp2f(s[2 * e]), __builtin_amdgcn_exp2f(s[2 * e + 1]));
            p1[e] = pk2(__builtin_amdgcn_exp2f(s[8 + 2 * e]), __builtin_amdgcn_exp2f(s[8 + 2 * e + 1]));
        }
        const bf16x8 v0 = *(LAS const bf16x8*)(lds + AL_V + (((qb + i) * 2 + hd) * 2 + 0) * 1024 + lane * 16);
        const bf16x8 v1 = *(LAS const bf16x8*)(lds + AL_V + (((qb + i) * 2 + hd) * 2 + 1) * 1024 + lane * 16);
        o = mfma32(v0, __builtin_bit_cast(bf16x8, p0), o);
        o = mfma32(v1, __builtin_bit_cast(bf16x8, p1), o);
    }
}

__global__ __launch_bounds__(512, 2) void k_attn(Params p, int layer) {
    extern __shared__ __attribute__((aligned(16))) unsigned char lds[];
    int team, mem; block_map(blockIdx.x, team, mem);
    const int tid = threadIdx.x, lane = tid & 63, wid = rfl(tid >> 6);
    const unsigned lds0 = (unsigned)(uintptr_t)lds;
    unsigned char* wl = p.ws + WS_LAYER + (size_t)layer * L_SIZE;
    for (int f = wid; f < 48; f += 8) if (lane & 16) *(LAS u32x4*)(lds + AL_V + f * 1024 + lane * 16) = (u32x4){0x3f803f80u, 0x3f803f80u, 0x3f803f80u, 0x3f803f80u};
    for (int i = wid; i < 24; i += 8) { const int kb = i >> 1, hd = i & 1, gb = 4 * team - 8 + kb;
        if (gb >= 0) {
            const size_t bh = (size_t)gb * 32 + 2 * mem + hd;
            glds16(wl + L_KF + bh * KB + lane * 16, rfl(lds0 + AL_K + i * 1024));
            if (!(lane & 16)) {
                const int slot = (lane >> 5) * 16 + (lane & 15);
                glds16(wl + L_VF + (bh * 2 + 0) * 512 + slot * 16, rfl(lds0 + AL_V + (i * 2 + 0) * 1024));
                glds16(wl + L_VF + (bh * 2 + 1) * 512 + slot * 16, rfl(lds0 + AL_V + (i * 2 + 1) * 1024));
            }
        } }
    { const int qb = wid >> 1, hd = wid & 1; glds16(wl + L_QF + ((size_t)(4 * team + qb) * 32 + 2 * mem + hd) * KB + lane * 16, rfl(lds0 + AL_Q + wid * 1024)); }
    WAITV0(); __syncthreads();
    const int qb = wid >> 1, hd = wid & 1, q32 = lane & 31, h = lane >> 5;
    f32x16 o; attn_unit((LAS const unsigned char*)lds, team, qb, hd, lane, o);
    const float rl = __builtin_amdgcn_rcpf(o[8]);
    const u32x4 w = {pk2(o[0] * rl, o[1] * rl), pk2(o[2] * rl, o[3] * rl), pk2(o[4] * rl, o[5] * rl), pk2(o[6] * rl, o[7] * rl)};
    const int tt = 8 * team + 2 * qb + (q32 >> 4);
    *(u32x4*)(wl + L_OF + ((size_t)tt * 16 + mem) * KB + ((q32 & 15) + 16 * (2 * hd + h)) * 16) = w;
}

DEV void khalf_exchange(f32x4 (&acc)[4], LAS unsigned char* xb, int wid, int lane, float (&v)[8]) {
    const int kh = wid >> 2;
    LAS f32x4* mine = (LAS f32x4*)(xb + wid * 2048) + lane;
    LAS const f32x4* theirs = (LAS const f32x4*)(xb + (wid ^ 4) * 2048) + lane;
    mine[0] = kh ? acc[0] : acc[2]; mine[64] = kh ? acc[1] : acc[3];
    __syncthreads();
    const f32x4 t0 = theirs[0], t1 = theirs[64];
    const f32x4 k0 = kh ? acc[2] : acc[0], k1 = kh ? acc[3] : acc[1];
#pragma unroll
    for (int r = 0; r < 4; ++r) { v[r] = k0[r] + t0[r]; v[4 + r] = k1[r] + t1[r]; }
}
DEV u32x4 resid_out(const float (&v)[8], u32x4 res, unsigned char* xf, unsigned char* st, int tt, int s, int lane) {
    float f[8] = {bflo(res.x) + v[0], bfhi(res.x) + v[1], bflo(res.y) + v[2], bfhi(res.y) + v[3], bflo(res.z) + v[4], bfhi(res.z) + v[5], bflo(res.w) + v[6], bfhi(res.w) + v[7]};
    const u32x4 o = {pk2(f[0], f[1]), pk2(f[2], f[3]), pk2(f[4], f[5]), pk2(f[6], f[7])};
    if (xf) {
        *(u32x4*)(xf + ((size_t)tt * 16 + s) * KB + lane * 16) = o;
        const float r[8] = {bflo(o.x), bfhi(o.x), bflo(o.y), bfhi(o.y), bflo(o.z), bfhi(o.z), bflo(o.w), bfhi(o.w)};
        float sm = ((r[0] + r[1]) + (r[2] + r[3])) + ((r[4] + r[5]) + (r[6] + r[7]));
        const float mean = xsum32(xsum16(sm)) * (1.f / 32.f); float q = 0.f;
#pragma unroll
        for (int e = 0; e < 8; ++e) { const float d = r[e] - mean; q += d * d; }
        q = xsum32(xsum16(q));
        if (lane < 16) *(f32x2*)(st + (((size_t)tt * 16 + s) * 16 + lane) * 8) = (f32x2){mean, q};
    }
    return o;
}

__global__ __launch_bounds__(512, 2) void k_wo(Params p, int layer) {
    extern __shared__ __attribute__((aligned(16))) unsigned char lds[];
    int team, mem; block_map(blockIdx.x, team, mem);
    const int tid = threadIdx.x, lane = tid & 63, wid = rfl(tid >> 6);
    const unsigned lds0 = (unsigned)(uintptr_t)lds;
    unsigned char* wl = p.ws + WS_LAYER + (size_t)layer * L_SIZE;
    const int cb = mem >> 1, tt = 8 * team + 4 * (mem & 1) + (wid & 3), kh = wid >> 2;
    dma_frags<64>(p.ws + WS_WO + (size_t)cb * 64 * KB, lds0, wid, lane);
    bf16x8 b[8];
#pragma unroll
    for (int s = 0; s < 8; ++s) b[s] = *(const bf16x8*)(wl + L_OF + ((size_t)tt * 16 + 8 * kh + s) * KB + lane * 16);
    const unsigned char* xf = layer == 0 ? p.ws + WS_XF0 : p.ws + WS_LAYER + L_X2F;
    const u32x4 res = *(const u32x4*)(xf + ((size_t)tt * 16 + 2 * cb + kh) * KB + lane * 16);
    WAITV0(); __syncthreads();
    f32x4 acc[4];
#pragma unroll
    for (int t = 0; t < 4; ++t) acc[t] = (f32x4){0.f, 0.f, 0.f, 0.f};
    gemm_loop<8, 4, 4, 99>(acc, b, (LAS const unsigned char*)lds + kh * 8 * 4 * 1024 + lane * 16);
    float v[8]; khalf_exchange(acc, (LAS unsigned char*)lds + 64 * 1024, wid, lane, v);
    resid_out(v, res, wl + L_X1F, wl + L_ST1, tt, 2 * cb + kh, lane);
}

__global__ __launch_bounds__(512, 2) void k_ffn1(Params p, int layer) {
    extern __shared__ __attribute__((aligned(16))) unsigned char lds[];
    int team, mem; block_map(blockIdx.x, team, mem);
    if (mem >= 14) return;
    const int tid = threadIdx.x, lane = tid & 63, wid = rfl(tid >> 6), g = lane >> 4;
    const unsigned lds0 = (unsigned)(uintptr_t)lds;
    unsigned char* wl = p.ws + WS_LAYER + (size_t)layer * L_SIZE;
    dma_frags<128>(p.ws + WS_W1 + (size_t)mem * 128 * KB, lds0, wid, lane);
    LAS float* csl = (LAS float*)(lds + 128 * 1024);
    if (tid < 128) { const float* c = (const float*)(p.ws + WS_CS1) + mem * 16 * 128 + tid; float s = 0.f;
#pragma unroll
        for (int i = 0; i < 16; ++i) s += c[i * 128];
        csl[tid] = s; }
    const int tt = 8 * team + wid;
    bf16x8 b[16];
#pragma unroll
    for (int s = 0; s < 16; ++s) b[s] = *(const bf16x8*)(wl + L_X1F + ((size_t)tt * 16 + s) * KB + lane * 16);
    float la, lb; load_stats(wl + L_ST1, tt, lane, la, lb);
    WAITV0(); __syncthreads();
    f32x4 acc[8];
#pragma unroll
    for (int t = 0; t < 8; ++t) acc[t] = (f32x4){0.f, 0.f, 0.f, 0.f};
    gemm_loop<16, 8, 8, 99>(acc, b, (LAS const unsigned char*)lds + lane * 16);
#pragma unroll
    for (int u = 0; u < 4; ++u) {
        float hv[8];
#pragma unroll
        for (int i = 0; i < 2; ++i) { const int t = 2 * u + i; const f32x4 cs = *(LAS const f32x4*)(csl + 16 * t + 4 * g);
#pragma unroll
            for (int r = 0; r < 4; ++r) { const float v = la * acc[t][r] + lb * cs[r]; hv[4 * i + r] = v * __builtin_amdgcn_rcpf(1.f + __builtin_amdgcn_exp2f(-LOG2E * v)); } }
        const u32x4 o = {pk2(hv[0], hv[1]), pk2(hv[2], hv[3]), pk2(hv[4], hv[5]), pk2(hv[6], hv[7])};
        *(u32x4*)(wl + L_HF + ((size_t)tt * 56 + 4 * mem + u) * KB + lane * 16) = o;
    }
}

__global__ __launch_bounds__(512, 2) void k_ffn2(Params p, int layer) {
    extern __shared__ __attribute__((aligned(16))) unsigned char lds[];
    int team, mem; block_map(blockIdx.x, team, mem);
    const int tid = threadIdx.x, lane = tid & 63, wid = rfl(tid >> 6);
    const unsigned lds0 = (unsigned)(uintptr_t)lds;
    unsigned char* wl = p.ws + WS_LAYER + (size_t)layer * L_SIZE;
    const int cb = mem >> 1, tt = 8 * team + 4 * (mem & 1) + (wid & 3), kh = wid >> 2;
    const unsigned char* w2 = p.ws + WS_W2 + (size_t)cb * 224 * KB;
    f32x4 acc[4];
#pragma unroll
    for (int t = 0; t < 4; ++t) acc[t] = (f32x4){0.f, 0.f, 0.f, 0.f};
    const u32x4 res = *(const u32x4*)(wl + L_X1F + ((size_t)tt * 16 + 2 * cb + kh) * KB + lane * 16);
#pragma unroll
    for (int ps = 0; ps < 2; ++ps) {
        if (ps) __syncthreads();
        dma_frags<112>(w2 + (size_t)ps * 112 * KB, lds0, wid, lane);
        bf16x8 b[14];
#pragma unroll
        for (int s = 0; s < 14; ++s) b[s] = *(const bf16x8*)(wl + L_HF + ((size_t)tt * 56 + 28 * ps + 14 * kh + s) * KB + lane * 16);
        WAITV0(); __syncthreads();
        gemm_loop<14, 4, 4, 99>(acc, b, (LAS const unsigned char*)lds + kh * 14 * 4 * 1024 + lane * 16);
    }
    float v[8]; khalf_exchange(acc, (LAS unsigned char*)lds + 112 * 1024, wid, lane, v);
    if (layer == 0) resid_out(v, res, wl + L_X2F, wl + L_ST2, tt, 2 * cb + kh, lane);
    else {
        const u32x4 o = resid_out(v, res, nullptr, nullptr, tt, 0, lane);
        unsigned short* dst = p.out + (size_t)(16 * tt + (lane & 15)) * D + 64 * cb + 32 * kh + 4 * (lane >> 4);
        *(u32x2*)dst = (u32x2){o.x, o.y}; *(u32x2*)(dst + 16) = (u32x2){o.z, o.w};
    }
}

extern "C" void kernel_launch(void* const* d_in, const int* in_sizes, int n_in, void* d_out, int out_size, void* d_ws, size_t ws_size, hipStream_t stream) {
    Params p;
    p.x = (const unsigned short*)d_in[0]; p.wqkv = (const unsigned short*)d_in[1]; p.wo = (const unsigned short*)d_in[2];
    p.w1 = (const unsigned short*)d_in[4]; p.w2 = (const unsigned short*)d_in[5];
    p.out = (unsigned short*)d_out; p.ws = (unsigned char*)d_ws;
    hipFuncSetAttribute((const void*)k_qkv, hipFuncAttributeMaxDynamicSharedMemorySize, 100 * 1024);
    hipFuncSetAttribute((const void*)k_attn, hipFuncAttributeMaxDynamicSharedMemorySize, 80 * 1024);
    hipFuncSetAttribute((const void*)k_wo, hipFuncAttributeMaxDynamicSharedMemorySize, 80 * 1024);
    hipFuncSetAttribute((const void*)k_ffn1, hipFuncAttributeMaxDynamicSharedMemorySize, 130 * 1024);
    hipFuncSetAttribute((const void*)k_ffn2, hipFuncAttributeMaxDynamicSharedMemorySize, 128 * 1024);
    k_prep<<<(5632 + 2048) / 4, 256, 0, stream>>>(p);
    for (int layer = 0; layer < 2; ++layer) {
        k_qkv<<<256, 512, 100 * 1024, stream>>>(p, layer);
        k_attn<<<256, 512, 80 * 1024, stream>>>(p, layer);
        k_wo<<<256, 512, 80 * 1024, stream>>>(p, layer);
        k_ffn1<<<256, 512, 130 * 1024, stream>>>(p, layer);
        k_ffn2<<<256, 512, 128 * 1024, stream>>>(p, layer);
    }
}
```

```cpp
#include <hip/hip_runtime.h>
#include <hip/hip_bf16.h>
#include <stdint.h>

typedef short bf16x8 __attribute__((ext_vector_type(8)));
typedef float f32x2 __attribute__((ext_vector_type(2)));
typedef float f32x4 __attribute__((ext_vector_type(4)));
typedef float f32x8 __attribute__((ext_vector_type(8)));
typedef float f32x16 __attribute__((ext_vector_type(16)));
typedef unsigned u32x2 __attribute__((ext_vector_type(2)));
typedef unsigned u32x4 __attribute__((ext_vector_type(4)));
typedef __bf16 bf16x2_t __attribute__((ext_vector_type(2)));
#define GAS __attribute__((address_space(1)))
#define LAS __attribute__((address_space(3)))
#define DEV __device__ __forceinline__
#define SB() __builtin_amdgcn_sched_barrier(0)
typedef __amdgpu_buffer_rsrc_t rsrc_t;

constexpr int S = 2048, D = 512, H = 32, F = 1792;
constexpr float EPS = 1e-5f;
constexpr float LOG2E = 1.4426950408889634f;
constexpr float QSCALE = 0.25f * LOG2E;

constexpr unsigned KB = 1024, MB = 1024 * 1024;
constexpr unsigned WS_TMO   = 0;
constexpr unsigned WS_CSQ   = 64 * KB;
constexpr unsigned WS_CS1   = WS_CSQ + 192 * KB;
constexpr unsigned WS_WQKV  = 512 * KB;
constexpr unsigned WS_WO    = WS_WQKV + 1536 * KB;
constexpr unsigned WS_W1    = WS_WO + 512 * KB;
constexpr unsigned WS_W2    = WS_W1 + 1792 * KB;
constexpr unsigned WS_XF0   = WS_W2 + 1792 * KB;
constexpr unsigned WS_ST0   = WS_XF0 + 2 * MB;
constexpr unsigned WS_LAYER = WS_ST0 + 512 * KB;
constexpr unsigned L_KV  = 0;
constexpr unsigned L_OF  = L_KV + 4 * MB;
constexpr unsigned L_X1F = L_OF + 2 * MB;
constexpr unsigned L_HF  = L_X1F + 2 * MB;
constexpr unsigned L_X2F = L_HF + 7 * MB;
constexpr unsigned L_ST1 = L_X2F + 2 * MB;
constexpr unsigned L_ST2 = L_ST1 + 512 * KB;
constexpr unsigned L_SIZE = L_ST2 + 512 * KB;
constexpr unsigned WSTR = 128;
constexpr unsigned WS_FLAGS = 64 * MB;
constexpr unsigned FL_KV = 0, FL_O = FL_KV + 64 * 16 * 2 * WSTR, FL_X1 = FL_O + 64 * 32 * WSTR, FL_H = FL_X1 + 64 * 32 * WSTR, FL_X2 = FL_H + 64 * 32 * WSTR, FL_LAYER = FL_X2 + 64 * 32 * WSTR;
constexpr unsigned WS_END = 72 * MB;
static_assert(WS_LAYER + 2 * L_SIZE <= WS_FLAGS && WS_FLAGS + 2 * FL_LAYER <= WS_END, "workspace map");

constexpr int NCW = 8, NLW = 4, NTHR = 64 * (NCW + NLW);
constexpr int SLOT = 16384, NSLOT = 5;
constexpr int LD_RING = 0;
constexpr int LD_XA = 80 * 1024;
constexpr int LD_K = 96 * 1024;
constexpr int LD_V = 120 * 1024;
constexpr int LD_XB = LD_V;
constexpr int LD_XC0 = LD_K + 16 * 1024, LD_XC1 = LD_V + 16 * 1024;
constexpr int LD_Q = 144 * 1024;
constexpr int LD_MISC = 152 * 1024;
constexpr int LD_CSQ = LD_MISC, LD_CS1 = LD_MISC + 384, LD_ONES = LD_MISC + 896, LD_CTR = LD_MISC + 1024, LD_ST = LD_MISC + 1792  , LD_AB = LD_ST + 2048  , LD_STAMP = LD_AB + 2048;
constexpr int LD_DUMMY = LD_CTR + 256;
constexpr int C_FULL = LD_CTR, C_FREE = LD_CTR + 32, C_LOCAL = LD_CTR + 64, C_HALO = LD_CTR + 96, C_XF = LD_CTR + 128, C_XRD = LD_CTR + 160, C_ATT = LD_CTR + 176;
constexpr int LDS_BYTES = 160 * 1024;
static_assert(LD_STAMP + 8 * 64 * 4 <= LDS_BYTES + 512, "LDS map");
constexpr int slot_of(int c) { return c % NSLOT; }
constexpr int uses_of(int slot) { return slot == 4 ? 6 : 7; }

struct Params {
    const unsigned short* x; const unsigned short* wqkv; const unsigned short* wo; const unsigned short* w1; const unsigned short* w2;
    unsigned short* out; unsigned char* ws;
};

#define STAMPV(kid, i)
#define STAMPW(kid, i, ...) (void)(__VA_ARGS__)
#define STAMP_RT(i)
#define STAMP_DUMP()
#define STAMP_INIT()

DEV float bf2f(unsigned short b) { return __uint_as_float((unsigned)b << 16); }
DEV float bflo(unsigned u) { return __uint_as_float(u << 16); }
DEV float bfhi(unsigned u) { return __uint_as_float(u & 0xffff0000u); }
DEV unsigned pk2(float lo, float hi) { f32x2 v = {lo, hi}; bf16x2_t r = __builtin_convertvector(v, bf16x2_t); return __builtin_bit_cast(unsigned, r); }
DEV float xsum32(float v) { auto r = __builtin_amdgcn_permlane32_swap(__float_as_uint(v), __float_as_uint(v), false, false); return __uint_as_float(r[0]) + __uint_as_float(r[1]); }
DEV float xmax32(float v) { auto r = __builtin_amdgcn_permlane32_swap(__float_as_uint(v), __float_as_uint(v), false, false); return __builtin_fmaxf(__uint_as_float(r[0]), __uint_as_float(r[1])); }
DEV f32x16 mfma32(bf16x8 a, bf16x8 b, f32x16 c) { return __builtin_amdgcn_mfma_f32_32x32x16_bf16(a, b, c, 0, 0, 0); }
DEV float dot2(unsigned a, unsigned b, float c) { return __builtin_amdgcn_fdot2_f32_bf16(__builtin_bit_cast(bf16x2_t, a), __builtin_bit_cast(bf16x2_t, b), c, false); }
DEV unsigned opq(unsigned x) { asm volatile("" : "+s"(x)); return x; }
DEV int opqv(int x) { asm volatile("" : "+v"(x)); return x; }
DEV unsigned rfl(unsigned v) { return __builtin_amdgcn_readfirstlane(v); }
#define WAITV(n) asm volatile("s_waitcnt vmcnt(" #n ")" ::: "memory")
constexpr int SC1 = 16;
DEV bf16x8 ldf(rsrc_t r, unsigned vo, unsigned so) { return __builtin_bit_cast(bf16x8, __builtin_amdgcn_raw_buffer_load_b128(r, vo, so, SC1)); }
DEV u32x4 ld16(rsrc_t r, unsigned vo, unsigned so) { return __builtin_amdgcn_raw_buffer_load_b128(r, vo, so, SC1); }
DEV u32x4 ldw(rsrc_t r, unsigned vo, unsigned so) { return __builtin_amdgcn_raw_buffer_load_b128(r, vo, so, 0); }
DEV void st16(rsrc_t r, unsigned vo, unsigned so, u32x4 v) { __builtin_amdgcn_raw_buffer_store_b128(v, r, vo, so, SC1); asm volatile("s_nop 1" :: "v"(v) : "memory"); }

DEV void block_map(int b, int& team, int& mem) {
    const int x = b & 7, r = b >> 3;
    team = 2 * x + (r >> 4); mem = r & 15;
}
DEV int kperm(int h, int e) { return 8 * (e >> 2) + 4 * h + (e & 3); }

__global__ __launch_bounds__(256) void k_prep(Params p) {
    const int lane = threadIdx.x & 63, id = blockIdx.x * 4 + (threadIdx.x >> 6);
    const int n32 = lane & 31, h = lane >> 5;
    for (unsigned i = blockIdx.x * 256 + threadIdx.x; i < 2 * FL_LAYER / 4; i += gridDim.x * 256) ((unsigned*)(p.ws + WS_FLAGS))[i] = 0u;
    if (blockIdx.x == 0 && threadIdx.x == 0) *(unsigned*)(p.ws + WS_TMO) = 0u;
    if (id < 5632) {
        const unsigned short* W; int ldw_, col, s; size_t dst; float* cs = nullptr;
        if (id < 1536) { const int j = id / 96, r = id % 96, c = r / 12, kh = (r / 6) & 1, jj = (r / 3) & 1, t = r % 3; s = 16 * kh + 2 * c + jj;
            W = p.wqkv; ldw_ = 1536; col = t * 512 + 32 * j + n32; dst = WS_WQKV + (size_t)id * KB; cs = (float*)(p.ws + WS_CSQ) + (j * 32 + s) * 96 + t * 32 + n32; }
        else if (id < 2048) { const int i = id - 1536, cb = i / 64, r = i % 64, c = r >> 4, kq = (r >> 2) & 3, jj = (r >> 1) & 1, t = r & 1; s = 8 * kq + 2 * c + jj;
            W = p.wo; ldw_ = 512; col = 64 * cb + 16 * ((2 * t + (n32 >> 4)) ^ kq) + (n32 & 15); dst = WS_WO + (size_t)i * KB; }
        else if (id < 3840) { const int i = id - 2048, j = i / 128, r = i % 128, c = r >> 4, kh = (r >> 3) & 1, jj = (r >> 2) & 1, t = r & 3; s = 16 * kh + 2 * c + jj;
            W = p.w1; ldw_ = 1792; col = 128 * j + 32 * (t ^ (2 * kh)) + n32; dst = WS_W1 + (size_t)i * KB; cs = (float*)(p.ws + WS_CS1) + (j * 32 + s) * 128 + (t ^ (2 * kh)) * 32 + n32; }
        else { const int i = id - 3840, cb = i / 224, r = i % 224, c = r >> 4, kq = (r >> 2) & 3, jj = (r >> 1) & 1, t = r & 1; s = 8 * c + 2 * kq + jj;
            W = p.w2; ldw_ = 512; col = 64 * cb + 16 * ((2 * t + (n32 >> 4)) ^ kq) + (n32 & 15); dst = WS_W2 + (size_t)i * KB; }
        unsigned short v[8]; float sum = 0.f;
#pragma unroll
        for (int e = 0; e < 8; ++e) { const int k = 16 * s + kperm(h, e); v[e] = W[(size_t)k * ldw_ + col]; sum += bf2f(v[e]); }
        u32x4 o = {(unsigned)v[0] | ((unsigned)v[1] << 16), (unsigned)v[2] | ((unsigned)v[3] << 16), (unsigned)v[4] | ((unsigned)v[5] << 16), (unsigned)v[6] | ((unsigned)v[7] << 16)};
        *(u32x4*)(p.ws + dst + lane * 16) = o;
        sum = xsum32(sum);
        if (cs && h == 0) *cs = sum;
    } else if (id < 5632 + 2048) {
        const int i = id - 5632, tb = i >> 5, s = i & 31;
        const unsigned short* xr = p.x + (size_t)(32 * tb + n32) * D + 16 * s + 4 * h;
        const u32x2 a = *(const u32x2*)xr, b = *(const u32x2*)(xr + 8);
        *(u32x4*)(p.ws + WS_XF0 + (size_t)i * KB + lane * 16) = (u32x4){a.x, a.y, b.x, b.y};
        const float f[8] = {bflo(a.x), bfhi(a.x), bflo(a.y), bfhi(a.y), bflo(b.x), bfhi(b.x), bflo(b.y), bfhi(b.y)};
        float sm = 0.f;
#pragma unroll
        for (int e = 0; e < 8; ++e) sm += f[e];
        const float mean = xsum32(sm) * (1.f / 16.f); float q = 0.f;
#pragma unroll
        for (int e = 0; e < 8; ++e) { const float d = f[e] - mean; q += d * d; }
        q = xsum32(q);
        if (h == 0) *(f32x2*)(p.ws + WS_ST0 + ((size_t)(tb * 16 + (s >> 1)) * 32 + n32) * 16 + (s & 1) * 8) = (f32x2){mean, q};
    }
}

#define GATE_O 6
#define GATE_F 18
#define GATE_Q 31
#define PACE 0
#define PACE_O 3
#define PACE_F 2
#define PACE_Q 1
#define RB_Q 8
#define RB_F1 6
#define RB_F2 12
#define POLL_SLEEP 1
constexpr unsigned SPIN_CAP = 1u << 15;
DEV void tmo_set(rsrc_t r, int lane) { if (lane == 0) __builtin_amdgcn_raw_buffer_store_b32(1u, r, 0, WS_TMO, SC1); }
DEV void wait_flags(rsrc_t r, unsigned off, unsigned mask, int lane) {
    bool need = lane < 32 && ((mask >> (lane & 31)) & 1u);
#pragma unroll 1
    for (unsigned spins = 0;; ++spins) {
        if (need) need = __builtin_amdgcn_raw_buffer_load_b32(r, lane * WSTR, off, SC1) == 0u;
        if (!__any(need)) break;
        if (spins > SPIN_CAP) { tmo_set(r, lane); break; }
        __builtin_amdgcn_s_sleep(POLL_SLEEP);
    }
    __builtin_amdgcn_fence(__ATOMIC_ACQUIRE, "wavefront");
}
DEV void publish(rsrc_t r, unsigned word_off, int lane) {
    WAITV(0);
    if (lane == 0) __builtin_amdgcn_raw_buffer_store_b32(1u, r, 0, word_off, SC1);
}
DEV unsigned lds_ld(LAS unsigned char* L, int off) { return *(volatile LAS unsigned*)(L + off); }
DEV unsigned lds_ldu(LAS unsigned char* L, int off) { return *(volatile LAS unsigned*)(L + opqv(off)); }
DEV void lds_add1(unsigned addr, int lane) {
    const unsigned a = lane == 0 ? addr : (unsigned)(LD_DUMMY + lane * 4);
    asm volatile("ds_add_u32 %0, %1" :: "v"(a), "v"(1u) : "memory");
}
DEV void lds_signal(unsigned addr, int lane) { lds_add1(addr, lane); }
DEV void lds_wait_ge(LAS unsigned char* L, int off, unsigned target, rsrc_t r, int lane) {
#pragma unroll 1
    for (unsigned spins = 0; rfl(lds_ldu(L, off)) < target; ++spins) {
        if (spins > (SPIN_CAP << 3)) { tmo_set(r, lane); break; }
        if (spins > 2) __builtin_amdgcn_s_sleep(1);
    }
    asm volatile("" ::: "memory");
}
DEV void lds_wait_vec(LAS unsigned char* L, int off, unsigned need, rsrc_t r, int lane) {
#pragma unroll 1
    for (unsigned spins = 0; !__all(lds_ld(L, off) >= need); ++spins) {
        if (spins > (SPIN_CAP << 3)) { tmo_set(r, lane); break; }
        if (spins > 2) __builtin_amdgcn_s_sleep(1);
    }
    asm volatile("" ::: "memory");
}

template <int C0> DEV int ring_ready(LAS unsigned char* L, int c, int layer, rsrc_t r, int lane) {
    const int ca = C0 + c + (lane & 7), sl = ca % NSLOT;
    const unsigned tgt = 4u * (layer * (sl == 4 ? 6 : 7) + ca / NSLOT + 1);
    unsigned long long ok;
#pragma unroll 1
    for (unsigned spins = 0;; ++spins) {
        ok = __ballot(lds_ld(L, C_FULL + sl * 4) >= tgt);
        if (ok & 1ull) break;
        if (spins > (SPIN_CAP << 3)) { tmo_set(r, lane); ok = ~0ull; break; }
        if (spins > 2) __builtin_amdgcn_s_sleep(1);
    }
    asm volatile("" ::: "memory");
    const int n = __builtin_ctzll(~ok);
    return c + (n < NSLOT ? n : NSLOT);
}
template <int C0, int NCH, int NT, int SWAPV, int RB, int AMODE>
DEV unsigned gemm_stage(int wid, unsigned char* lds, f32x16 (&acc)[NT], LAS unsigned char* L, unsigned lds0, int wsub, int layer, rsrc_t r, unsigned so, int lane) {
    unsigned twait = 0;
    constexpr int NS = 2 * NCH, JS = NT * 1024;
    bf16x8 b[RB];
    const unsigned l16 = lane * 16;
#define AOFF(s_) (AMODE == 0 ? (s_) * KB : (((s_) >> 1) * 8 + ((s_) & 1)) * KB)
#pragma unroll
    for (int s = 0; s < RB - 1; ++s) if (s < NS) b[s] = ldf(r, l16, so + AOFF(s));
    LAS const unsigned char* wl = L + LD_RING + wsub + l16;
    LAS const unsigned char* wl4 = L + opqv(LD_RING + 4 * SLOT + wsub + l16);
    const unsigned fadr = lane == 0 ? lds0 + C_FREE : (unsigned)(LD_DUMMY + lane * 4);
#define WADDR(s_) ((slot_of(C0 + ((s_) >> 1)) == 4 ? wl4 : wl + slot_of(C0 + ((s_) >> 1)) * SLOT) + ((s_) & 1) * JS)
    int ready = ring_ready<C0>(L, 0, layer, r, lane);
    bf16x8 fa[NT];
#pragma unroll
    for (int t = 0; t < NT; ++t) fa[t] = *(LAS const bf16x8*)(WADDR(0) + t * 1024);
    SB();
#pragma unroll
    for (int s = 0; s < NS; ++s) {
        if (s + RB - 1 < NS) b[(s + RB - 1) % RB] = ldf(r, l16, so + AOFF(s + RB - 1));
        const int sp = s + 1;
        if (sp < NS && (sp & 1) == 0) { if ((sp >> 1) >= ready) {
            ready = ring_ready<C0>(L, sp >> 1, layer, r, lane);
        } }
        SB();
#pragma unroll
        for (int t = 0; t < NT; ++t) {
            if (t >= SWAPV) acc[t] = mfma32(b[s % RB], fa[t], acc[t]); else acc[t] = mfma32(fa[t], b[s % RB], acc[t]);
            if (sp < NS) fa[t] = *(LAS const bf16x8*)(WADDR(sp) + t * 1024);
            SB();
        }
        if (s & 1) asm volatile("ds_add_u32 %0, %1 offset:%2" :: "v"(fadr), "v"(1u), "n"(slot_of(C0 + (s >> 1)) * 4) : "memory");
        if (s == 0) STAMPV(layer, C0 == 0 ? 28 : C0 == 8 ? 29 : C0 == 12 ? 30 : 31);
    }
#undef AOFF
#undef WADDR
    return twait;
}
DEV void stats_issue(rsrc_t r, unsigned st, int tb, int kh, int lane, f32x4 (&pr)[4]) {
    const unsigned so = opq(st + (tb * 16 + 8 * kh) * 512);
#pragma unroll
    for (int i = 0; i < 4; ++i) pr[i] = __builtin_bit_cast(f32x4, ld16(r, ((lane >> 5) * 4 * 32 + (lane & 31)) * 16 + i * 512, so));
}
DEV void stats_half(const f32x4 (&pr)[4], float& mean, float& m2) {
    float ms = 0.f;
#pragma unroll
    for (int i = 0; i < 4; ++i) ms += pr[i][0] + pr[i][2];
    mean = xsum32(ms) * (1.f / 16.f);
    float q = 0.f;
#pragma unroll
    for (int i = 0; i < 4; ++i) { const float d0 = pr[i][0] - mean, d1 = pr[i][2] - mean; q += pr[i][1] + pr[i][3] + 16.f * (d0 * d0 + d1 * d1); }
    m2 = xsum32(q);
}
DEV void stats_out(u32x4 o, rsrc_t r, unsigned st, int tb, int s, int lane) {
    const float q8[8] = {bflo(o.x), bfhi(o.x), bflo(o.y), bfhi(o.y), bflo(o.z), bfhi(o.z), bflo(o.w), bfhi(o.w)};
    const float mean = xsum32(((q8[0] + q8[1]) + (q8[2] + q8[3])) + ((q8[4] + q8[5]) + (q8[6] + q8[7]))) * (1.f / 16.f);
    float q = 0.f;
#pragma unroll
    for (int e = 0; e < 8; ++e) { const float d = q8[e] - mean; q += d * d; }
    q = xsum32(q);
    if (lane < 32) __builtin_amdgcn_raw_buffer_store_b64((u32x2){__float_as_uint(mean), __float_as_uint(q)}, r, lane * 16 + (s & 1) * 8, st + (tb * 16 + (s >> 1)) * 512, SC1);
}

DEV int xch_off(int w, int slot) { return slot < 2 ? LD_XA + w * 4096 + slot * 2048 : slot == 2 ? LD_XB + w * 2048 : (w < 4 ? LD_XC0 + w * 2048 : LD_XC1 + (w - 4) * 2048); }
DEV void xch_put(LAS unsigned char* L, int w, int slot, int lane, const f32x16& a, int half) {
    LAS f32x4* d = (LAS f32x4*)(L + xch_off(w, slot)) + lane;
    d[0] = (f32x4){a[8 * half], a[8 * half + 1], a[8 * half + 2], a[8 * half + 3]}; d[64] = (f32x4){a[8 * half + 4], a[8 * half + 5], a[8 * half + 6], a[8 * half + 7]};
}
DEV void xch_add(LAS unsigned char* L, int w, int slot, int lane, f32x16& a, int half) {
    LAS const f32x4* d = (LAS const f32x4*)(L + xch_off(w, slot)) + lane;
    const f32x4 u = d[0], v = d[64];
#pragma unroll
    for (int e = 0; e < 4; ++e) { a[8 * half + e] += u[e]; a[8 * half + 4 + e] += v[e]; }
}
DEV void ln_coef(LAS unsigned char* L, int wid, int lane, float mean_h, float m2_h, float& la, float& lb) {
    const f32x2 o = ((LAS const f32x2*)(L + LD_ST))[(wid ^ 1) * 32 + (lane & 31)];
    const float mean = 0.5f * (mean_h + o.x), d = mean_h - o.x;
    const float var = (m2_h + o.y + 128.f * d * d) * (1.f / 512.f);
    la = __builtin_amdgcn_rsqf(var + EPS); lb = -mean * la;
}
DEV void kq_reduce(f32x16 (&acc)[2], LAS unsigned char* L, unsigned lds0, int wid, int lane, unsigned seq, rsrc_t r, float (&v)[8]) {
    const int base = wid & 4;
    xch_put(L, wid, 0, lane, acc[0], 1); xch_put(L, wid, 1, lane, acc[1], 0); xch_put(L, wid, 2, lane, acc[1], 1);
    lds_signal(lds0 + C_XF + wid * 4, lane);
    lds_wait_vec(L, C_XF + (base + (lane & 3)) * 4, seq, r, lane);
#pragma unroll
    for (int x = 1; x < 4; ++x) xch_add(L, wid ^ x, x - 1, lane, acc[0], 0);
    lds_signal(lds0 + C_XRD + (wid >> 2) * 4, lane);
#pragma unroll
    for (int e = 0; e < 8; ++e) v[e] = acc[0][e];
}
DEV u32x4 pack8(const float (&f)[8]) { return (u32x4){pk2(f[0], f[1]), pk2(f[2], f[3]), pk2(f[4], f[5]), pk2(f[6], f[7])}; }
DEV void resid_add(const float (&v)[8], u32x4& res) {
    const float f[8] = {bflo(res.x) + v[0], bfhi(res.x) + v[1], bflo(res.y) + v[2], bfhi(res.y) + v[3], bflo(res.z) + v[4], bfhi(res.z) + v[5], bflo(res.w) + v[6], bfhi(res.w) + v[7]};
    res = pack8(f);
}

template <int KH>
DEV void qkv_epi(f32x16 (&acc)[3], float sx, float sq, LAS unsigned char* L, unsigned lds0, int wid, int lane, int layer, int kb, rsrc_t r, unsigned kvo) {
    const int t32 = lane & 31, h = lane >> 5;
    xch_put(L, wid, 0, lane, acc[0], 1 - KH); xch_put(L, wid, 1, lane, acc[1], 1 - KH); xch_put(L, wid, 2, lane, acc[2], 1 - KH);
    lds_signal(lds0 + C_XF + wid * 4, lane);
    LAS const float* csl = (LAS const float*)(L + LD_CSQ);
    f32x4 cq[2], ck[2];
#pragma unroll
    for (int u = 0; u < 2; ++u) { cq[u] = *(LAS const f32x4*)(csl + 16 * KH + 8 * u + 4 * h); ck[u] = *(LAS const f32x4*)(csl + 32 + 16 * KH + 8 * u + 4 * h); }
    const float cv = csl[64 + t32];
    lds_wait_ge(L, C_XF + (wid ^ 1) * 4, 4u * layer + 1, r, lane);
    float la, lb; ln_coef(L, wid, lane, sx, sq, la, lb);
    xch_add(L, wid ^ 1, 0, lane, acc[0], KH); xch_add(L, wid ^ 1, 1, lane, acc[1], KH); xch_add(L, wid ^ 1, 2, lane, acc[2], KH);
    lds_signal(lds0 + C_XRD + (wid >> 2) * 4, lane);
    LAS f32x2* ab = (LAS f32x2*)(L + LD_AB) + wid * 32;
    if (h == 0) ab[t32] = (f32x2){la, lb};
    f32x4 s01[2], s23[2];
#pragma unroll
    for (int u = 0; u < 2; ++u) { s01[u] = *(LAS const f32x4*)(ab + 16 * KH + 8 * u + 4 * h); s23[u] = *(LAS const f32x4*)(ab + 16 * KH + 8 * u + 4 * h + 2); }
    float v[8];
#pragma unroll
    for (int u = 0; u < 2; ++u)
#pragma unroll
        for (int e = 0; e < 4; ++e) v[4 * u + e] = la * acc[1][8 * KH + 4 * u + e] + lb * ck[u][e];
    const u32x4 ok = pack8(v);
    st16(r, lane * 16, kvo + KH * KB, ok);
    *(LAS u32x4*)(L + LD_K + (kb * 2 + KH) * 1024 + lane * 16) = ok;
#pragma unroll
    for (int u = 0; u < 2; ++u) {
        v[4 * u + 0] = s01[u][0] * acc[2][8 * KH + 4 * u + 0] + s01[u][1] * cv; v[4 * u + 1] = s01[u][2] * acc[2][8 * KH + 4 * u + 1] + s01[u][3] * cv;
        v[4 * u + 2] = s23[u][0] * acc[2][8 * KH + 4 * u + 2] + s23[u][1] * cv; v[4 * u + 3] = s23[u][2] * acc[2][8 * KH + 4 * u + 3] + s23[u][3] * cv; }
    const u32x4 ov = pack8(v);
    st16(r, lane * 16, kvo + (2 + KH) * KB, ov);
    *(LAS u32x4*)(L + LD_V + (kb * 2 + KH) * 1024 + lane * 16) = ov;
    const float qa = la * QSCALE, qb_ = lb * QSCALE;
#pragma unroll
    for (int u = 0; u < 2; ++u)
#pragma unroll
        for (int e = 0; e < 4; ++e) v[4 * u + e] = qa * acc[0][8 * KH + 4 * u + e] + qb_ * cq[u][e];
    *(LAS u32x4*)(L + LD_Q + ((kb - 8) * 2 + KH) * 1024 + lane * 16) = pack8(v);
}

#define EXP2(x) __builtin_amdgcn_exp2f(x)
DEV void mask_first(f32x16& s, int q32, int h) {
#pragma unroll
    for (int r = 0; r < 16; ++r) { const int key = (r & 3) + 8 * (r >> 2) + 4 * h; if (key <= q32) s[r] = -INFINITY; }
}
DEV void mask_last(f32x16& s, int q32, int h) {
#pragma unroll
    for (int r = 0; r < 16; ++r) { const int key = (r & 3) + 8 * (r >> 2) + 4 * h; if (key > q32) s[r] = -INFINITY; }
}
DEV float max16(const f32x16& s, float m) {
    float a = __builtin_fmaxf(__builtin_fmaxf(s[0], s[1]), m), b = __builtin_fmaxf(__builtin_fmaxf(s[2], s[3]), s[4]);
    a = __builtin_fmaxf(__builtin_fmaxf(a, s[5]), s[6]); b = __builtin_fmaxf(__builtin_fmaxf(b, s[7]), s[8]);
    a = __builtin_fmaxf(__builtin_fmaxf(a, s[9]), s[10]); b = __builtin_fmaxf(__builtin_fmaxf(b, s[11]), s[12]);
    a = __builtin_fmaxf(__builtin_fmaxf(a, s[13]), s[14]);
    return __builtin_fmaxf(__builtin_fmaxf(a, b), s[15]);
}
template <bool FULL9>
DEV void attn_unit(LAS const unsigned char* L, int team, int qb, int hd, int lane, f32x16& o) {
    const int q32 = lane & 31, h = lane >> 5;
    const bf16x8 qf = *(LAS const bf16x8*)(L + LD_Q + (qb * 2 + hd) * 1024 + lane * 16);
    const int first = FULL9 ? 0 : 8 - 4 * team - qb;
    LAS const unsigned char* kp = L + LD_K + (qb * 2 + hd) * 1024 + lane * 16;
    const bool onel = ((lane >> 4) & 1) != hd;
    const unsigned vstr = onel ? 0u : 2048u;
    LAS const unsigned char* vp = onel ? L + LD_ONES : L + LD_V + qb * 2048 + lane * 16;
    LAS const unsigned char* vq = onel ? L + LD_ONES : L + LD_V + qb * 2048 + 1024 + lane * 16;
    bf16x8 kf[9];
#pragma unroll
    for (int i = 0; i < 9; ++i) if (FULL9 || i >= first) kf[i] = *(LAS const bf16x8*)(kp + i * 2048);
    const f32x16 zero = {};
    float m = -INFINITY;
    if (FULL9) {
        f32x16 sa = mfma32(kf[0], qf, zero), sb;
#pragma unroll
        for (int i = 0; i < 9; ++i) {
            f32x16& cur = (i & 1) ? sb : sa; f32x16& nxt = (i & 1) ? sa : sb;
            if (i + 1 < 9) nxt = mfma32(kf[i + 1], qf, zero);
            SB();
            if (i == 0) mask_first(cur, q32, h);
            if (i == 8) mask_last(cur, q32, h);
            m = max16(cur, m);
            SB();
        }
    } else {
#pragma unroll
        for (int i = 0; i < 9; ++i) if (i >= first) {
            f32x16 s = mfma32(kf[i], qf, zero);
            if (i == 0) mask_first(s, q32, h);
            if (i == 8) mask_last(s, q32, h);
            m = max16(s, m);
        }
    }
    m = xmax32(m);
    f32x16 nm;
#pragma unroll
    for (int r = 0; r < 16; ++r) nm[r] = -m;
    if (FULL9) {
        f32x16 sa = mfma32(kf[0], qf, nm), sb;
        bf16x8 vn0 = *(LAS const bf16x8*)(vp), vn1 = *(LAS const bf16x8*)(vq);
#pragma unroll
        for (int i = 0; i < 9; ++i) {
            f32x16& cur = (i & 1) ? sb : sa; f32x16& nxt = (i & 1) ? sa : sb;
            const bf16x8 v0 = vn0, v1 = vn1;
            if (i + 1 < 9) { vn0 = *(LAS const bf16x8*)(vp + (i + 1) * vstr); vn1 = *(LAS const bf16x8*)(vq + (i + 1) * vstr); }
            if (i + 1 < 9) nxt = mfma32(kf[i + 1], qf, nm);
            SB();
            if (i == 0) mask_first(cur, q32, h);
            if (i == 8) mask_last(cur, q32, h);
            u32x4 p0, p1;
#pragma unroll
            for (int e = 0; e < 4; ++e) p0[e] = pk2(EXP2(cur[2 * e]), EXP2(cur[2 * e + 1]));
            SB();
            if (i == 0) o = mfma32(v0, __builtin_bit_cast(bf16x8, p0), zero); else o = mfma32(v0, __builtin_bit_cast(bf16x8, p0), o);
#pragma unroll
            for (int e = 0; e < 4; ++e) p1[e] = pk2(EXP2(cur[8 + 2 * e]), EXP2(cur[8 + 2 * e + 1]));
            SB();
            o = mfma32(v1, __builtin_bit_cast(bf16x8, p1), o);
            SB();
        }
    } else {
        float z = 0.f; asm volatile("" : "+v"(z));
#pragma unroll
        for (int r = 0; r < 16; ++r) o[r] = z;
#pragma unroll
        for (int i = 0; i < 9; ++i) if (i >= first) {
            f32x16 s = mfma32(kf[i], qf, nm);
            if (i == 0) mask_first(s, q32, h);
            if (i == 8) mask_last(s, q32, h);
            u32x4 p0, p1;
#pragma unroll
            for (int e = 0; e < 4; ++e) { p0[e] = pk2(EXP2(s[2 * e]), EXP2(s[2 * e + 1])); p1[e] = pk2(EXP2(s[8 + 2 * e]), EXP2(s[8 + 2 * e + 1])); }
            const bf16x8 v0 = *(LAS const bf16x8*)(vp + i * vstr), v1 = *(LAS const bf16x8*)(vq + i * vstr);
            o = mfma32(v0, __builtin_bit_cast(bf16x8, p0), o);
            o = mfma32(v1, __builtin_bit_cast(bf16x8, p1), o);
        }
    }
}

DEV unsigned chunk_src(int c, int mem, int mem1, int cb) {
    if (c < 8) return WS_WQKV + (mem * 96 + c * 12) * KB;
    if (c < 12) return WS_WO + (cb * 64 + (c - 8) * 16) * KB;
    if (c < 20) return WS_W1 + (mem1 * 128 + (c - 12) * 16) * KB;
    return WS_W2 + (cb * 224 + (c - 20) * 16) * KB;
}
template <int C, int PC = PACE> DEV void loader_fetch(u32x4 (&buf)[7][4], rsrc_t r, int lw, int mem, int mem1, int cb, unsigned l16) {
    constexpr int NI = C < 8 ? 3 : 4;
    const unsigned so = opq(chunk_src(C, mem, mem1, cb) + lw * NI * 1024);
#pragma unroll
    for (int i = 0; i < NI; ++i) { buf[C % 7][i] = ldw(r, l16 + i * 1024, so); if (PC) __builtin_amdgcn_s_sleep(PC); }
}
template <int C> DEV void loader_step(u32x4 (&buf)[7][4], LAS unsigned char* L, unsigned lds0, int lw, int mem, int mem1, int cb, int layer, rsrc_t r, int lane) {
    constexpr int NI = C < 8 ? 3 : 4, SL = C % NSLOT;
    const unsigned l16 = lane * 16;
    if (C < 34) {
        lds_wait_ge(L, C_FREE + SL * 4, 8u * (layer * uses_of(SL) + C / NSLOT), r, lane);
        LAS unsigned char* dst = L + LD_RING + SL * SLOT + lw * NI * 1024 + l16;
#pragma unroll
        for (int i = 0; i < NI; ++i) *(LAS u32x4*)(dst + i * 1024) = buf[C % 7][i];
        lds_signal(lds0 + C_FULL + SL * 4, lane);
    }
    constexpr int N = (C + 7) % 35;
    if (N != 34 && (C + 7 < 35 || layer == 0)) loader_fetch<N>(buf, r, lw, mem, mem1, cb, l16);
    SB();
}
template <int... Cs> struct seq {};
template <int N, int... Cs> struct mkseq : mkseq<N - 1, N - 1, Cs...> {};
template <int... Cs> struct mkseq<0, Cs...> { typedef seq<Cs...> type; };
template <int... Cs> DEV void loader_layer(seq<Cs...>, u32x4 (&buf)[7][4], LAS unsigned char* L, unsigned lds0, int lw, int mem, int mem1, int cb, int layer, rsrc_t r, int lane) {
    (loader_step<Cs>(buf, L, lds0, lw, mem, mem1, cb, layer, r, lane), ...);
}
template <int... Cs> DEV void loader_prime(seq<Cs...>, u32x4 (&buf)[7][4], rsrc_t r, int lw, int mem, int mem1, int cb, unsigned l16) { (loader_fetch<Cs>(buf, r, lw, mem, mem1, cb, l16), ...); }
DEV void loader(LAS unsigned char* L, unsigned lds0, int lw, int mem, rsrc_t r, int lane) {
    const int cb = mem >> 1, mem1 = mem < 14 ? mem : 13;
    u32x4 buf[7][4];
    loader_prime(mkseq<7>::type(), buf, r, lw, mem, mem1, cb, lane * 16);
    for (int layer = 0; layer < 2; ++layer) loader_layer(mkseq<35>::type(), buf, L, lds0, lw, mem, mem1, cb, layer, r, lane);
}

__global__ __launch_bounds__(NTHR, 3) void k_main(Params p) {
    extern __shared__ __attribute__((aligned(16))) unsigned char lds[];
    int team, mem; block_map(blockIdx.x, team, mem);
    const int tid = threadIdx.x, wid = rfl(tid >> 6); int lane = tid & 63;
    LAS unsigned char* L = (LAS unsigned char*)lds;
    const unsigned lds0 = (unsigned)(size_t)L;
    const rsrc_t r = __builtin_amdgcn_make_buffer_rsrc(p.ws, 0, WS_END, 0x00020000);
    STAMP_RT(0); STAMP_INIT();
    if (tid < 64) ((LAS unsigned*)(L + LD_CTR))[tid] = 0u;
    if (tid >= 64 && tid < 68) ((LAS unsigned*)(L + LD_ONES))[tid & 3] = 0x3f803f80u;
    __syncthreads();
    STAMPV(0, 0);
    if (wid >= NCW) { loader(L, lds0, wid - NCW, mem, r, (int)__builtin_amdgcn_mbcnt_hi(~0u, __builtin_amdgcn_mbcnt_lo(~0u, 0u))); STAMPV(0, 1); STAMP_DUMP(); return; }

    const int mem1 = mem < 14 ? mem : 13;
    const int cb = mem >> 1, kh = wid & 1, kq = wid & 3;
    const int tb1 = 4 * team + (wid >> 1);
    const int tb2 = 4 * team + 2 * (wid >> 2) + (mem & 1);
    {
        if (tid < 96) { const float* c = (const float*)(p.ws + WS_CSQ) + mem * 32 * 96 + tid; float s = 0.f;
#pragma unroll
            for (int i = 0; i < 32; ++i) s += c[i * 96];
            ((LAS float*)(L + LD_CSQ))[tid] = s; }
        else if (tid >= 128 && tid < 256) { const float* c = (const float*)(p.ws + WS_CS1) + mem1 * 32 * 128 + (tid - 128); float s = 0.f;
#pragma unroll
            for (int i = 0; i < 32; ++i) s += c[i * 128];
            ((LAS float*)(L + LD_CS1))[tid - 128] = s; }
        lds_signal(lds0 + C_ATT, lane);
    }

    for (int layer = 0; layer < 2; ++layer) {
        const unsigned wl = opq(WS_LAYER + layer * L_SIZE), fl = opq(WS_FLAGS + layer * FL_LAYER);
        if (layer) STAMPV(layer, 0);
        {
            lane = opqv(lane);
            const unsigned xf = layer == 0 ? WS_XF0 : WS_LAYER + L_X2F;
            if (layer > 0) wait_flags(r, WS_FLAGS + FL_X2 + tb1 * 32 * WSTR, 0xFFFFu << (16 * kh), lane);
            STAMPV(layer, 1);
            {
                const unsigned need = lane == 0 ? 8u * (layer + 1) : (lane == 1 + (wid >> 2) ? 4u * (4 * layer) : (lane < 3 && layer ? 4u * (4 * layer - 1) : 0u));
                lds_wait_vec(L, lane == 0 ? C_ATT : C_XRD + ((lane - 1) & 1) * 4, need, r, lane);
            }
            f32x4 pr[4]; stats_issue(r, layer == 0 ? WS_ST0 : WS_LAYER + L_ST2, tb1, kh, lane, pr);
            f32x16 acc[3];
#pragma unroll
            for (int t = 0; t < 3; ++t) acc[t] = (f32x16){};
            STAMPW(layer, 24, gemm_stage<0, 8, 3, 2, RB_Q, 0>(wid, lds, acc, L, lds0, kh * 6144, layer, r, opq(xf + (tb1 * 32 + 16 * kh) * KB), lane));
            STAMPV(layer, 2);
            lane = opqv(lane); const int t32 = lane & 31, h = lane >> 5;
            float sx, sq; stats_half(pr, sx, sq);
            if (h == 0) ((LAS f32x2*)(L + LD_ST))[wid * 32 + t32] = (f32x2){sx, sq};
            const int blk = 4 * team + (wid >> 1), kb = 8 + (wid >> 1);
            const unsigned kvo = wl + L_KV + (blk * 16 + mem) * 4 * KB;
            if (kh == 0) { asm volatile("; kh 0"); qkv_epi<0>(acc, sx, sq, L, lds0, wid, lane, layer, kb, r, kvo); asm volatile("; kh 0 end"); }
            else { asm volatile("; kh 1"); qkv_epi<1>(acc, sx, sq, L, lds0, wid, lane, layer, kb, r, kvo); asm volatile("; kh 1 end"); }
            lds_signal(lds0 + C_LOCAL + (wid >> 1) * 4, lane);
            STAMPV(layer, 3);
            publish(r, fl + FL_KV + ((blk * 16 + mem) * 2 + kh) * WSTR, lane);
            STAMPV(layer, 4);
        }
        {
            lane = opqv(lane);
            const int gb = 4 * team - 8 + wid;
            if (gb >= 0) {
                wait_flags(r, fl + FL_KV + (gb * 16 + mem) * 2 * WSTR, 3u, lane);
                STAMPV(layer, 5);
                u32x4 kv[4];
                const unsigned so = opq(wl + L_KV + (gb * 16 + mem) * 4 * KB);
#pragma unroll
                for (int i = 0; i < 4; ++i) kv[i] = ld16(r, lane * 16, so + i * KB);
                lds_wait_vec(L, C_XRD + (lane & 1) * 4, 4u * (4 * layer + 1), r, lane);
#pragma unroll
                for (int i = 0; i < 2; ++i) {
                    *(LAS u32x4*)(L + LD_K + (wid * 2 + i) * 1024 + lane * 16) = kv[i];
                    *(LAS u32x4*)(L + LD_V + (wid * 2 + i) * 1024 + lane * 16) = kv[2 + i];
                }
            }
            lds_signal(lds0 + C_HALO + wid * 4, lane);
            const int qb = wid >> 1, hd = wid & 1;
            STAMPV(layer, 6);
            {
                const bool isl = lane < 4, ish = lane >= 8 && lane < 16;
                const unsigned need = isl ? (lane <= qb ? 2u * (layer + 1) : 0u) : (ish && (lane - 8) >= qb ? (unsigned)(layer + 1) : 0u);
                lds_wait_vec(L, C_LOCAL + (lane & 15) * 4, need, r, lane);
            }
            STAMPV(layer, 7);
            f32x16 o;
            if (team >= 2) attn_unit<true>(L, team, qb, hd, lane, o); else attn_unit<false>(L, team, qb, hd, lane, o);
            STAMPV(layer, 8);
            lane = opqv(lane);
            lds_add1(lds0 + C_ATT, lane);
            const float rl = __builtin_amdgcn_rcpf(hd ? o[0] : o[8]);
            float v[8];
#pragma unroll
            for (int e = 0; e < 8; ++e) v[e] = (hd ? o[8 + e] : o[e]) * rl;
            st16(r, lane * 16, wl + L_OF + ((4 * team + qb) * 32 + 2 * mem + hd) * KB, pack8(v));
            publish(r, fl + FL_O + ((4 * team + qb) * 32 + 2 * mem + hd) * WSTR, lane);
            STAMPV(layer, 9);
        }
        {
            lane = opqv(lane);
            wait_flags(r, fl + FL_O + tb2 * 32 * WSTR, 0xFFu << (8 * kq), lane);
            STAMPV(layer, 10);
            u32x4 res = ld16(r, lane * 16, (layer == 0 ? WS_XF0 : WS_LAYER + L_X2F) + (tb2 * 32 + 4 * cb + kq) * KB);
            f32x16 acc[2];
            acc[0] = (f32x16){}; acc[1] = (f32x16){};
            STAMPW(layer, 25, gemm_stage<8, 4, 2, 99, 9, 0>(wid, lds, acc, L, lds0, kq * 4096, layer, r, opq(wl + L_OF + (tb2 * 32 + 8 * kq) * KB), lane));
            STAMPV(layer, 11);
            lane = opqv(lane);
            lds_wait_vec(L, (lane & 1) ? C_ATT : C_XRD + (wid >> 2) * 4, (lane & 1) ? 8u * (layer + 2) : 4u * (4 * layer + 1), r, lane);
            float v[8]; kq_reduce(acc, L, lds0, wid, lane, 4 * layer + 2, r, v);
            resid_add(v, res);
            st16(r, lane * 16, wl + L_X1F + (tb2 * 32 + 4 * cb + kq) * KB, res);
            stats_out(res, r, wl + L_ST1, tb2, 4 * cb + kq, lane);
            STAMPV(layer, 12);
            publish(r, fl + FL_X1 + (tb2 * 32 + 4 * cb + kq) * WSTR, lane);
            STAMPV(layer, 13);
        }
        {
            lane = opqv(lane);
            wait_flags(r, fl + FL_X1 + tb1 * 32 * WSTR, 0xFFFFu << (16 * kh), lane);
            STAMPV(layer, 14);
            lds_wait_ge(L, C_XRD + (wid >> 2) * 4, 4u * (4 * layer + 2), r, lane);
            f32x4 pr[4]; stats_issue(r, wl + L_ST1, tb1, kh, lane, pr);
            f32x16 acc[4];
#pragma unroll
            for (int t = 0; t < 4; ++t) acc[t] = (f32x16){};
            STAMPW(layer, 26, gemm_stage<12, 8, 4, 99, RB_F1, 0>(wid, lds, acc, L, lds0, kh * 8192, layer, r, opq(wl + L_X1F + (tb1 * 32 + 16 * kh) * KB), lane));
            STAMPV(layer, 15);
            lane = opqv(lane); const int t32 = lane & 31, h = lane >> 5;
            float sx, sq; stats_half(pr, sx, sq);
            if (h == 0) ((LAS f32x2*)(L + LD_ST))[wid * 32 + t32] = (f32x2){sx, sq};
            xch_put(L, wid, 0, lane, acc[2], 0); xch_put(L, wid, 1, lane, acc[2], 1); xch_put(L, wid, 2, lane, acc[3], 0); xch_put(L, wid, 3, lane, acc[3], 1);
            lds_signal(lds0 + C_XF + wid * 4, lane);
            LAS const float* csl = (LAS const float*)(L + LD_CS1) + 64 * kh;
            f32x4 cs[8];
#pragma unroll
            for (int i = 0; i < 8; ++i) cs[i] = *(LAS const f32x4*)(csl + 8 * i + 4 * h);
            lds_wait_ge(L, C_XF + (wid ^ 1) * 4, 4u * layer + 3, r, lane);
            float la, lb; ln_coef(L, wid, lane, sx, sq, la, lb);
            xch_add(L, wid ^ 1, 0, lane, acc[0], 0); xch_add(L, wid ^ 1, 1, lane, acc[0], 1); xch_add(L, wid ^ 1, 2, lane, acc[1], 0); xch_add(L, wid ^ 1, 3, lane, acc[1], 1);
            lds_signal(lds0 + C_XRD + (wid >> 2) * 4, lane);
            if (mem < 14) {
#pragma unroll
                for (int i = 0; i < 4; ++i) {
                    float hv[8], ex[8];
#pragma unroll
                    for (int e = 0; e < 8; ++e) { hv[e] = la * acc[i >> 1][8 * (i & 1) + e] + lb * cs[2 * i + (e >> 2)][e & 3]; ex[e] = -LOG2E * hv[e]; }
                    SB();
#pragma unroll
                    for (int e = 0; e < 8; ++e) ex[e] = __builtin_amdgcn_exp2f(ex[e]);
                    SB();
#pragma unroll
                    for (int e = 0; e < 8; ++e) ex[e] += 1.f;
                    SB();
#pragma unroll
                    for (int e = 0; e < 8; ++e) ex[e] = __builtin_amdgcn_rcpf(ex[e]);
                    SB();
#pragma unroll
                    for (int e = 0; e < 8; ++e) hv[e] *= ex[e];
                    st16(r, lane * 16, wl + L_HF + (tb1 * 112 + 8 * mem + 4 * kh + i) * KB, pack8(hv));
                    SB();
                }
                STAMPV(layer, 16);
                publish(r, fl + FL_H + (tb1 * 32 + 2 * mem + kh) * WSTR, lane);
            }
            STAMPV(layer, 17);
        }
        {
            lane = opqv(lane);
            wait_flags(r, fl + FL_H + tb2 * 32 * WSTR, (0x5555555u << (kq >> 1)) & 0xFFFFFFFu, lane);
            STAMPV(layer, 18);
            lds_wait_ge(L, C_XRD + (wid >> 2) * 4, 4u * (4 * layer + 3), r, lane);
            u32x4 res = ld16(r, lane * 16, wl + L_X1F + (tb2 * 32 + 4 * cb + kq) * KB);
            f32x16 acc[2];
            acc[0] = (f32x16){}; acc[1] = (f32x16){};
            STAMPW(layer, 27, gemm_stage<20, 14, 2, 99, RB_F2, 1>(wid, lds, acc, L, lds0, kq * 4096, layer, r, opq(wl + L_HF + (tb2 * 112 + 2 * kq) * KB), lane));
            STAMPV(layer, 19);
            lane = opqv(lane);
            float v[8]; kq_reduce(acc, L, lds0, wid, lane, 4 * layer + 4, r, v);
            resid_add(v, res);
            if (layer == 0) {
                st16(r, lane * 16, wl + L_X2F + (tb2 * 32 + 4 * cb + kq) * KB, res);
                stats_out(res, r, wl + L_ST2, tb2, 4 * cb + kq, lane);
                STAMPV(layer, 20);
                publish(r, fl + FL_X2 + (tb2 * 32 + 4 * cb + kq) * WSTR, lane);
            } else {
                auto s0 = __builtin_amdgcn_permlane32_swap(res.x, res.z, false, false), s1 = __builtin_amdgcn_permlane32_swap(res.y, res.w, false, false);
                unsigned short* dst = p.out + (size_t)(32 * tb2 + (lane & 31)) * D + 64 * cb + 16 * kq + 8 * (lane >> 5);
                *(u32x4*)dst = (u32x4){s0[0], s1[0], s0[1], s1[1]};
            }
            STAMPV(layer, 21);
        }
    }
    STAMP_RT(1); STAMP_DUMP();
}

extern "C" void kernel_launch(void* const* d_in, const int* in_sizes, int n_in, void* d_out, int out_size, void* d_ws, size_t ws_size, hipStream_t stream) {
    Params p;
    p.x = (const unsigned short*)d_in[0]; p.wqkv = (const unsigned short*)d_in[1]; p.wo = (const unsigned short*)d_in[2];
    p.w1 = (const unsigned short*)d_in[4]; p.w2 = (const unsigned short*)d_in[5];
    p.out = (unsigned short*)d_out; p.ws = (unsigned char*)d_ws;
    (void)hipFuncSetAttribute((const void*)k_main, hipFuncAttributeMaxDynamicSharedMemorySize, LDS_BYTES);
    k_prep<<<(5632 + 2048) / 4, 256, 0, stream>>>(p);
    k_main<<<256, NTHR, LDS_BYTES, stream>>>(p);
}
```

```cpp
#include <hip/hip_runtime.h>
#include <hip/hip_bf16.h>
#include <stdint.h>

typedef short bf16x8 __attribute__((ext_vector_type(8)));
typedef float f32x2 __attribute__((ext_vector_type(2)));
typedef float f32x4 __attribute__((ext_vector_type(4)));
typedef float f32x8 __attribute__((ext_vector_type(8)));
typedef float f32x16 __attribute__((ext_vector_type(16)));
typedef unsigned u32x2 __attribute__((ext_vector_type(2)));
typedef unsigned u32x4 __attribute__((ext_vector_type(4)));
typedef __bf16 bf16x2_t __attribute__((ext_vector_type(2)));
#define GAS __attribute__((address_space(1)))
#define LAS __attribute__((address_space(3)))
#define DEV __device__ __forceinline__
#define SB() __builtin_amdgcn_sched_barrier(0)
typedef __amdgpu_buffer_rsrc_t rsrc_t;

constexpr int S = 2048, D = 512, H = 32, F = 1792;
constexpr float EPS = 1e-5f;
constexpr float LOG2E = 1.4426950408889634f;
constexpr float QSCALE = 0.25f * LOG2E;

constexpr unsigned KB = 1024, MB = 1024 * 1024;
constexpr unsigned WS_TMO   = 0;
constexpr unsigned WS_EPOCH = 256;
constexpr unsigned WS_START = 4096;
constexpr unsigned WS_LAYER = 64 * KB;
constexpr unsigned L_KV  = 0;
constexpr unsigned L_OF  = L_KV + 4 * MB;
constexpr unsigned L_X1F = L_OF + 2 * MB;
constexpr unsigned L_HF  = L_X1F + 2 * MB;
constexpr unsigned L_X2F = L_HF + 7 * MB;
constexpr unsigned L_ST1 = L_X2F + 2 * MB;
constexpr unsigned L_ST2 = L_ST1 + 512 * KB;
constexpr unsigned L_SIZE = L_ST2 + 512 * KB;
constexpr unsigned WSTR = 128;
constexpr unsigned WS_FLAGS = 64 * MB;
constexpr unsigned FL_KV = 0, FL_O = FL_KV + 64 * 16 * 2 * WSTR, FL_X1 = FL_O + 64 * 32 * WSTR, FL_H = FL_X1 + 64 * 32 * WSTR, FL_X2 = FL_H + 64 * 32 * WSTR, FL_LAYER = FL_X2 + 64 * 32 * WSTR;
constexpr unsigned WS_END = 72 * MB;
static_assert(WS_LAYER + 2 * L_SIZE <= WS_FLAGS && WS_FLAGS + 2 * FL_LAYER <= WS_END, "workspace map");

constexpr int NCW = 8, NLW = 4, NTHR = 64 * (NCW + NLW);
constexpr int SLOT = 16384, NSLOT = 5;
constexpr int LD_RING = 0;
constexpr int LD_XA = 80 * 1024;
constexpr int LD_K = 96 * 1024;
constexpr int LD_V = 120 * 1024;
constexpr int LD_XB = LD_V;
constexpr int LD_XC0 = LD_K + 16 * 1024, LD_XC1 = LD_V + 16 * 1024;
constexpr int LD_Q = 144 * 1024;
constexpr int LD_MISC = 152 * 1024;
constexpr int LD_CSQ = LD_MISC  , LD_CS1 = LD_MISC + 768  , LD_ONES = LD_MISC + 1792, LD_CTR = LD_MISC + 1856, LD_DUMMY = LD_CTR + 256  ,
              LD_ST = LD_DUMMY + 512  , LD_AB = LD_ST + 2048  , LD_STAMP = LD_AB + 1024;
constexpr int C_FULL = LD_CTR, C_FREE = LD_CTR + 32, C_LOCAL = LD_CTR + 64, C_HALO = LD_CTR + 96, C_XF = LD_CTR + 128, C_XRD = LD_CTR + 160, C_ATT = LD_CTR + 176, C_BAR = LD_CTR + 180, C_CS = LD_CTR + 184;
constexpr int LDS_BYTES = 160 * 1024;
static_assert(LD_STAMP + 8 * 64 * 4 <= LDS_BYTES && LD_CTR % 16 == 0 && LD_ST % 16 == 0, "LDS map");
constexpr int slot_of(int c) { return c % NSLOT; }
constexpr int uses_of(int slot) { return slot == 4 ? 6 : 7; }

struct Params {
    const unsigned short* x; const unsigned short* wqkv; const unsigned short* wo; const unsigned short* w1; const unsigned short* w2;
    unsigned short* out; unsigned char* ws;
};

#define STAMPV(kid, i)
#define STAMPW(kid, i, ...) (void)(__VA_ARGS__)
#define STAMP_RT(i)
#define STAMP_DUMP()
#define STAMP_INIT()

DEV float bf2f(unsigned short b) { return __uint_as_float((unsigned)b << 16); }
DEV float bflo(unsigned u) { return __uint_as_float(u << 16); }
DEV float bfhi(unsigned u) { return __uint_as_float(u & 0xffff0000u); }
DEV unsigned pk2(float lo, float hi) { f32x2 v = {lo, hi}; bf16x2_t r = __builtin_convertvector(v, bf16x2_t); return __builtin_bit_cast(unsigned, r); }
DEV float xsum32(float v) { auto r = __builtin_amdgcn_permlane32_swap(__float_as_uint(v), __float_as_uint(v), false, false); return __uint_as_float(r[0]) + __uint_as_float(r[1]); }
DEV float xmax32(float v) { auto r = __builtin_amdgcn_permlane32_swap(__float_as_uint(v), __float_as_uint(v), false, false); return __builtin_fmaxf(__uint_as_float(r[0]), __uint_as_float(r[1])); }
DEV f32x16 mfma32(bf16x8 a, bf16x8 b, f32x16 c) { return __builtin_amdgcn_mfma_f32_32x32x16_bf16(a, b, c, 0, 0, 0); }
DEV float dot2(unsigned a, unsigned b, float c) { return __builtin_amdgcn_fdot2_f32_bf16(__builtin_bit_cast(bf16x2_t, a), __builtin_bit_cast(bf16x2_t, b), c, false); }
DEV unsigned opq(unsigned x) { asm volatile("" : "+s"(x)); return x; }
DEV int opqv(int x) { asm volatile("" : "+v"(x)); return x; }
DEV unsigned rfl(unsigned v) { return __builtin_amdgcn_readfirstlane(v); }
#define WAITV(n) asm volatile("s_waitcnt vmcnt(" #n ")" ::: "memory")
constexpr int SC1 = 16;
DEV bf16x8 ldf(rsrc_t r, unsigned vo, unsigned so) { return __builtin_bit_cast(bf16x8, __builtin_amdgcn_raw_buffer_load_b128(r, vo, so, SC1)); }
DEV u32x4 ld16(rsrc_t r, unsigned vo, unsigned so) { return __builtin_amdgcn_raw_buffer_load_b128(r, vo, so, SC1); }
DEV u32x4 ldw(rsrc_t r, unsigned vo, unsigned so) { return __builtin_amdgcn_raw_buffer_load_b128(r, vo, so, 0); }
DEV void st16(rsrc_t r, unsigned vo, unsigned so, u32x4 v) { __builtin_amdgcn_raw_buffer_store_b128(v, r, vo, so, SC1); asm volatile("s_nop 1" :: "v"(v) : "memory"); }

DEV void block_map(int b, int& team, int& mem) {
    const int x = b & 7, r = b >> 3;
    team = 4 * (x >> 1) + (r >> 3); mem = 8 * (x & 1) + (r & 7);
}

#define GATE_O 3
#define GATE_F 18
#define GATE_Q 31
#define PACE 0
#define PACE_O 3
#define PACE_F 2
#define PACE_Q 1
#define RB_Q 8
#define RB_F1 6
#define RB_F2 12
#define POLL_SLEEP 1
constexpr unsigned SPIN_CAP = 1u << 15;
DEV void tmo_set(rsrc_t r, int lane) { if (lane == 0) __builtin_amdgcn_raw_buffer_store_b32(1u, r, 0, WS_TMO, SC1); }
DEV void wait_flags(rsrc_t r, unsigned off, unsigned mask, int lane, unsigned ep) {
    bool need = lane < 32 && ((mask >> (lane & 31)) & 1u);
#pragma unroll 1
    for (unsigned spins = 0;; ++spins) {
        if (need) need = __builtin_amdgcn_raw_buffer_load_b32(r, lane * WSTR, off, SC1) != ep;
        if (!__any(need)) break;
        if (spins > SPIN_CAP) { tmo_set(r, lane); break; }
        __builtin_amdgcn_s_sleep(POLL_SLEEP);
    }
    __builtin_amdgcn_fence(__ATOMIC_ACQUIRE, "wavefront");
}
DEV void publish(rsrc_t r, unsigned word_off, int lane, unsigned ep) {
    WAITV(0);
    if (lane == 0) __builtin_amdgcn_raw_buffer_store_b32(ep, r, 0, word_off, SC1);
}
DEV unsigned lds_ld(LAS unsigned char* L, int off) { return *(volatile LAS unsigned*)(L + off); }
DEV int s2v(int x) { int v; asm volatile("v_mov_b32 %0, %1" : "=v"(v) : "s"(x)); return v; }
DEV unsigned lds_ldu(LAS unsigned char* L, int off) { return *(volatile LAS unsigned*)(L + s2v(off)); }
DEV void lds_add1(unsigned addr, int lane) {
    const unsigned a = lane == 0 ? addr : (unsigned)(LD_DUMMY + lane * 4);
    asm volatile("ds_add_u32 %0, %1" :: "v"(a), "v"(1u) : "memory");
}
DEV void lds_signal(unsigned addr, int lane) { lds_add1(addr, lane); }
DEV void lds_wait_ge(LAS unsigned char* L, int off, unsigned target, rsrc_t r, int lane) {
#pragma unroll 1
    for (unsigned spins = 0; rfl(lds_ldu(L, off)) < target; ++spins) {
        if (spins > (SPIN_CAP << 3)) break;
        if (spins > 2) __builtin_amdgcn_s_sleep(1);
    }
    asm volatile("" ::: "memory");
}
DEV void lds_wait_vec(LAS unsigned char* L, int off, unsigned need, rsrc_t r, int lane) {
#pragma unroll 1
    for (unsigned spins = 0; !__all(lds_ld(L, off) >= need); ++spins) {
        if (spins > (SPIN_CAP << 3)) break;
        if (spins > 2) __builtin_amdgcn_s_sleep(1);
    }
    asm volatile("" ::: "memory");
}

template <int C0> DEV int ring_ready(LAS unsigned char* L, int c, int layer, rsrc_t r, int lane) {
    const int ca = C0 + c + (lane & 7), sl = ca % NSLOT;
    const unsigned tgt = 4u * (layer * (sl == 4 ? 6 : 7) + ca / NSLOT + 1);
    unsigned long long ok;
#pragma unroll 1
    for (unsigned spins = 0;; ++spins) {
        ok = __ballot(lds_ld(L, C_FULL + sl * 4) >= tgt);
        if (ok & 1ull) break;
        if (spins > (SPIN_CAP << 3)) { ok = ~0ull; break; }
        if (spins > 2) __builtin_amdgcn_s_sleep(1);
    }
    asm volatile("" ::: "memory");
    const int n = __builtin_ctzll(~ok);
    return c + (n < NSLOT ? n : NSLOT);
}
DEV void stats_issue(rsrc_t r, unsigned st, int tb, int kh, int lane, f32x4 (&pr)[4]) {
    const unsigned so = opq(st + (tb * 16 + 8 * kh) * 512);
#pragma unroll
    for (int i = 0; i < 4; ++i) pr[i] = __builtin_bit_cast(f32x4, ld16(r, ((lane >> 5) * 4 * 32 + (lane & 31)) * 16 + i * 512, so));
}
DEV void stats_half(const f32x4 (&pr)[4], float& mean, float& m2) {
    float ms = 0.f;
#pragma unroll
    for (int i = 0; i < 4; ++i) ms += pr[i][0] + pr[i][2];
    mean = xsum32(ms) * (1.f / 16.f);
    float q = 0.f;
#pragma unroll
    for (int i = 0; i < 4; ++i) { const float d0 = pr[i][0] - mean, d1 = pr[i][2] - mean; q += pr[i][1] + pr[i][3] + 16.f * (d0 * d0 + d1 * d1); }
    m2 = xsum32(q);
}
DEV void stats_out(u32x4 o, rsrc_t r, unsigned st, int tb, int s, int lane) {
    const float q8[8] = {bflo(o.x), bfhi(o.x), bflo(o.y), bfhi(o.y), bflo(o.z), bfhi(o.z), bflo(o.w), bfhi(o.w)};
    const float mean = xsum32(((q8[0] + q8[1]) + (q8[2] + q8[3])) + ((q8[4] + q8[5]) + (q8[6] + q8[7]))) * (1.f / 16.f);
    float q = 0.f;
#pragma unroll
    for (int e = 0; e < 8; ++e) { const float d = q8[e] - mean; q += d * d; }
    q = xsum32(q);
    if (lane < 32) __builtin_amdgcn_raw_buffer_store_b64((u32x2){__float_as_uint(mean), __float_as_uint(q)}, r, lane * 16 + (s & 1) * 8, st + (tb * 16 + (s >> 1)) * 512, SC1);
}

typedef short v4i16 __attribute__((ext_vector_type(4)));
DEV v4i16 trd(LAS const unsigned char* p) { return __builtin_amdgcn_ds_read_tr16_b64_v4i16((LAS v4i16*)p); }
template <int C0, int NCH, int NT, int SWAPV, int P, int RB, int AMODE, bool PRE, bool CS, bool ST>
DEV unsigned gemm_stage(int wid, unsigned char* lds, f32x16 (&acc)[NT], bf16x8 (&b)[RB], const int (&wa)[NT], int tau, f32x16& acs, int cst, const f32x4 (&pr)[4], float& sx, float& sq, LAS unsigned char* L, unsigned lds0, int layer, rsrc_t r, unsigned so, int lane) {
    unsigned twait = 0;
    constexpr int NS = 2 * NCH, JS = 16 * P;
    const unsigned l16 = lane * 16;
#define AOFF(s_) (AMODE == 0 ? (s_) * KB : (((s_) >> 1) * 8 + ((s_) & 1)) * KB)
    if (!PRE) {
#pragma unroll
        for (int s = 0; s < RB - 1; ++s) if (s < NS) b[s] = ldf(r, l16, so + AOFF(s));
    }
    LAS const unsigned char* w0[NT]; LAS const unsigned char* w1[NT];
#pragma unroll
    for (int t = 0; t < NT; ++t) { w0[t] = L + LD_RING + wa[t]; w1[t] = L + opqv(LD_RING + 4 * SLOT + wa[t]); }
    const unsigned fadr = lane == 0 ? lds0 + C_FREE : (unsigned)(LD_DUMMY + lane * 4);
#define WADDR(s_, t_) ((slot_of(C0 + ((s_) >> 1)) == 4 ? w1[t_] : w0[t_] + slot_of(C0 + ((s_) >> 1)) * SLOT) + ((s_) & 1) * JS)
#define WFRAG(s_, t_) __builtin_shufflevector(trd(WADDR(s_, t_)), trd(WADDR(s_, t_) + tau), 0, 1, 2, 3, 4, 5, 6, 7)
    const unsigned one2 = (unsigned)s2v(0x3f803f80);
    const bf16x8 ones = __builtin_bit_cast(bf16x8, (u32x4){one2, one2, one2, one2});
    int ready = ring_ready<C0>(L, 0, layer, r, lane);
    bf16x8 fa[NT];
#pragma unroll
    for (int t = 0; t < NT; ++t) fa[t] = WFRAG(0, t);
    SB();
#pragma unroll
    for (int s = 0; s < NS; ++s) {
        if (!PRE && s + RB - 1 < NS) b[(s + RB - 1) % RB] = ldf(r, l16, so + AOFF(s + RB - 1));
        const int sp = s + 1;
        if (sp < NS && (sp & 1) == 0) { if ((sp >> 1) >= ready) {
            ready = ring_ready<C0>(L, sp >> 1, layer, r, lane);
        } }
        if (CS) {
            u32x4 f = __builtin_bit_cast(u32x4, fa[0]);
#pragma unroll
            for (int t = 1; t < NT; ++t) { const u32x4 g = __builtin_bit_cast(u32x4, fa[t]);
#pragma unroll
                for (int e = 0; e < 4; ++e) f[e] = cst == t ? g[e] : f[e]; }
            if (s == 0) acs = mfma32(__builtin_bit_cast(bf16x8, f), ones, (f32x16){}); else acs = mfma32(__builtin_bit_cast(bf16x8, f), ones, acs);
        }
        SB();
#pragma unroll
        for (int t = 0; t < NT; ++t) {
            if (t >= SWAPV) acc[t] = mfma32(b[s % RB], fa[t], acc[t]); else acc[t] = mfma32(fa[t], b[s % RB], acc[t]);
            if (sp < NS) fa[t] = WFRAG(sp, t);
            SB();
        }
        if (s & 1) asm volatile("ds_add_u32 %0, %1 offset:%2" :: "v"(fadr), "v"(1u), "n"(slot_of(C0 + (s >> 1)) * 4) : "memory");
        if (ST && s == 3) { stats_half(pr, sx, sq); asm volatile("" : "+v"(sx), "+v"(sq)); }
        if (s == 0) STAMPV(layer, C0 == 0 ? 28 : C0 == 8 ? 29 : C0 == 12 ? 30 : 31);
    }
#undef AOFF
#undef WADDR
#undef WFRAG
    return twait;
}
DEV int xch_off(int w, int slot) { return slot < 2 ? LD_XA + w * 4096 + slot * 2048 : slot == 2 ? LD_XB + w * 2048 : (w < 4 ? LD_XC0 + w * 2048 : LD_XC1 + (w - 4) * 2048); }
DEV void xch_put(LAS unsigned char* L, int w, int slot, int lane, const f32x16& a, int half) {
    LAS f32x4* d = (LAS f32x4*)(L + xch_off(w, slot)) + lane;
    d[0] = (f32x4){a[8 * half], a[8 * half + 1], a[8 * half + 2], a[8 * half + 3]}; d[64] = (f32x4){a[8 * half + 4], a[8 * half + 5], a[8 * half + 6], a[8 * half + 7]};
}
DEV void xch_add(LAS unsigned char* L, int w, int slot, int lane, f32x16& a, int half) {
    LAS const f32x4* d = (LAS const f32x4*)(L + xch_off(w, slot)) + lane;
    const f32x4 u = d[0], v = d[64];
#pragma unroll
    for (int e = 0; e < 4; ++e) { a[8 * half + e] += u[e]; a[8 * half + 4 + e] += v[e]; }
}
DEV void ln_coef(f32x2 o, float mean_h, float m2_h, float& la, float& lb) {
    const float mean = 0.5f * (mean_h + o.x), d = mean_h - o.x;
    const float var = (m2_h + o.y + 128.f * d * d) * (1.f / 512.f);
    la = __builtin_amdgcn_rsqf(var + EPS); lb = -mean * la;
}
DEV void kq_reduce(f32x16 (&acc)[2], LAS unsigned char* L, unsigned lds0, int wid, int lane, unsigned seq, rsrc_t r, float (&v)[8]) {
    const int base = wid & 4;
    xch_put(L, wid, 0, lane, acc[0], 1); xch_put(L, wid, 1, lane, acc[1], 0); xch_put(L, wid, 2, lane, acc[1], 1);
    lds_signal(lds0 + C_XF + wid * 4, lane);
    lds_wait_vec(L, C_XF + (base + (lane & 3)) * 4, seq, r, lane);
#pragma unroll
    for (int x = 1; x < 4; ++x) xch_add(L, wid ^ x, x - 1, lane, acc[0], 0);
    lds_signal(lds0 + C_XRD + (wid >> 2) * 4, lane);
#pragma unroll
    for (int e = 0; e < 8; ++e) v[e] = acc[0][e];
}
DEV u32x4 pack8(const float (&f)[8]) { return (u32x4){pk2(f[0], f[1]), pk2(f[2], f[3]), pk2(f[4], f[5]), pk2(f[6], f[7])}; }
DEV void resid_add(const float (&v)[8], u32x4& res) {
    const float f[8] = {bflo(res.x) + v[0], bfhi(res.x) + v[1], bflo(res.y) + v[2], bfhi(res.y) + v[3], bflo(res.z) + v[4], bfhi(res.z) + v[5], bflo(res.w) + v[6], bfhi(res.w) + v[7]};
    res = pack8(f);
}

template <int KH>
DEV void qkv_epi(f32x16 (&acc)[3], float sx, float sq, LAS unsigned char* L, unsigned lds0, int wid, int lane, int layer, int kb, rsrc_t r, unsigned kvo) {
    const int t32 = lane & 31, h = lane >> 5;
    xch_put(L, wid, 0, lane, acc[0], 1 - KH); xch_put(L, wid, 1, lane, acc[1], 1 - KH); xch_put(L, wid, 2, lane, acc[2], 1 - KH);
    lds_signal(lds0 + C_XF + wid * 4, lane);
    if (layer == 0) lds_wait_ge(L, C_CS, 6u, r, lane);
    LAS const float* csl = (LAS const float*)(L + LD_CSQ);
    f32x4 cq[2], ck[2];
#pragma unroll
    for (int u = 0; u < 2; ++u) { cq[u] = *(LAS const f32x4*)(csl + 16 * KH + 8 * u + 4 * h) + *(LAS const f32x4*)(csl + 96 + 16 * KH + 8 * u + 4 * h);
        ck[u] = *(LAS const f32x4*)(csl + 32 + 16 * KH + 8 * u + 4 * h) + *(LAS const f32x4*)(csl + 96 + 32 + 16 * KH + 8 * u + 4 * h); }
    const float cv = csl[64 + t32] + csl[96 + 64 + t32];
    lds_wait_ge(L, C_XF + (wid ^ 1) * 4, 4u * layer + 1, r, lane);
    const f32x2 os = ((LAS const f32x2*)(L + LD_ST))[(wid ^ 1) * 32 + t32];
    xch_add(L, wid ^ 1, 0, lane, acc[0], KH); xch_add(L, wid ^ 1, 1, lane, acc[1], KH); xch_add(L, wid ^ 1, 2, lane, acc[2], KH);
    lds_signal(lds0 + C_XRD + (wid >> 2) * 4, lane);
    float la, lb; ln_coef(os, sx, sq, la, lb);
    LAS f32x2* ab = (LAS f32x2*)(L + LD_AB) + (wid >> 1) * 32;
    if (h == 0) ab[t32] = (f32x2){la, lb};
    f32x4 s01[2], s23[2];
#pragma unroll
    for (int u = 0; u < 2; ++u) { s01[u] = *(LAS const f32x4*)(ab + 16 * KH + 8 * u + 4 * h); s23[u] = *(LAS const f32x4*)(ab + 16 * KH + 8 * u + 4 * h + 2); }
    float v[8];
#pragma unroll
    for (int u = 0; u < 2; ++u)
#pragma unroll
        for (int e = 0; e < 4; ++e) v[4 * u + e] = la * acc[1][8 * KH + 4 * u + e] + lb * ck[u][e];
    const u32x4 ok = pack8(v);
    st16(r, lane * 16, kvo + KH * KB, ok);
    *(LAS u32x4*)(L + LD_K + (kb * 2 + KH) * 1024 + lane * 16) = ok;
#pragma unroll
    for (int u = 0; u < 2; ++u) {
        v[4 * u + 0] = s01[u][0] * acc[2][8 * KH + 4 * u + 0] + s01[u][1] * cv; v[4 * u + 1] = s01[u][2] * acc[2][8 * KH + 4 * u + 1] + s01[u][3] * cv;
        v[4 * u + 2] = s23[u][0] * acc[2][8 * KH + 4 * u + 2] + s23[u][1] * cv; v[4 * u + 3] = s23[u][2] * acc[2][8 * KH + 4 * u + 3] + s23[u][3] * cv; }
    const u32x4 ov = pack8(v);
    st16(r, lane * 16, kvo + (2 + KH) * KB, ov);
    *(LAS u32x4*)(L + LD_V + (kb * 2 + KH) * 1024 + lane * 16) = ov;
    const float qa = la * QSCALE, qb_ = lb * QSCALE;
#pragma unroll
    for (int u = 0; u < 2; ++u)
#pragma unroll
        for (int e = 0; e < 4; ++e) v[4 * u + e] = qa * acc[0][8 * KH + 4 * u + e] + qb_ * cq[u][e];
    *(LAS u32x4*)(L + LD_Q + ((kb - 8) * 2 + KH) * 1024 + lane * 16) = pack8(v);
}
DEV void cs_store(const f32x16& a, LAS float* dst, int lane) {
    if ((lane & 31) == 0) {
#pragma unroll
        for (int u = 0; u < 4; ++u) *(LAS f32x4*)(dst + 8 * u + 4 * (lane >> 5)) = (f32x4){a[4 * u], a[4 * u + 1], a[4 * u + 2], a[4 * u + 3]};
    }
}

#define EXP2(x) __builtin_amdgcn_exp2f(x)
DEV void mask_first(f32x16& s, int q32, int h) {
#pragma unroll
    for (int r = 0; r < 16; ++r) { const int key = (r & 3) + 8 * (r >> 2) + 4 * h; if (key <= q32) s[r] = -INFINITY; }
}
DEV void mask_last(f32x16& s, int q32, int h) {
#pragma unroll
    for (int r = 0; r < 16; ++r) { const int key = (r & 3) + 8 * (r >> 2) + 4 * h; if (key > q32) s[r] = -INFINITY; }
}
DEV float max16(const f32x16& s, float m) {
    float a = __builtin_fmaxf(__builtin_fmaxf(s[0], s[1]), m), b = __builtin_fmaxf(__builtin_fmaxf(s[2], s[3]), s[4]);
    a = __builtin_fmaxf(__builtin_fmaxf(a, s[5]), s[6]); b = __builtin_fmaxf(__builtin_fmaxf(b, s[7]), s[8]);
    a = __builtin_fmaxf(__builtin_fmaxf(a, s[9]), s[10]); b = __builtin_fmaxf(__builtin_fmaxf(b, s[11]), s[12]);
    a = __builtin_fmaxf(__builtin_fmaxf(a, s[13]), s[14]);
    return __builtin_fmaxf(__builtin_fmaxf(a, b), s[15]);
}
template <bool FULL9>
DEV void attn_unit(LAS const unsigned char* L, int team, int qb, int hd, int lane, f32x16& o) {
    const int q32 = lane & 31, h = lane >> 5;
    const bf16x8 qf = *(LAS const bf16x8*)(L + LD_Q + (qb * 2 + hd) * 1024 + lane * 16);
    const int first = FULL9 ? 0 : 8 - 4 * team - qb;
    LAS const unsigned char* kp = L + LD_K + (qb * 2 + hd) * 1024 + lane * 16;
    const bool onel = ((lane >> 4) & 1) != hd;
    const unsigned vstr = onel ? 0u : 2048u;
    LAS const unsigned char* vp = onel ? L + LD_ONES : L + LD_V + qb * 2048 + lane * 16;
    LAS const unsigned char* vq = onel ? L + LD_ONES : L + LD_V + qb * 2048 + 1024 + lane * 16;
    bf16x8 kf[9];
#pragma unroll
    for (int i = 0; i < 9; ++i) if (FULL9 || i >= first) kf[i] = *(LAS const bf16x8*)(kp + i * 2048);
    const f32x16 zero = {};
    float m = -INFINITY;
    if (FULL9) {
        f32x16 sa = mfma32(kf[0], qf, zero), sb;
#pragma unroll
        for (int i = 0; i < 9; ++i) {
            f32x16& cur = (i & 1) ? sb : sa; f32x16& nxt = (i & 1) ? sa : sb;
            if (i + 1 < 9) nxt = mfma32(kf[i + 1], qf, zero);
            SB();
            if (i == 0) mask_first(cur, q32, h);
            if (i == 8) mask_last(cur, q32, h);
            m = max16(cur, m);
            SB();
        }
    } else {
#pragma unroll
        for (int i = 0; i < 9; ++i) if (i >= first) {
            f32x16 s = mfma32(kf[i], qf, zero);
            if (i == 0) mask_first(s, q32, h);
            if (i == 8) mask_last(s, q32, h);
            m = max16(s, m);
        }
    }
    m = xmax32(m);
    f32x16 nm;
#pragma unroll
    for (int r = 0; r < 16; ++r) nm[r] = -m;
    if (FULL9) {
        f32x16 sa = mfma32(kf[0], qf, nm), sb;
        bf16x8 vn0 = *(LAS const bf16x8*)(vp), vn1 = *(LAS const bf16x8*)(vq);
#pragma unroll
        for (int i = 0; i < 9; ++i) {
            f32x16& cur = (i & 1) ? sb : sa; f32x16& nxt = (i & 1) ? sa : sb;
            const bf16x8 v0 = vn0, v1 = vn1;
            if (i + 1 < 9) { vn0 = *(LAS const bf16x8*)(vp + (i + 1) * vstr); vn1 = *(LAS const bf16x8*)(vq + (i + 1) * vstr); }
            if (i + 1 < 9) nxt = mfma32(kf[i + 1], qf, nm);
            SB();
            if (i == 0) mask_first(cur, q32, h);
            if (i == 8) mask_last(cur, q32, h);
            u32x4 p0, p1;
#pragma unroll
            for (int e = 0; e < 4; ++e) p0[e] = pk2(EXP2(cur[2 * e]), EXP2(cur[2 * e + 1]));
            SB();
            if (i == 0) o = mfma32(v0, __builtin_bit_cast(bf16x8, p0), zero); else o = mfma32(v0, __builtin_bit_cast(bf16x8, p0), o);
#pragma unroll
            for (int e = 0; e < 4; ++e) p1[e] = pk2(EXP2(cur[8 + 2 * e]), EXP2(cur[8 + 2 * e + 1]));
            SB();
            o = mfma32(v1, __builtin_bit_cast(bf16x8, p1), o);
            SB();
        }
    } else {
        float z = 0.f; asm volatile("" : "+v"(z));
#pragma unroll
        for (int r = 0; r < 16; ++r) o[r] = z;
#pragma unroll
        for (int i = 0; i < 9; ++i) if (i >= first) {
            f32x16 s = mfma32(kf[i], qf, nm);
            if (i == 0) mask_first(s, q32, h);
            if (i == 8) mask_last(s, q32, h);
            u32x4 p0, p1;
#pragma unroll
            for (int e = 0; e < 4; ++e) { p0[e] = pk2(EXP2(s[2 * e]), EXP2(s[2 * e + 1])); p1[e] = pk2(EXP2(s[8 + 2 * e]), EXP2(s[8 + 2 * e + 1])); }
            const bf16x8 v0 = *(LAS const bf16x8*)(vp + i * vstr), v1 = *(LAS const bf16x8*)(vq + i * vstr);
            o = mfma32(v0, __builtin_bit_cast(bf16x8, p0), o);
            o = mfma32(v1, __builtin_bit_cast(bf16x8, p1), o);
        }
    }
}

struct LoaderCtx { rsrc_t rq, ro, r1, r2; unsigned sq, so_, s1; int lw; };
template <int C, int PC = PACE> DEV void loader_fetch(u32x4 (&buf)[7][4], const LoaderCtx& x, int lane_) {
    constexpr int NI = C < 8 ? 3 : 4;
    const int lane = opqv(lane_), lw = x.lw;
#pragma unroll
    for (int j = 0; j < NI; ++j) {
        if (C < 8) {
            const int i = lw * 192 + j * 64 + lane, row = (i * 683) >> 13, c = i - 12 * row;
            buf[C % 7][j] = ldw(x.rq, ((256 * (row >> 5) + (row & 31)) * 1536 + (c >> 2) * 512 + 8 * (c & 3)) * 2, opq(x.sq + C * 32 * 1536 * 2));
        } else if (C < 12 || C >= 20) {
            const int r8 = lw * 32 + j * 8 + (lane >> 3), c8 = (lane & 7) ^ (((r8 >> 1) & 1) << 2);
            if (C < 12) buf[C % 7][j] = ldw(x.ro, ((128 * (r8 >> 5) + (r8 & 31)) * 512 + 8 * c8) * 2, opq(x.so_ + (C - 8) * 32 * 512 * 2));
            else buf[C % 7][j] = ldw(x.r2, (r8 * 512 + 8 * c8) * 2, opq(x.so_ + (C - 20) * 128 * 512 * 2));
        } else {
            const int r16 = lw * 16 + j * 4 + (lane >> 4), c16 = lane & 15;
            buf[C % 7][j] = ldw(x.r1, ((256 * (r16 >> 5) + (r16 & 31)) * 1792 + (((c16 >> 2) ^ (r16 & 3)) * 32) + 8 * (c16 & 3)) * 2, opq(x.s1 + (C - 12) * 32 * 1792 * 2));
        }
        if (PC) __builtin_amdgcn_s_sleep(PC);
    }
}
template <int C> DEV void loader_step(u32x4 (&buf)[7][4], const LoaderCtx& x, LAS unsigned char* L, unsigned lds0, int lw, int layer, rsrc_t r, int lane) {
    constexpr int NI = C < 8 ? 3 : 4, SL = C % NSLOT;
    const unsigned l16 = lane * 16;
    if (C < 34) {
        lds_wait_ge(L, C_FREE + SL * 4, 8u * (layer * uses_of(SL) + C / NSLOT), r, lane);
        LAS unsigned char* dst = L + LD_RING + SL * SLOT + lw * NI * 1024 + l16;
#pragma unroll
        for (int i = 0; i < NI; ++i) *(LAS u32x4*)(dst + i * 1024) = buf[C % 7][i];
        lds_signal(lds0 + C_FULL + SL * 4, lane);
    }
    constexpr int N = (C + 7) % 35;
    constexpr bool DEFER = N >= 8 && N <= 14;
    if (!DEFER && N != 34 && (C + 7 < 35 || layer == 0)) loader_fetch<N>(buf, x, lane);
    if (C == 7) {
        lds_wait_ge(L, C_FREE + (GATE_O % NSLOT) * 4, 8u * (layer * uses_of(GATE_O % NSLOT) + GATE_O / NSLOT + 1), r, lane);
        loader_fetch<8>(buf, x, lane); loader_fetch<9>(buf, x, lane); loader_fetch<10>(buf, x, lane); loader_fetch<11>(buf, x, lane); loader_fetch<12>(buf, x, lane); loader_fetch<13>(buf, x, lane); loader_fetch<14>(buf, x, lane);
    }
    SB();
}
template <int... Cs> struct seq {};
template <int N, int... Cs> struct mkseq : mkseq<N - 1, N - 1, Cs...> {};
template <int... Cs> struct mkseq<0, Cs...> { typedef seq<Cs...> type; };
template <int... Cs> DEV void loader_layer(seq<Cs...>, u32x4 (&buf)[7][4], const LoaderCtx& x, LAS unsigned char* L, unsigned lds0, int lw, int layer, rsrc_t r, int lane) {
    (loader_step<Cs>(buf, x, L, lds0, lw, layer, r, lane), ...);
}
template <int... Cs> DEV void loader_prime(seq<Cs...>, u32x4 (&buf)[7][4], const LoaderCtx& x, int lane) { (loader_fetch<Cs>(buf, x, lane), ...); }
DEV unsigned loader(const Params& p, LAS unsigned char* L, unsigned lds0, int lw, int mem, rsrc_t r, int lane, bool first) {
    const int cb = mem >> 1, mem1 = mem < 14 ? mem : 13;
    LoaderCtx x;
    x.rq = __builtin_amdgcn_make_buffer_rsrc((void*)p.wqkv, 0, D * 3 * D * 2, 0x00020000); x.ro = __builtin_amdgcn_make_buffer_rsrc((void*)p.wo, 0, D * D * 2, 0x00020000);
    x.r1 = __builtin_amdgcn_make_buffer_rsrc((void*)p.w1, 0, D * F * 2, 0x00020000); x.r2 = __builtin_amdgcn_make_buffer_rsrc((void*)p.w2, 0, F * D * 2, 0x00020000);
    x.sq = 32 * mem * 2; x.so_ = 64 * cb * 2; x.s1 = 128 * mem1 * 2; x.lw = lw;
    u32x4 buf[7][4];
    loader_prime(mkseq<7>::type(), buf, x, lane);
    unsigned ep = 0;
    if (first) { ep = rfl(__builtin_amdgcn_raw_buffer_load_b32(r, 0, WS_EPOCH, SC1)) + 1u; lds_wait_ge(L, C_ATT, 8u, r, lane); if (lane == 0) __builtin_amdgcn_raw_buffer_store_b32(ep, r, 0, WS_START + blockIdx.x * 4, SC1); }
    for (int layer = 0; layer < 2; ++layer) loader_layer(mkseq<35>::type(), buf, x, L, lds0, lw, layer, r, lane);
    return ep;
}

DEV void n64_addr(int (&wa)[2], int kq, int lane) {
    const int q4 = (lane & 15) >> 2;
#pragma unroll
    for (int t = 0; t < 2; ++t) { const int g = (2 * t + ((lane >> 4) & 1)) ^ kq;
        wa[t] = kq * 4096 + (4 * (lane >> 5) + q4) * 128 + (((g >> 1) ^ ((q4 >> 1) & 1)) * 64) + (g & 1) * 32 + (lane & 3) * 8; }
}
#define STAGE_BAR(n) do { lds_signal(lds0 + C_BAR, lane); lds_wait_ge(L, C_BAR, 8u * (5 * layer + (n)), r, lane); } while (0)
__global__ __launch_bounds__(NTHR, 3) void k_main(Params p) {
    extern __shared__ __attribute__((aligned(16))) unsigned char lds[];
    int team, mem; block_map(blockIdx.x, team, mem);
    const int tid = threadIdx.x, wid = rfl(tid >> 6); int lane = tid & 63;
    LAS unsigned char* L = (LAS unsigned char*)lds;
    const unsigned lds0 = (unsigned)(size_t)L;
    const rsrc_t r = __builtin_amdgcn_make_buffer_rsrc(p.ws, 0, WS_END, 0x00020000);
    STAMP_RT(0); STAMP_INIT();
    if (tid < 64) ((LAS unsigned*)(L + LD_CTR))[tid] = 0u;
    if (tid >= 64 && tid < 68) ((LAS unsigned*)(L + LD_ONES))[tid & 3] = 0x3f803f80u;
    __syncthreads();
    STAMPV(0, 0);
    if (wid >= NCW) {
        const int ln = (int)__builtin_amdgcn_mbcnt_hi(~0u, __builtin_amdgcn_mbcnt_lo(~0u, 0u));
        const unsigned ep = loader(p, L, lds0, wid - NCW, mem, r, ln, wid == NCW);
        if (blockIdx.x == 0 && wid == NCW) {
#pragma unroll 1
            for (unsigned spins = 0; spins < SPIN_CAP; ++spins) {
                const u32x4 v = ld16(r, ln * 16, WS_START);
                if (__all(v.x == ep && v.y == ep && v.z == ep && v.w == ep)) { if (ln == 0) __builtin_amdgcn_raw_buffer_store_b32(ep, r, 0, WS_EPOCH, SC1); break; }
                __builtin_amdgcn_s_sleep(8);
            }
        }
        STAMPV(0, 1); STAMP_DUMP(); return;
    }

    const int cb = mem >> 1, kh = wid & 1, kq = wid & 3;
    const int tb1 = 4 * team + (wid >> 1);
    const int tb2 = 4 * team + 2 * (wid >> 2) + (mem & 1);
    const rsrc_t rx = __builtin_amdgcn_make_buffer_rsrc((void*)p.x, 0, S * D * 2, 0x00020000);
    unsigned ep = 0;

    for (int layer = 0; layer < 2; ++layer) {
        const unsigned wl = opq(WS_LAYER + layer * L_SIZE), fl = opq(WS_FLAGS + layer * FL_LAYER);
        if (layer) STAMPV(layer, 0);
        {
            lane = opqv(lane);
            float sx, sq;
            f32x16 acc[3];
#pragma unroll
            for (int t = 0; t < 3; ++t) acc[t] = (f32x16){};
            const unsigned gneed = lane == 0 ? 8u * (layer + 1) : (lane == 1 + (wid >> 2) ? 4u * (4 * layer) : (lane < 3 && layer ? 4u * (4 * layer - 1) : 0u));
            const int goff = lane == 0 ? C_ATT : C_XRD + ((lane - 1) & 1) * 4;
            int wa[3];
            if (layer == 0) {
                bf16x8 b[16]; f32x4 pr[4]; f32x16 acs;
                const unsigned epv = __builtin_amdgcn_raw_buffer_load_b32(r, 0, WS_EPOCH, SC1);
                u32x4 xr[16];
                const unsigned vo = (lane >> 4) * (D * 2) + (lane & 15) * 16, so = opq((tb1 * 32 * D + 256 * kh) * 2);
#pragma unroll
                for (int i = 0; i < 16; ++i) xr[i] = ldw(rx, vo + (i & 7) * 4 * (D * 2) + (i >> 3) * 256, so);
                ep = rfl(epv) + 1u;
                lds_signal(lds0 + C_ATT, lane);
                STAMPV(1, 22);
                LAS unsigned char* xt = L + LD_XA + wid * 8192;
#pragma unroll
                for (int hf = 0; hf < 2; ++hf) {
#pragma unroll
                    for (int j = 0; j < 8; ++j) { const int row = 4 * j + (lane >> 4); *(LAS u32x4*)(xt + row * 256 + (((lane & 15) ^ (row & 15)) * 16)) = xr[8 * hf + j]; }
#pragma unroll
                    for (int s = 0; s < 8; ++s) b[8 * hf + s] = *(LAS const bf16x8*)(xt + (lane & 31) * 256 + (((2 * s + (lane >> 5)) ^ (lane & 15)) * 16));
                }
                STAMPV(layer, 1);
                lds_wait_vec(L, goff, gneed, r, lane);
                float a1[4] = {0.f, 0.f, 0.f, 0.f}, a2[4] = {0.f, 0.f, 0.f, 0.f};
#pragma unroll
                for (int s = 0; s < 16; ++s) { const u32x4 u = __builtin_bit_cast(u32x4, b[s]);
#pragma unroll
                    for (int e = 0; e < 4; ++e) { const float lo = bflo(u[e]), hi = bfhi(u[e]); a1[e] += lo + hi; a2[e] = __builtin_fmaf(lo, lo, __builtin_fmaf(hi, hi, a2[e])); } }
                const float s1 = xsum32((a1[0] + a1[1]) + (a1[2] + a1[3])), s2 = xsum32((a2[0] + a2[1]) + (a2[2] + a2[3]));
                sx = s1 * (1.f / 256.f); sq = __builtin_fmaxf(s2 - s1 * sx, 0.f);
                asm volatile("" : "+v"(sx), "+v"(sq));
                STAMPV(1, 23);
#pragma unroll
                for (int t = 0; t < 3; ++t) wa[t] = kh * 6144 + (8 * (lane >> 5) + ((lane & 15) >> 2)) * 192 + t * 64 + ((lane >> 4) & 1) * 32 + (lane & 3) * 8;
                STAMPW(layer, 24, gemm_stage<0, 8, 3, 2, 192, 16, 0, true, true, false>(wid, lds, acc, b, wa, 4 * 192, acs, wid >> 1, pr, sx, sq, L, lds0, layer, r, 0, lane));
                lane = opqv(lane);
                if ((wid >> 1) < 3) { cs_store(acs, (LAS float*)(L + LD_CSQ) + 96 * kh + 32 * (wid >> 1), lane); lds_signal(lds0 + C_CS, lane); }
            } else {
                bf16x8 b[16]; f32x4 pr[4]; f32x16 acs;
                wait_flags(r, WS_FLAGS + FL_X2 + tb1 * 32 * WSTR, 0xFFFFu << (16 * kh), lane, ep);
                const unsigned so = opq(WS_LAYER + L_X2F + (tb1 * 32 + 16 * kh) * KB);
#pragma unroll
                for (int s = 0; s < 16; ++s) { b[s] = ldf(r, lane * 16, so + s * KB); if (s == 3) stats_issue(r, WS_LAYER + L_ST2, tb1, kh, lane, pr); }
                STAMPV(layer, 1);
                lds_wait_vec(L, goff, gneed, r, lane);
#pragma unroll
                for (int t = 0; t < 3; ++t) wa[t] = kh * 6144 + (4 * (lane >> 5) + ((lane & 15) >> 2)) * 192 + t * 64 + ((lane >> 4) & 1) * 32 + (lane & 3) * 8;
                STAMPW(layer, 24, gemm_stage<0, 8, 3, 2, 192, 16, 0, true, false, true>(wid, lds, acc, b, wa, 8 * 192, acs, -1, pr, sx, sq, L, lds0, layer, r, 0, lane));
            }
            STAMPV(layer, 2);
            lane = opqv(lane);
            if (lane < 32) ((LAS f32x2*)(L + LD_ST))[wid * 32 + lane] = (f32x2){sx, sq};
            const int blk = 4 * team + (wid >> 1), kb = 8 + (wid >> 1);
            const unsigned kvo = wl + L_KV + (blk * 16 + mem) * 4 * KB;
            if (kh == 0) { asm volatile("; kh 0"); qkv_epi<0>(acc, sx, sq, L, lds0, wid, lane, layer, kb, r, kvo); asm volatile("; kh 0 end"); }
            else { asm volatile("; kh 1"); qkv_epi<1>(acc, sx, sq, L, lds0, wid, lane, layer, kb, r, kvo); asm volatile("; kh 1 end"); }
            lds_signal(lds0 + C_LOCAL + (wid >> 1) * 4, lane);
            STAMPV(layer, 3);
            publish(r, fl + FL_KV + ((blk * 16 + mem) * 2 + kh) * WSTR, lane, ep);
            STAGE_BAR(1);
            STAMPV(layer, 4);
        }
        {
            lane = opqv(lane);
            const int gb = 4 * team - 8 + wid;
            if (gb >= 0) {
                wait_flags(r, fl + FL_KV + (gb * 16 + mem) * 2 * WSTR, 3u, lane, ep);
                STAMPV(layer, 5);
                u32x4 kv[4];
                const unsigned so = opq(wl + L_KV + (gb * 16 + mem) * 4 * KB);
#pragma unroll
                for (int i = 0; i < 4; ++i) kv[i] = ld16(r, lane * 16, so + i * KB);
                lds_wait_vec(L, C_XRD + (lane & 1) * 4, 4u * (4 * layer + 1), r, lane);
#pragma unroll
                for (int i = 0; i < 2; ++i) {
                    *(LAS u32x4*)(L + LD_K + (wid * 2 + i) * 1024 + lane * 16) = kv[i];
                    *(LAS u32x4*)(L + LD_V + (wid * 2 + i) * 1024 + lane * 16) = kv[2 + i];
                }
            }
            lds_signal(lds0 + C_HALO + wid * 4, lane);
            const int qb = wid >> 1, hd = wid & 1;
            STAMPV(layer, 6);
            {
                const bool isl = lane < 4, ish = lane >= 8 && lane < 16;
                const unsigned need = isl ? (lane <= qb ? 2u * (layer + 1) : 0u) : (ish && (lane - 8) >= qb ? (unsigned)(layer + 1) : 0u);
                lds_wait_vec(L, C_LOCAL + (lane & 15) * 4, need, r, lane);
            }
            STAMPV(layer, 7);
            f32x16 o;
            if (team >= 2) attn_unit<true>(L, team, qb, hd, lane, o); else attn_unit<false>(L, team, qb, hd, lane, o);
            STAMPV(layer, 8);
            lane = opqv(lane);
            lds_add1(lds0 + C_ATT, lane);
            const float rl = __builtin_amdgcn_rcpf(hd ? o[0] : o[8]);
            float v[8];
#pragma unroll
            for (int e = 0; e < 8; ++e) v[e] = (hd ? o[8 + e] : o[e]) * rl;
            st16(r, lane * 16, wl + L_OF + ((4 * team + qb) * 32 + 2 * mem + hd) * KB, pack8(v));
            publish(r, fl + FL_O + ((4 * team + qb) * 32 + 2 * mem + hd) * WSTR, lane, ep);
            STAGE_BAR(2);
            STAMPV(layer, 9);
        }
        {
            lane = opqv(lane);
            wait_flags(r, fl + FL_O + tb2 * 32 * WSTR, 0xFFu << (8 * kq), lane, ep);
            STAMPV(layer, 10);
            f32x16 acc[2], acs; bf16x8 b[9]; f32x4 pr[4]; float sx, sq;
            acc[0] = (f32x16){}; acc[1] = (f32x16){};
            int wa[2]; n64_addr(wa, kq, lane);
            u32x4 res;
            if (layer == 0) { const unsigned vo = (lane & 31) * (D * 2) + (lane >> 5) * 8, so = (tb2 * 32 * D + 64 * cb + 16 * kq) * 2;
                const u32x2 a = __builtin_amdgcn_raw_buffer_load_b64(rx, vo, so, 0), c = __builtin_amdgcn_raw_buffer_load_b64(rx, vo + 16, so, 0); res = (u32x4){a.x, a.y, c.x, c.y}; }
            else res = ld16(r, lane * 16, WS_LAYER + L_X2F + (tb2 * 32 + 4 * cb + kq) * KB);
            STAMPW(layer, 25, gemm_stage<8, 4, 2, 99, 128, 9, 0, false, false, false>(wid, lds, acc, b, wa, 8 * 128, acs, -1, pr, sx, sq, L, lds0, layer, r, opq(wl + L_OF + (tb2 * 32 + 8 * kq) * KB), lane));
            STAMPV(layer, 11);
            lane = opqv(lane);
            lds_wait_vec(L, (lane & 1) ? C_ATT : C_XRD + (wid >> 2) * 4, (lane & 1) ? 8u * (layer + 2) : 4u * (4 * layer + 1), r, lane);
            float v[8]; kq_reduce(acc, L, lds0, wid, lane, 4 * layer + 2, r, v);
            resid_add(v, res);
            st16(r, lane * 16, wl + L_X1F + (tb2 * 32 + 4 * cb + kq) * KB, res);
            stats_out(res, r, wl + L_ST1, tb2, 4 * cb + kq, lane);
            STAMPV(layer, 12);
            publish(r, fl + FL_X1 + (tb2 * 32 + 4 * cb + kq) * WSTR, lane, ep);
            STAGE_BAR(3);
            STAMPV(layer, 13);
        }
        {
            lane = opqv(lane);
            wait_flags(r, fl + FL_X1 + tb1 * 32 * WSTR, 0xFFFFu << (16 * kh), lane, ep);
            STAMPV(layer, 14);
            f32x16 acc[4];
#pragma unroll
            for (int t = 0; t < 4; ++t) acc[t] = (f32x16){};
            int wa[4];
            {
                const int q4 = (lane & 15) >> 2;
#pragma unroll
                for (int t = 0; t < 4; ++t) wa[t] = kh * 8192 + (4 * (lane >> 5) + q4) * 256 + (((t ^ (2 * kh)) ^ q4) * 64) + ((lane >> 4) & 1) * 32 + (lane & 3) * 8;
            }
            const unsigned so = opq(wl + L_X1F + (tb1 * 32 + 16 * kh) * KB);
            float sx, sq;
            if (layer == 0) {
                f32x4 pr[4]; f32x16 acs; stats_issue(r, wl + L_ST1, tb1, kh, lane, pr);
                bf16x8 b[4];
                STAMPW(layer, 26, gemm_stage<12, 8, 4, 99, 256, 4, 0, false, true, true>(wid, lds, acc, b, wa, 8 * 256, acs, wid >> 1, pr, sx, sq, L, lds0, layer, r, so, lane));
                lane = opqv(lane);
                cs_store(acs, (LAS float*)(L + LD_CS1) + 128 * kh + 32 * ((wid >> 1) ^ (2 * kh)), lane);
                lds_signal(lds0 + C_CS, lane);
            } else {
                f32x4 pr[4]; f32x16 acs; stats_issue(r, wl + L_ST1, tb1, kh, lane, pr);
                bf16x8 b[RB_F1];
                STAMPW(layer, 26, gemm_stage<12, 8, 4, 99, 256, RB_F1, 0, false, false, true>(wid, lds, acc, b, wa, 8 * 256, acs, -1, pr, sx, sq, L, lds0, layer, r, so, lane));
            }
            STAMPV(layer, 15);
            lane = opqv(lane); const int t32 = lane & 31, h = lane >> 5;
            lds_wait_ge(L, C_XRD + (wid >> 2) * 4, 4u * (4 * layer + 2), r, lane);
            xch_put(L, wid, 0, lane, acc[2], 0); xch_put(L, wid, 1, lane, acc[2], 1); xch_put(L, wid, 2, lane, acc[3], 0); xch_put(L, wid, 3, lane, acc[3], 1);
            if (h == 0) ((LAS f32x2*)(L + LD_ST))[wid * 32 + t32] = (f32x2){sx, sq};
            lds_signal(lds0 + C_XF + wid * 4, lane);
            if (layer == 0) lds_wait_ge(L, C_CS, 14u, r, lane);
            LAS const float* csl = (LAS const float*)(L + LD_CS1) + 64 * kh;
            f32x4 cs[8];
#pragma unroll
            for (int i = 0; i < 8; ++i) cs[i] = *(LAS const f32x4*)(csl + 8 * i + 4 * h) + *(LAS const f32x4*)(csl + 128 + 8 * i + 4 * h);
            lds_wait_ge(L, C_XF + (wid ^ 1) * 4, 4u * layer + 3, r, lane);
            const f32x2 os = ((LAS const f32x2*)(L + LD_ST))[(wid ^ 1) * 32 + t32];
            xch_add(L, wid ^ 1, 0, lane, acc[0], 0); xch_add(L, wid ^ 1, 1, lane, acc[0], 1); xch_add(L, wid ^ 1, 2, lane, acc[1], 0); xch_add(L, wid ^ 1, 3, lane, acc[1], 1);
            lds_signal(lds0 + C_XRD + (wid >> 2) * 4, lane);
            float la, lb; ln_coef(os, sx, sq, la, lb);
            if (mem < 14) {
#pragma unroll
                for (int i = 0; i < 4; ++i) {
                    float hv[8], ex[8];
#pragma unroll
                    for (int e = 0; e < 8; ++e) { hv[e] = la * acc[i >> 1][8 * (i & 1) + e] + lb * cs[2 * i + (e >> 2)][e & 3]; ex[e] = -LOG2E * hv[e]; }
                    SB();
#pragma unroll
                    for (int e = 0; e < 8; ++e) ex[e] = __builtin_amdgcn_exp2f(ex[e]);
                    SB();
#pragma unroll
                    for (int e = 0; e < 8; ++e) ex[e] += 1.f;
                    SB();
#pragma unroll
                    for (int e = 0; e < 8; ++e) ex[e] = __builtin_amdgcn_rcpf(ex[e]);
                    SB();
#pragma unroll
                    for (int e = 0; e < 8; ++e) hv[e] *= ex[e];
                    st16(r, lane * 16, wl + L_HF + (tb1 * 112 + 8 * mem + 4 * kh + i) * KB, pack8(hv));
                    SB();
                }
                STAMPV(layer, 16);
                publish(r, fl + FL_H + (tb1 * 32 + 2 * mem + kh) * WSTR, lane, ep);
            }
            STAGE_BAR(4);
            STAMPV(layer, 17);
        }
        {
            lane = opqv(lane);
            wait_flags(r, fl + FL_H + tb2 * 32 * WSTR, (0x5555555u << (kq >> 1)) & 0xFFFFFFFu, lane, ep);
            STAMPV(layer, 18);
            f32x16 acc[2], acs; bf16x8 b[RB_F2]; f32x4 pr[4]; float sx, sq;
            acc[0] = (f32x16){}; acc[1] = (f32x16){};
            int wa[2]; n64_addr(wa, kq, lane);
            STAMPW(layer, 27, gemm_stage<20, 14, 2, 99, 128, RB_F2, 1, false, false, false>(wid, lds, acc, b, wa, 8 * 128, acs, -1, pr, sx, sq, L, lds0, layer, r, opq(wl + L_HF + (tb2 * 112 + 2 * kq) * KB), lane));
            STAMPV(layer, 19);
            lane = opqv(lane);
            u32x4 res = ld16(r, lane * 16, wl + L_X1F + (tb2 * 32 + 4 * cb + kq) * KB);
            lds_wait_ge(L, C_XRD + (wid >> 2) * 4, 4u * (4 * layer + 3), r, lane);
            float v[8]; kq_reduce(acc, L, lds0, wid, lane, 4 * layer + 4, r, v);
            resid_add(v, res);
            if (layer == 0) {
                st16(r, lane * 16, wl + L_X2F + (tb2 * 32 + 4 * cb + kq) * KB, res);
                stats_out(res, r, wl + L_ST2, tb2, 4 * cb + kq, lane);
                STAMPV(layer, 20);
                publish(r, fl + FL_X2 + (tb2 * 32 + 4 * cb + kq) * WSTR, lane, ep);
                STAGE_BAR(5);
            } else {
                auto s0 = __builtin_amdgcn_permlane32_swap(res.x, res.z, false, false), s1 = __builtin_amdgcn_permlane32_swap(res.y, res.w, false, false);
                unsigned short* dst = p.out + (size_t)(32 * tb2 + (lane & 31)) * D + 64 * cb + 16 * kq + 8 * (lane >> 5);
                *(u32x4*)dst = (u32x4){s0[0], s1[0], s0[1], s1[1]};
            }
            STAMPV(layer, 21);
        }
    }
    STAMP_RT(1); STAMP_DUMP();
}

extern "C" void kernel_launch(void* const* d_in, const int* in_sizes, int n_in, void* d_out, int out_size, void* d_ws, size_t ws_size, hipStream_t stream) {
    Params p;
    p.x = (const unsigned short*)d_in[0]; p.wqkv = (const unsigned short*)d_in[1]; p.wo = (const unsigned short*)d_in[2];
    p.w1 = (const unsigned short*)d_in[4]; p.w2 = (const unsigned short*)d_in[5];
    p.out = (unsigned short*)d_out; p.ws = (unsigned char*)d_ws;
    (void)hipFuncSetAttribute((const void*)k_main, hipFuncAttributeMaxDynamicSharedMemorySize, LDS_BYTES);
    k_main<<<256, NTHR, LDS_BYTES, stream>>>(p);
}
```

```cpp
#include <hip/hip_runtime.h>
#include <hip/hip_bf16.h>
#include <stdint.h>

typedef short bf16x8 __attribute__((ext_vector_type(8)));
typedef float f32x2 __attribute__((ext_vector_type(2)));
typedef float f32x4 __attribute__((ext_vector_type(4)));
typedef float f32x8 __attribute__((ext_vector_type(8)));
typedef float f32x16 __attribute__((ext_vector_type(16)));
typedef unsigned u32x2 __attribute__((ext_vector_type(2)));
typedef unsigned u32x4 __attribute__((ext_vector_type(4)));
typedef __bf16 bf16x2_t __attribute__((ext_vector_type(2)));
#define GAS __attribute__((address_space(1)))
#define LAS __attribute__((address_space(3)))
#define DEV __device__ __forceinline__
#define SB() __builtin_amdgcn_sched_barrier(0)
typedef __amdgpu_buffer_rsrc_t rsrc_t;

constexpr int S = 2048, D = 512, H = 32, F = 1792;
constexpr float EPS = 1e-5f;
constexpr float LOG2E = 1.4426950408889634f;
constexpr float QSCALE = 0.25f * LOG2E;

constexpr unsigned KB = 1024, MB = 1024 * 1024;
constexpr unsigned WS_TMO   = 0;
constexpr unsigned WS_EPOCH = 256;
constexpr unsigned WS_START = 4096;
constexpr unsigned WS_LAYER = 64 * KB;
constexpr unsigned L_KV  = 0;
constexpr unsigned L_OF  = L_KV + 4 * MB;
constexpr unsigned L_X1F = L_OF + 2 * MB;
constexpr unsigned L_HF  = L_X1F + 2 * MB;
constexpr unsigned L_X2F = L_HF + 7 * MB;
constexpr unsigned L_ST1 = L_X2F + 2 * MB;
constexpr unsigned L_ST2 = L_ST1 + 512 * KB;
constexpr unsigned L_SIZE = L_ST2 + 512 * KB;
constexpr unsigned WSTR = 128;
constexpr unsigned WS_FLAGS = 64 * MB;
constexpr unsigned FL_KV = 0, FL_O = FL_KV + 64 * 16 * 2 * WSTR, FL_X1 = FL_O + 64 * 32 * WSTR, FL_H = FL_X1 + 64 * 32 * WSTR, FL_X2 = FL_H + 64 * 32 * WSTR, FL_LAYER = FL_X2 + 64 * 32 * WSTR;
constexpr unsigned WS_END = 72 * MB;
static_assert(WS_LAYER + 2 * L_SIZE <= WS_FLAGS && WS_FLAGS + 2 * FL_LAYER <= WS_END, "workspace map");

constexpr int NCW = 8, NLW = 4, NTHR = 64 * (NCW + NLW);
constexpr int SLOT = 16384, NSLOT = 5;
constexpr int LD_RING = 0;
constexpr int LD_XA = 80 * 1024;
constexpr int LD_K = 96 * 1024;
constexpr int LD_V = 120 * 1024;
constexpr int LD_XB = LD_V;
constexpr int LD_XC0 = LD_K + 16 * 1024, LD_XC1 = LD_V + 16 * 1024;
constexpr int LD_Q = 144 * 1024;
constexpr int LD_MISC = 152 * 1024;
constexpr int LD_CSQ = LD_MISC  , LD_CS1 = LD_MISC + 768  , LD_ONES = LD_MISC + 1792, LD_CTR = LD_MISC + 1856, LD_DUMMY = LD_CTR + 256  ,
              LD_ST = LD_DUMMY + 512  , LD_AB = LD_ST + 2048  , LD_STAMP = LD_AB + 1024;
constexpr int C_FULL = LD_CTR, C_FREE = LD_CTR + 32, C_LOCAL = LD_CTR + 64, C_HALO = LD_CTR + 96, C_XF = LD_CTR + 128, C_XRD = LD_CTR + 160, C_ATT = LD_CTR + 176, C_BAR = LD_CTR + 180, C_CS = LD_CTR + 184;
constexpr int LDS_BYTES = 160 * 1024;
static_assert(LD_STAMP + 8 * 64 * 4 <= LDS_BYTES && LD_CTR % 16 == 0 && LD_ST % 16 == 0, "LDS map");
constexpr int slot_of(int c) { return c % NSLOT; }
constexpr int uses_of(int slot) { return slot == 4 ? 6 : 7; }

struct Params {
    const unsigned short* x; const unsigned short* wqkv; const unsigned short* wo; const unsigned short* w1; const unsigned short* w2;
    unsigned short* out; unsigned char* ws;
};

#define STAMPV(kid, i)
#define STAMPW(kid, i, ...) (void)(__VA_ARGS__)
#define STAMP_RT(i)
#define STAMP_DUMP()
#define STAMP_INIT()

DEV float bf2f(unsigned short b) { return __uint_as_float((unsigned)b << 16); }
DEV float bflo(unsigned u) { return __uint_as_float(u << 16); }
DEV float bfhi(unsigned u) { return __uint_as_float(u & 0xffff0000u); }
DEV unsigned pk2(float lo, float hi) { f32x2 v = {lo, hi}; bf16x2_t r = __builtin_convertvector(v, bf16x2_t); return __builtin_bit_cast(unsigned, r); }
DEV float xsum32(float v) { auto r = __builtin_amdgcn_permlane32_swap(__float_as_uint(v), __float_as_uint(v), false, false); return __uint_as_float(r[0]) + __uint_as_float(r[1]); }
DEV float xmax32(float v) { auto r = __builtin_amdgcn_permlane32_swap(__float_as_uint(v), __float_as_uint(v), false, false); return __builtin_fmaxf(__uint_as_float(r[0]), __uint_as_float(r[1])); }
DEV f32x16 mfma32(bf16x8 a, bf16x8 b, f32x16 c) { return __builtin_amdgcn_mfma_f32_32x32x16_bf16(a, b, c, 0, 0, 0); }
DEV float dot2(unsigned a, unsigned b, float c) { return __builtin_amdgcn_fdot2_f32_bf16(__builtin_bit_cast(bf16x2_t, a), __builtin_bit_cast(bf16x2_t, b), c, false); }
DEV unsigned opq(unsigned x) { asm volatile("" : "+s"(x)); return x; }
DEV int opqv(int x) { asm volatile("" : "+v"(x)); return x; }
DEV unsigned rfl(unsigned v) { return __builtin_amdgcn_readfirstlane(v); }
#define WAITV(n) asm volatile("s_waitcnt vmcnt(" #n ")" ::: "memory")
constexpr int SC1 = 16;
DEV bf16x8 ldf(rsrc_t r, unsigned vo, unsigned so) { return __builtin_bit_cast(bf16x8, __builtin_amdgcn_raw_buffer_load_b128(r, vo, so, SC1)); }
DEV u32x4 ld16(rsrc_t r, unsigned vo, unsigned so) { return __builtin_amdgcn_raw_buffer_load_b128(r, vo, so, SC1); }
DEV u32x4 ldw(rsrc_t r, unsigned vo, unsigned so) { return __builtin_amdgcn_raw_buffer_load_b128(r, vo, so, 0); }
DEV void st16(rsrc_t r, unsigned vo, unsigned so, u32x4 v) { __builtin_amdgcn_raw_buffer_store_b128(v, r, vo, so, SC1); asm volatile("s_nop 1" :: "v"(v) : "memory"); }

DEV void block_map(int b, int& team, int& mem) {
    const int x = b & 7, r = b >> 3;
    team = 4 * (x >> 1) + (r >> 3); mem = 8 * (x & 1) + (r & 7);
}

#define GATE_O 3
#define GATE_F 18
#define GATE_Q 31
#define PACE 0
#define PACE_O 3
#define PACE_F 2
#define PACE_Q 1
#define RB_Q 8
#define RB_F1 6
#define RB_F2 12
#define POLL_SLEEP 1
constexpr unsigned SPIN_CAP = 1u << 15;
DEV void tmo_set(rsrc_t r, int lane) { if (lane == 0) __builtin_amdgcn_raw_buffer_store_b32(1u, r, 0, WS_TMO, SC1); }
DEV void wait_flags(rsrc_t r, unsigned off, unsigned mask, int lane, unsigned ep) {
    bool need = lane < 32 && ((mask >> (lane & 31)) & 1u);
#pragma unroll 1
    for (unsigned spins = 0;; ++spins) {
        if (need) need = __builtin_amdgcn_raw_buffer_load_b32(r, lane * WSTR, off, SC1) != ep;
        if (!__any(need)) break;
        if (spins > SPIN_CAP) { tmo_set(r, lane); break; }
        __builtin_amdgcn_s_sleep(POLL_SLEEP);
    }
    __builtin_amdgcn_fence(__ATOMIC_ACQUIRE, "wavefront");
}
DEV void publish(rsrc_t r, unsigned word_off, int lane, unsigned ep) {
    WAITV(0);
    if (lane == 0) __builtin_amdgcn_raw_buffer_store_b32(ep, r, 0, word_off, SC1);
}
DEV unsigned lds_ld(LAS unsigned char* L, int off) { return *(volatile LAS unsigned*)(L + off); }
DEV int s2v(int x) { int v; asm volatile("v_mov_b32 %0, %1" : "=v"(v) : "s"(x)); return v; }
DEV unsigned lds_ldu(LAS unsigned char* L, int off) { return *(volatile LAS unsigned*)(L + s2v(off)); }
DEV void lds_add1(unsigned addr, int lane) {
    const unsigned a = lane == 0 ? addr : (unsigned)(LD_DUMMY + lane * 4);
    asm volatile("ds_add_u32 %0, %1" :: "v"(a), "v"(1u) : "memory");
}
DEV void lds_signal(unsigned addr, int lane) { lds_add1(addr, lane); }
DEV void lds_wait_ge(LAS unsigned char* L, int off, unsigned target, rsrc_t r, int lane) {
#pragma unroll 1
    for (unsigned spins = 0; rfl(lds_ldu(L, off)) < target; ++spins) {
        if (spins > (SPIN_CAP << 3)) break;
        if (spins > 2) __builtin_amdgcn_s_sleep(1);
    }
    asm volatile("" ::: "memory");
}
DEV void lds_wait_vec(LAS unsigned char* L, int off, unsigned need, rsrc_t r, int lane) {
#pragma unroll 1
    for (unsigned spins = 0; !__all(lds_ld(L, off) >= need); ++spins) {
        if (spins > (SPIN_CAP << 3)) break;
        if (spins > 2) __builtin_amdgcn_s_sleep(1);
    }
    asm volatile("" ::: "memory");
}

template <int C0> DEV int ring_ready(LAS unsigned char* L, int c, int layer, rsrc_t r, int lane) {
    const int ca = C0 + c + (lane & 7), sl = ca % NSLOT;
    const unsigned tgt = 4u * (layer * (sl == 4 ? 6 : 7) + ca / NSLOT + 1);
    unsigned long long ok;
#pragma unroll 1
    for (unsigned spins = 0;; ++spins) {
        ok = __ballot(lds_ld(L, C_FULL + sl * 4) >= tgt);
        if (ok & 1ull) break;
        if (spins > (SPIN_CAP << 3)) { ok = ~0ull; break; }
        if (spins > 2) __builtin_amdgcn_s_sleep(1);
    }
    asm volatile("" ::: "memory");
    const int n = __builtin_ctzll(~ok);
    return c + (n < NSLOT ? n : NSLOT);
}
DEV void stats_issue(rsrc_t r, unsigned st, int tb, int kh, int lane, f32x4 (&pr)[4]) {
    const unsigned so = opq(st + (tb * 16 + 8 * kh) * 512);
#pragma unroll
    for (int i = 0; i < 4; ++i) pr[i] = __builtin_bit_cast(f32x4, ld16(r, ((lane >> 5) * 4 * 32 + (lane & 31)) * 16 + i * 512, so));
}
DEV void stats_half(const f32x4 (&pr)[4], float& mean, float& m2) {
    float ms = 0.f;
#pragma unroll
    for (int i = 0; i < 4; ++i) ms += pr[i][0] + pr[i][2];
    mean = xsum32(ms) * (1.f / 16.f);
    float q = 0.f;
#pragma unroll
    for (int i = 0; i < 4; ++i) { const float d0 = pr[i][0] - mean, d1 = pr[i][2] - mean; q += pr[i][1] + pr[i][3] + 16.f * (d0 * d0 + d1 * d1); }
    m2 = xsum32(q);
}
DEV void stats_out(u32x4 o, rsrc_t r, unsigned st, int tb, int s, int lane) {
    const float q8[8] = {bflo(o.x), bfhi(o.x), bflo(o.y), bfhi(o.y), bflo(o.z), bfhi(o.z), bflo(o.w), bfhi(o.w)};
    const float mean = xsum32(((q8[0] + q8[1]) + (q8[2] + q8[3])) + ((q8[4] + q8[5]) + (q8[6] + q8[7]))) * (1.f / 16.f);
    float q = 0.f;
#pragma unroll
    for (int e = 0; e < 8; ++e) { const float d = q8[e] - mean; q += d * d; }
    q = xsum32(q);
    if (lane < 32) __builtin_amdgcn_raw_buffer_store_b64((u32x2){__float_as_uint(mean), __float_as_uint(q)}, r, lane * 16 + (s & 1) * 8, st + (tb * 16 + (s >> 1)) * 512, SC1);
}

typedef short v4i16 __attribute__((ext_vector_type(4)));
DEV v4i16 trd(LAS const unsigned char* p) { return __builtin_amdgcn_ds_read_tr16_b64_v4i16((LAS v4i16*)p); }
template <int C0, int NCH, int NT, int SWAPV, int P, int RB, int AMODE, bool PRE, bool CS, bool ST>
DEV unsigned gemm_stage(int wid, unsigned char* lds, f32x16 (&acc)[NT], bf16x8 (&b)[RB], const int (&wa)[NT], int tau, f32x16& acs, int cst, const f32x4 (&pr)[4], float& sx, float& sq, LAS unsigned char* L, unsigned lds0, int layer, rsrc_t r, unsigned so, int lane) {
    unsigned twait = 0;
    constexpr int NS = 2 * NCH, JS = 16 * P;
    const unsigned l16 = lane * 16;
#define AOFF(s_) (AMODE == 0 ? (s_) * KB : (((s_) >> 1) * 8 + ((s_) & 1)) * KB)
    if (!PRE) {
#pragma unroll
        for (int s = 0; s < RB - 1; ++s) if (s < NS) b[s] = ldf(r, l16, so + AOFF(s));
    }
    LAS const unsigned char* w0[NT]; LAS const unsigned char* w1[NT];
#pragma unroll
    for (int t = 0; t < NT; ++t) { w0[t] = L + LD_RING + wa[t]; w1[t] = L + opqv(LD_RING + 4 * SLOT + wa[t]); }
    const unsigned fadr = lane == 0 ? lds0 + C_FREE : (unsigned)(LD_DUMMY + lane * 4);
#define WADDR(s_, t_) ((slot_of(C0 + ((s_) >> 1)) == 4 ? w1[t_] : w0[t_] + slot_of(C0 + ((s_) >> 1)) * SLOT) + ((s_) & 1) * JS)
#define WFRAG(s_, t_) __builtin_shufflevector(trd(WADDR(s_, t_)), trd(WADDR(s_, t_) + tau), 0, 1, 2, 3, 4, 5, 6, 7)
    const unsigned one2 = (unsigned)s2v(0x3f803f80);
    const bf16x8 ones = __builtin_bit_cast(bf16x8, (u32x4){one2, one2, one2, one2});
    int ready = ring_ready<C0>(L, 0, layer, r, lane);
    bf16x8 fa[NT];
#pragma unroll
    for (int t = 0; t < NT; ++t) fa[t] = WFRAG(0, t);
    SB();
#pragma unroll
    for (int s = 0; s < NS; ++s) {
        if (!PRE && s + RB - 1 < NS) b[(s + RB - 1) % RB] = ldf(r, l16, so + AOFF(s + RB - 1));
        const int sp = s + 1;
        if (sp < NS && (sp & 1) == 0) { if ((sp >> 1) >= ready) {
            ready = ring_ready<C0>(L, sp >> 1, layer, r, lane);
        } }
        if (CS) {
            u32x4 f = __builtin_bit_cast(u32x4, fa[0]);
#pragma unroll
            for (int t = 1; t < NT; ++t) { const u32x4 g = __builtin_bit_cast(u32x4, fa[t]);
#pragma unroll
                for (int e = 0; e < 4; ++e) f[e] = cst == t ? g[e] : f[e]; }
            if (s == 0) acs = mfma32(__builtin_bit_cast(bf16x8, f), ones, (f32x16){}); else acs = mfma32(__builtin_bit_cast(bf16x8, f), ones, acs);
        }
        SB();
#pragma unroll
        for (int t = 0; t < NT; ++t) {
            if (t >= SWAPV) acc[t] = mfma32(b[s % RB], fa[t], acc[t]); else acc[t] = mfma32(fa[t], b[s % RB], acc[t]);
            if (sp < NS) fa[t] = WFRAG(sp, t);
            SB();
        }
        if (s & 1) asm volatile("ds_add_u32 %0, %1 offset:%2" :: "v"(fadr), "v"(1u), "n"(slot_of(C0 + (s >> 1)) * 4) : "memory");
        if (ST && s == 3) { stats_half(pr, sx, sq); asm volatile("" : "+v"(sx), "+v"(sq)); }
        if (s == 0) STAMPV(layer, C0 == 0 ? 28 : C0 == 8 ? 29 : C0 == 12 ? 30 : 31);
    }
#undef AOFF
#undef WADDR
#undef WFRAG
    return twait;
}
DEV int xch_off(int w, int slot) { return slot < 2 ? LD_XA + w * 4096 + slot * 2048 : slot == 2 ? LD_XB + w * 2048 : (w < 4 ? LD_XC0 + w * 2048 : LD_XC1 + (w - 4) * 2048); }
DEV void xch_put(LAS unsigned char* L, int w, int slot, int lane, const f32x16& a, int half) {
    LAS f32x4* d = (LAS f32x4*)(L + xch_off(w, slot)) + lane;
    d[0] = (f32x4){a[8 * half], a[8 * half + 1], a[8 * half + 2], a[8 * half + 3]}; d[64] = (f32x4){a[8 * half + 4], a[8 * half + 5], a[8 * half + 6], a[8 * half + 7]};
}
DEV void xch_add(LAS unsigned char* L, int w, int slot, int lane, f32x16& a, int half) {
    LAS const f32x4* d = (LAS const f32x4*)(L + xch_off(w, slot)) + lane;
    const f32x4 u = d[0], v = d[64];
#pragma unroll
    for (int e = 0; e < 4; ++e) { a[8 * half + e] += u[e]; a[8 * half + 4 + e] += v[e]; }
}
DEV void ln_coef(f32x2 o, float mean_h, float m2_h, float& la, float& lb) {
    const float mean = 0.5f * (mean_h + o.x), d = mean_h - o.x;
    const float var = (m2_h + o.y + 128.f * d * d) * (1.f / 512.f);
    la = __builtin_amdgcn_rsqf(var + EPS); lb = -mean * la;
}
DEV void kq_reduce(f32x16 (&acc)[2], LAS unsigned char* L, unsigned lds0, int wid, int lane, unsigned seq, rsrc_t r, float (&v)[8]) {
    const int base = wid & 4;
    xch_put(L, wid, 0, lane, acc[0], 1); xch_put(L, wid, 1, lane, acc[1], 0); xch_put(L, wid, 2, lane, acc[1], 1);
    lds_signal(lds0 + C_XF + wid * 4, lane);
    lds_wait_vec(L, C_XF + (base + (lane & 3)) * 4, seq, r, lane);
#pragma unroll
    for (int x = 1; x < 4; ++x) xch_add(L, wid ^ x, x - 1, lane, acc[0], 0);
    lds_signal(lds0 + C_XRD + (wid >> 2) * 4, lane);
#pragma unroll
    for (int e = 0; e < 8; ++e) v[e] = acc[0][e];
}
DEV u32x4 pack8(const float (&f)[8]) { return (u32x4){pk2(f[0], f[1]), pk2(f[2], f[3]), pk2(f[4], f[5]), pk2(f[6], f[7])}; }
DEV void resid_add(const float (&v)[8], u32x4& res) {
    const float f[8] = {bflo(res.x) + v[0], bfhi(res.x) + v[1], bflo(res.y) + v[2], bfhi(res.y) + v[3], bflo(res.z) + v[4], bfhi(res.z) + v[5], bflo(res.w) + v[6], bfhi(res.w) + v[7]};
    res = pack8(f);
}

template <int KH>
DEV void qkv_epi(f32x16 (&acc)[3], float sx, float sq, LAS unsigned char* L, unsigned lds0, int wid, int lane, int layer, int kb, rsrc_t r, unsigned kvo) {
    const int t32 = lane & 31, h = lane >> 5;
    xch_put(L, wid, 0, lane, acc[0], 1 - KH); xch_put(L, wid, 1, lane, acc[1], 1 - KH); xch_put(L, wid, 2, lane, acc[2], 1 - KH);
    lds_signal(lds0 + C_XF + wid * 4, lane);
    if (layer == 0) lds_wait_ge(L, C_CS, 6u, r, lane);
    LAS const float* csl = (LAS const float*)(L + LD_CSQ);
    f32x4 cq[2], ck[2];
#pragma unroll
    for (int u = 0; u < 2; ++u) { cq[u] = *(LAS const f32x4*)(csl + 16 * KH + 8 * u + 4 * h) + *(LAS const f32x4*)(csl + 96 + 16 * KH + 8 * u + 4 * h);
        ck[u] = *(LAS const f32x4*)(csl + 32 + 16 * KH + 8 * u + 4 * h) + *(LAS const f32x4*)(csl + 96 + 32 + 16 * KH + 8 * u + 4 * h); }
    const float cv = csl[64 + t32] + csl[96 + 64 + t32];
    lds_wait_ge(L, C_XF + (wid ^ 1) * 4, 4u * layer + 1, r, lane);
    const f32x2 os = ((LAS const f32x2*)(L + LD_ST))[(wid ^ 1) * 32 + t32];
    xch_add(L, wid ^ 1, 0, lane, acc[0], KH); xch_add(L, wid ^ 1, 1, lane, acc[1], KH); xch_add(L, wid ^ 1, 2, lane, acc[2], KH);
    lds_signal(lds0 + C_XRD + (wid >> 2) * 4, lane);
    float la, lb; ln_coef(os, sx, sq, la, lb);
    LAS f32x2* ab = (LAS f32x2*)(L + LD_AB) + (wid >> 1) * 32;
    if (h == 0) ab[t32] = (f32x2){la, lb};
    f32x4 s01[2], s23[2];
#pragma unroll
    for (int u = 0; u < 2; ++u) { s01[u] = *(LAS const f32x4*)(ab + 16 * KH + 8 * u + 4 * h); s23[u] = *(LAS const f32x4*)(ab + 16 * KH + 8 * u + 4 * h + 2); }
    float v[8];
#pragma unroll
    for (int u = 0; u < 2; ++u)
#pragma unroll
        for (int e = 0; e < 4; ++e) v[4 * u + e] = la * acc[1][8 * KH + 4 * u + e] + lb * ck[u][e];
    const u32x4 ok = pack8(v);
    st16(r, lane * 16, kvo + KH * KB, ok);
    *(LAS u32x4*)(L + LD_K + (kb * 2 + KH) * 1024 + lane * 16) = ok;
#pragma unroll
    for (int u = 0; u < 2; ++u) {
        v[4 * u + 0] = s01[u][0] * acc[2][8 * KH + 4 * u + 0] + s01[u][1] * cv; v[4 * u + 1] = s01[u][2] * acc[2][8 * KH + 4 * u + 1] + s01[u][3] * cv;
        v[4 * u + 2] = s23[u][0] * acc[2][8 * KH + 4 * u + 2] + s23[u][1] * cv; v[4 * u + 3] = s23[u][2] * acc[2][8 * KH + 4 * u + 3] + s23[u][3] * cv; }
    const u32x4 ov = pack8(v);
    st16(r, lane * 16, kvo + (2 + KH) * KB, ov);
    *(LAS u32x4*)(L + LD_V + (kb * 2 + KH) * 1024 + lane * 16) = ov;
    const float qa = la * QSCALE, qb_ = lb * QSCALE;
#pragma unroll
    for (int u = 0; u < 2; ++u)
#pragma unroll
        for (int e = 0; e < 4; ++e) v[4 * u + e] = qa * acc[0][8 * KH + 4 * u + e] + qb_ * cq[u][e];
    *(LAS u32x4*)(L + LD_Q + ((kb - 8) * 2 + KH) * 1024 + lane * 16) = pack8(v);
}
DEV void cs_store(const f32x16& a, LAS float* dst, int lane) {
    if ((lane & 31) == 0) {
#pragma unroll
        for (int u = 0; u < 4; ++u) *(LAS f32x4*)(dst + 8 * u + 4 * (lane >> 5)) = (f32x4){a[4 * u], a[4 * u + 1], a[4 * u + 2], a[4 * u + 3]};
    }
}

#define EXP2(x) __builtin_amdgcn_exp2f(x)
DEV void mask_first(f32x16& s, int q32, int h) {
#pragma unroll
    for (int r = 0; r < 16; ++r) { const int key = (r & 3) + 8 * (r >> 2) + 4 * h; if (key <= q32) s[r] = -INFINITY; }
}
DEV void mask_last(f32x16& s, int q32, int h) {
#pragma unroll
    for (int r = 0; r < 16; ++r) { const int key = (r & 3) + 8 * (r >> 2) + 4 * h; if (key > q32) s[r] = -INFINITY; }
}
DEV float max16(const f32x16& s, float m) {
    float a = __builtin_fmaxf(__builtin_fmaxf(s[0], s[1]), m), b = __builtin_fmaxf(__builtin_fmaxf(s[2], s[3]), s[4]);
    a = __builtin_fmaxf(__builtin_fmaxf(a, s[5]), s[6]); b = __builtin_fmaxf(__builtin_fmaxf(b, s[7]), s[8]);
    a = __builtin_fmaxf(__builtin_fmaxf(a, s[9]), s[10]); b = __builtin_fmaxf(__builtin_fmaxf(b, s[11]), s[12]);
    a = __builtin_fmaxf(__builtin_fmaxf(a, s[13]), s[14]);
    return __builtin_fmaxf(__builtin_fmaxf(a, b), s[15]);
}
template <bool FULL9>
DEV void attn_unit(LAS const unsigned char* L, int team, int qb, int hd, int lane, f32x16& o) {
    const int q32 = lane & 31, h = lane >> 5;
    const bf16x8 qf = *(LAS const bf16x8*)(L + LD_Q + (qb * 2 + hd) * 1024 + lane * 16);
    const int first = FULL9 ? 0 : 8 - 4 * team - qb;
    LAS const unsigned char* kp = L + LD_K + (qb * 2 + hd) * 1024 + lane * 16;
    const bool onel = ((lane >> 4) & 1) != hd;
    const unsigned vstr = onel ? 0u : 2048u;
    LAS const unsigned char* vp = onel ? L + LD_ONES : L + LD_V + qb * 2048 + lane * 16;
    LAS const unsigned char* vq = onel ? L + LD_ONES : L + LD_V + qb * 2048 + 1024 + lane * 16;
    bf16x8 kf[9];
#pragma unroll
    for (int i = 0; i < 9; ++i) if (FULL9 || i >= first) kf[i] = *(LAS const bf16x8*)(kp + i * 2048);
    const f32x16 zero = {};
    float m = -INFINITY;
    if (FULL9) {
        f32x16 sa = mfma32(kf[0], qf, zero), sb;
#pragma unroll
        for (int i = 0; i < 9; ++i) {
            f32x16& cur = (i & 1) ? sb : sa; f32x16& nxt = (i & 1) ? sa : sb;
            if (i + 1 < 9) nxt = mfma32(kf[i + 1], qf, zero);
            SB();
            if (i == 0) mask_first(cur, q32, h);
            if (i == 8) mask_last(cur, q32, h);
            m = max16(cur, m);
            SB();
        }
    } else {
#pragma unroll
        for (int i = 0; i < 9; ++i) if (i >= first) {
            f32x16 s = mfma32(kf[i], qf, zero);
            if (i == 0) mask_first(s, q32, h);
            if (i == 8) mask_last(s, q32, h);
            m = max16(s, m);
        }
    }
    m = xmax32(m);
    f32x16 nm;
#pragma unroll
    for (int r = 0; r < 16; ++r) nm[r] = -m;
    if (FULL9) {
        f32x16 sa = mfma32(kf[0], qf, nm), sb;
        bf16x8 vn0 = *(LAS const bf16x8*)(vp), vn1 = *(LAS const bf16x8*)(vq);
#pragma unroll
        for (int i = 0; i < 9; ++i) {
            f32x16& cur = (i & 1) ? sb : sa; f32x16& nxt = (i & 1) ? sa : sb;
            const bf16x8 v0 = vn0, v1 = vn1;
            if (i + 1 < 9) { vn0 = *(LAS const bf16x8*)(vp + (i + 1) * vstr); vn1 = *(LAS const bf16x8*)(vq + (i + 1) * vstr); }
            if (i + 1 < 9) nxt = mfma32(kf[i + 1], qf, nm);
            SB();
            if (i == 0) mask_first(cur, q32, h);
            if (i == 8) mask_last(cur, q32, h);
            u32x4 p0, p1;
#pragma unroll
            for (int e = 0; e < 4; ++e) p0[e] = pk2(EXP2(cur[2 * e]), EXP2(cur[2 * e + 1]));
            SB();
            if (i == 0) o = mfma32(v0, __builtin_bit_cast(bf16x8, p0), zero); else o = mfma32(v0, __builtin_bit_cast(bf16x8, p0), o);
#pragma unroll
            for (int e = 0; e < 4; ++e) p1[e] = pk2(EXP2(cur[8 + 2 * e]), EXP2(cur[8 + 2 * e + 1]));
            SB();
            o = mfma32(v1, __builtin_bit_cast(bf16x8, p1), o);
            SB();
        }
    } else {
        float z = 0.f; asm volatile("" : "+v"(z));
#pragma unroll
        for (int r = 0; r < 16; ++r) o[r] = z;
#pragma unroll
        for (int i = 0; i < 9; ++i) if (i >= first) {
            f32x16 s = mfma32(kf[i], qf, nm);
            if (i == 0) mask_first(s, q32, h);
            if (i == 8) mask_last(s, q32, h);
            u32x4 p0, p1;
#pragma unroll
            for (int e = 0; e < 4; ++e) { p0[e] = pk2(EXP2(s[2 * e]), EXP2(s[2 * e + 1])); p1[e] = pk2(EXP2(s[8 + 2 * e]), EXP2(s[8 + 2 * e + 1])); }
            const bf16x8 v0 = *(LAS const bf16x8*)(vp + i * vstr), v1 = *(LAS const bf16x8*)(vq + i * vstr);
            o = mfma32(v0, __builtin_bit_cast(bf16x8, p0), o);
            o = mfma32(v1, __builtin_bit_cast(bf16x8, p1), o);
        }
    }
}

struct LoaderCtx { rsrc_t rq, ro, r1, r2; unsigned sq, so_, s1; int lw; };
template <int C, int PC = PACE> DEV void loader_fetch(u32x4 (&buf)[7][4], const LoaderCtx& x, int lane_) {
    constexpr int NI = C < 8 ? 3 : 4;
    const int lane = opqv(lane_), lw = x.lw;
#pragma unroll
    for (int j = 0; j < NI; ++j) {
        if (C < 8) {
            const int i = lw * 192 + j * 64 + lane, row = (i * 683) >> 13, c = i - 12 * row;
            buf[C % 7][j] = ldw(x.rq, ((256 * (row >> 5) + (row & 31)) * 1536 + (c >> 2) * 512 + 8 * (c & 3)) * 2, opq(x.sq + C * 32 * 1536 * 2));
        } else if (C < 12 || C >= 20) {
            const int r8 = lw * 32 + j * 8 + (lane >> 3), c8 = (lane & 7) ^ (((r8 >> 1) & 1) << 2);
            if (C < 12) buf[C % 7][j] = ldw(x.ro, ((128 * (r8 >> 5) + (r8 & 31)) * 512 + 8 * c8) * 2, opq(x.so_ + (C - 8) * 32 * 512 * 2));
            else buf[C % 7][j] = ldw(x.r2, (r8 * 512 + 8 * c8) * 2, opq(x.so_ + (C - 20) * 128 * 512 * 2));
        } else {
            const int r16 = lw * 16 + j * 4 + (lane >> 4), c16 = lane & 15;
            buf[C % 7][j] = ldw(x.r1, ((256 * (r16 >> 5) + (r16 & 31)) * 1792 + (((c16 >> 2) ^ (r16 & 3)) * 32) + 8 * (c16 & 3)) * 2, opq(x.s1 + (C - 12) * 32 * 1792 * 2));
        }
        if (PC) __builtin_amdgcn_s_sleep(PC);
    }
}
template <int C> DEV void loader_step(u32x4 (&buf)[7][4], const LoaderCtx& x, LAS unsigned char* L, unsigned lds0, int lw, int layer, rsrc_t r, int lane) {
    constexpr int NI = C < 8 ? 3 : 4, SL = C % NSLOT;
    const unsigned l16 = lane * 16;
    if (C < 34) {
        lds_wait_ge(L, C_FREE + SL * 4, 8u * (layer * uses_of(SL) + C / NSLOT), r, lane);
        LAS unsigned char* dst = L + LD_RING + SL * SLOT + lw * NI * 1024 + l16;
#pragma unroll
        for (int i = 0; i < NI; ++i) *(LAS u32x4*)(dst + i * 1024) = buf[C % 7][i];
        lds_signal(lds0 + C_FULL + SL * 4, lane);
    }
    constexpr int N = (C + 7) % 35;
    constexpr bool DEFER = N >= 8 && N <= 14;
    if (!DEFER && N != 34 && (C + 7 < 35 || layer == 0)) loader_fetch<N>(buf, x, lane);
    if (C == 7) {
        lds_wait_ge(L, C_FREE + (GATE_O % NSLOT) * 4, 8u * (layer * uses_of(GATE_O % NSLOT) + GATE_O / NSLOT + 1), r, lane);
        loader_fetch<8>(buf, x, lane); loader_fetch<9>(buf, x, lane); loader_fetch<10>(buf, x, lane); loader_fetch<11>(buf, x, lane); loader_fetch<12>(buf, x, lane); loader_fetch<13>(buf, x, lane); loader_fetch<14>(buf, x, lane);
    }
    SB();
}
template <int... Cs> struct seq {};
template <int N, int... Cs> struct mkseq : mkseq<N - 1, N - 1, Cs...> {};
template <int... Cs> struct mkseq<0, Cs...> { typedef seq<Cs...> type; };
template <int... Cs> DEV void loader_layer(seq<Cs...>, u32x4 (&buf)[7][4], const LoaderCtx& x, LAS unsigned char* L, unsigned lds0, int lw, int layer, rsrc_t r, int lane) {
    (loader_step<Cs>(buf, x, L, lds0, lw, layer, r, lane), ...);
}
template <int... Cs> DEV void loader_prime(seq<Cs...>, u32x4 (&buf)[7][4], const LoaderCtx& x, int lane) { (loader_fetch<Cs>(buf, x, lane), ...); }
DEV unsigned loader(const Params& p, LAS unsigned char* L, unsigned lds0, int lw, int mem, rsrc_t r, int lane, bool first) {
    const int cb = mem >> 1, mem1 = mem < 14 ? mem : 13;
    LoaderCtx x;
    x.rq = __builtin_amdgcn_make_buffer_rsrc((void*)p.wqkv, 0, D * 3 * D * 2, 0x00020000); x.ro = __builtin_amdgcn_make_buffer_rsrc((void*)p.wo, 0, D * D * 2, 0x00020000);
    x.r1 = __builtin_amdgcn_make_buffer_rsrc((void*)p.w1, 0, D * F * 2, 0x00020000); x.r2 = __builtin_amdgcn_make_buffer_rsrc((void*)p.w2, 0, F * D * 2, 0x00020000);
    x.sq = 32 * mem * 2; x.so_ = 64 * cb * 2; x.s1 = 128 * mem1 * 2; x.lw = lw;
    u32x4 buf[7][4];
    loader_prime(mkseq<7>::type(), buf, x, lane);
    unsigned ep = 0;
    if (first) { ep = rfl(__builtin_amdgcn_raw_buffer_load_b32(r, 0, WS_EPOCH, SC1)) + 1u; lds_wait_ge(L, C_ATT, 8u, r, lane); if (lane == 0) __builtin_amdgcn_raw_buffer_store_b32(ep, r, 0, WS_START + blockIdx.x * 4, SC1); }
    for (int layer = 0; layer < 2; ++layer) loader_layer(mkseq<35>::type(), buf, x, L, lds0, lw, layer, r, lane);
    return ep;
}

DEV void n64_addr(int (&wa)[2], int kq, int lane) {
    const int q4 = (lane & 15) >> 2;
#pragma unroll
    for (int t = 0; t < 2; ++t) { const int g = (2 * t + ((lane >> 4) & 1)) ^ kq;
        wa[t] = kq * 4096 + (4 * (lane >> 5) + q4) * 128 + (((g >> 1) ^ ((q4 >> 1) & 1)) * 64) + (g & 1) * 32 + (lane & 3) * 8; }
}
#define STAGE_BAR(n) do { lds_signal(lds0 + C_BAR, lane); lds_wait_ge(L, C_BAR, 8u * (5 * layer + (n)), r, lane); } while (0)
__global__ __launch_bounds__(NTHR, 3) void k_main(Params p) {
    extern __shared__ __attribute__((aligned(16))) unsigned char lds[];
    int team, mem; block_map(blockIdx.x, team, mem);
    const int tid = threadIdx.x, wid = rfl(tid >> 6); int lane = tid & 63;
    LAS unsigned char* L = (LAS unsigned char*)lds;
    const unsigned lds0 = (unsigned)(size_t)L;
    const rsrc_t r = __builtin_amdgcn_make_buffer_rsrc(p.ws, 0, WS_END, 0x00020000);
    STAMP_RT(0); STAMP_INIT();
    if (wid >= NCW) {
        if (wid < NCW + 2) {
            const unsigned ln = (blockIdx.x * 2 + (wid - NCW)) * 64 + lane;
            const unsigned char* a = ln < 4096 ? (const unsigned char*)p.wo + ln * 128 : ln < 4096 + 14336 ? (const unsigned char*)p.w1 + (ln - 4096) * 128 : (const unsigned char*)p.w2 + (ln - 4096 - 14336) * 128;
            unsigned sv; asm volatile("s_mov_b32 %0, m0\n\ts_mov_b32 m0, %2\n\ts_nop 2\n\tglobal_load_lds_dword %1, off\n\ts_mov_b32 m0, %0" : "=&s"(sv) : "v"(a), "s"(lds0 + LD_DUMMY) : "memory");
        }
    }
    if (tid < 64) ((LAS unsigned*)(L + LD_CTR))[tid] = 0u;
    if (tid >= 64 && tid < 68) ((LAS unsigned*)(L + LD_ONES))[tid & 3] = 0x3f803f80u;
    __syncthreads();
    STAMPV(0, 0);
    if (wid >= NCW) {
        const int ln = (int)__builtin_amdgcn_mbcnt_hi(~0u, __builtin_amdgcn_mbcnt_lo(~0u, 0u));
        const unsigned ep = loader(p, L, lds0, wid - NCW, mem, r, ln, wid == NCW);
        if (blockIdx.x == 0 && wid == NCW) {
#pragma unroll 1
            for (unsigned spins = 0; spins < SPIN_CAP; ++spins) {
                const u32x4 v = ld16(r, ln * 16, WS_START);
                if (__all(v.x == ep && v.y == ep && v.z == ep && v.w == ep)) { if (ln == 0) __builtin_amdgcn_raw_buffer_store_b32(ep, r, 0, WS_EPOCH, SC1); break; }
                __builtin_amdgcn_s_sleep(8);
            }
        }
        STAMPV(0, 1); STAMP_DUMP(); return;
    }

    const int cb = mem >> 1, kh = wid & 1, kq = wid & 3;
    const int tb1 = 4 * team + (wid >> 1);
    const int tb2 = 4 * team + 2 * (wid >> 2) + (mem & 1);
    const rsrc_t rx = __builtin_amdgcn_make_buffer_rsrc((void*)p.x, 0, S * D * 2, 0x00020000);
    unsigned ep = 0;

    for (int layer = 0; layer < 2; ++layer) {
        const unsigned wl = opq(WS_LAYER + layer * L_SIZE), fl = opq(WS_FLAGS + layer * FL_LAYER);
        if (layer) STAMPV(layer, 0);
        {
            lane = opqv(lane);
            float sx, sq;
            f32x16 acc[3];
#pragma unroll
            for (int t = 0; t < 3; ++t) acc[t] = (f32x16){};
            const unsigned gneed = lane == 0 ? 8u * (layer + 1) : (lane == 1 + (wid >> 2) ? 4u * (4 * layer) : (lane < 3 && layer ? 4u * (4 * layer - 1) : 0u));
            const int goff = lane == 0 ? C_ATT : C_XRD + ((lane - 1) & 1) * 4;
            int wa[3];
            if (layer == 0) {
                bf16x8 b[16]; f32x4 pr[4]; f32x16 acs;
                const unsigned epv = __builtin_amdgcn_raw_buffer_load_b32(r, 0, WS_EPOCH, SC1);
                u32x4 xr[16];
                const unsigned vo = (lane >> 4) * (D * 2) + (lane & 15) * 16, so = opq((tb1 * 32 * D + 256 * kh) * 2);
#pragma unroll
                for (int i = 0; i < 16; ++i) xr[i] = ldw(rx, vo + (i & 7) * 4 * (D * 2) + (i >> 3) * 256, so);
                ep = rfl(epv) + 1u;
                lds_signal(lds0 + C_ATT, lane);
                STAMPV(1, 22);
                LAS unsigned char* xt = L + LD_XA + wid * 8192;
#pragma unroll
                for (int hf = 0; hf < 2; ++hf) {
#pragma unroll
                    for (int j = 0; j < 8; ++j) { const int row = 4 * j + (lane >> 4); *(LAS u32x4*)(xt + row * 256 + (((lane & 15) ^ (row & 15)) * 16)) = xr[8 * hf + j]; }
#pragma unroll
                    for (int s = 0; s < 8; ++s) b[8 * hf + s] = *(LAS const bf16x8*)(xt + (lane & 31) * 256 + (((2 * s + (lane >> 5)) ^ (lane & 15)) * 16));
                }
                STAMPV(layer, 1);
                lds_wait_vec(L, goff, gneed, r, lane);
                float a1[4] = {0.f, 0.f, 0.f, 0.f}, a2[4] = {0.f, 0.f, 0.f, 0.f};
#pragma unroll
                for (int s = 0; s < 16; ++s) { const u32x4 u = __builtin_bit_cast(u32x4, b[s]);
#pragma unroll
                    for (int e = 0; e < 4; ++e) { const float lo = bflo(u[e]), hi = bfhi(u[e]); a1[e] += lo + hi; a2[e] = __builtin_fmaf(lo, lo, __builtin_fmaf(hi, hi, a2[e])); } }
                const float s1 = xsum32((a1[0] + a1[1]) + (a1[2] + a1[3])), s2 = xsum32((a2[0] + a2[1]) + (a2[2] + a2[3]));
                sx = s1 * (1.f / 256.f); sq = __builtin_fmaxf(s2 - s1 * sx, 0.f);
                asm volatile("" : "+v"(sx), "+v"(sq));
                STAMPV(1, 23);
#pragma unroll
                for (int t = 0; t < 3; ++t) wa[t] = kh * 6144 + (8 * (lane >> 5) + ((lane & 15) >> 2)) * 192 + t * 64 + ((lane >> 4) & 1) * 32 + (lane & 3) * 8;
                STAMPW(layer, 24, gemm_stage<0, 8, 3, 2, 192, 16, 0, true, true, false>(wid, lds, acc, b, wa, 4 * 192, acs, wid >> 1, pr, sx, sq, L, lds0, layer, r, 0, lane));
                lane = opqv(lane);
                if ((wid >> 1) < 3) { cs_store(acs, (LAS float*)(L + LD_CSQ) + 96 * kh + 32 * (wid >> 1), lane); lds_signal(lds0 + C_CS, lane); }
            } else {
                bf16x8 b[16]; f32x4 pr[4]; f32x16 acs;
                wait_flags(r, WS_FLAGS + FL_X2 + tb1 * 32 * WSTR, 0xFFFFu << (16 * kh), lane, ep);
                const unsigned so = opq(WS_LAYER + L_X2F + (tb1 * 32 + 16 * kh) * KB);
#pragma unroll
                for (int s = 0; s < 16; ++s) { b[s] = ldf(r, lane * 16, so + s * KB); if (s == 3) stats_issue(r, WS_LAYER + L_ST2, tb1, kh, lane, pr); }
                STAMPV(layer, 1);
                lds_wait_vec(L, goff, gneed, r, lane);
#pragma unroll
                for (int t = 0; t < 3; ++t) wa[t] = kh * 6144 + (4 * (lane >> 5) + ((lane & 15) >> 2)) * 192 + t * 64 + ((lane >> 4) & 1) * 32 + (lane & 3) * 8;
                STAMPW(layer, 24, gemm_stage<0, 8, 3, 2, 192, 16, 0, true, false, true>(wid, lds, acc, b, wa, 8 * 192, acs, -1, pr, sx, sq, L, lds0, layer, r, 0, lane));
            }
            STAMPV(layer, 2);
            lane = opqv(lane);
            if (lane < 32) ((LAS f32x2*)(L + LD_ST))[wid * 32 + lane] = (f32x2){sx, sq};
            const int blk = 4 * team + (wid >> 1), kb = 8 + (wid >> 1);
            const unsigned kvo = wl + L_KV + (blk * 16 + mem) * 4 * KB;
            if (kh == 0) { asm volatile("; kh 0"); qkv_epi<0>(acc, sx, sq, L, lds0, wid, lane, layer, kb, r, kvo); asm volatile("; kh 0 end"); }
            else { asm volatile("; kh 1"); qkv_epi<1>(acc, sx, sq, L, lds0, wid, lane, layer, kb, r, kvo); asm volatile("; kh 1 end"); }
            lds_signal(lds0 + C_LOCAL + (wid >> 1) * 4, lane);
            STAMPV(layer, 3);
            publish(r, fl + FL_KV + ((blk * 16 + mem) * 2 + kh) * WSTR, lane, ep);
            STAGE_BAR(1);
            STAMPV(layer, 4);
        }
        {
            lane = opqv(lane);
            const int gb = 4 * team - 8 + wid;
            if (gb >= 0) {
                wait_flags(r, fl + FL_KV + (gb * 16 + mem) * 2 * WSTR, 3u, lane, ep);
                STAMPV(layer, 5);
                u32x4 kv[4];
                const unsigned so = opq(wl + L_KV + (gb * 16 + mem) * 4 * KB);
#pragma unroll
                for (int i = 0; i < 4; ++i) kv[i] = ld16(r, lane * 16, so + i * KB);
                lds_wait_vec(L, C_XRD + (lane & 1) * 4, 4u * (4 * layer + 1), r, lane);
#pragma unroll
                for (int i = 0; i < 2; ++i) {
                    *(LAS u32x4*)(L + LD_K + (wid * 2 + i) * 1024 + lane * 16) = kv[i];
                    *(LAS u32x4*)(L + LD_V + (wid * 2 + i) * 1024 + lane * 16) = kv[2 + i];
                }
            }
            lds_signal(lds0 + C_HALO + wid * 4, lane);
            const int qb = wid >> 1, hd = wid & 1;
            STAMPV(layer, 6);
            {
                const bool isl = lane < 4, ish = lane >= 8 && lane < 16;
                const unsigned need = isl ? (lane <= qb ? 2u * (layer + 1) : 0u) : (ish && (lane - 8) >= qb ? (unsigned)(layer + 1) : 0u);
                lds_wait_vec(L, C_LOCAL + (lane & 15) * 4, need, r, lane);
            }
            STAMPV(layer, 7);
            f32x16 o;
            if (team >= 2) attn_unit<true>(L, team, qb, hd, lane, o); else attn_unit<false>(L, team, qb, hd, lane, o);
            STAMPV(layer, 8);
            lane = opqv(lane);
            lds_add1(lds0 + C_ATT, lane);
            const float rl = __builtin_amdgcn_rcpf(hd ? o[0] : o[8]);
            float v[8];
#pragma unroll
            for (int e = 0; e < 8; ++e) v[e] = (hd ? o[8 + e] : o[e]) * rl;
            st16(r, lane * 16, wl + L_OF + ((4 * team + qb) * 32 + 2 * mem + hd) * KB, pack8(v));
            publish(r, fl + FL_O + ((4 * team + qb) * 32 + 2 * mem + hd) * WSTR, lane, ep);
            STAGE_BAR(2);
            STAMPV(layer, 9);
        }
        {
            lane = opqv(lane);
            wait_flags(r, fl + FL_O + tb2 * 32 * WSTR, 0xFFu << (8 * kq), lane, ep);
            STAMPV(layer, 10);
            f32x16 acc[2], acs; bf16x8 b[9]; f32x4 pr[4]; float sx, sq;
            acc[0] = (f32x16){}; acc[1] = (f32x16){};
            int wa[2]; n64_addr(wa, kq, lane);
            u32x4 res;
            if (layer == 0) { const unsigned vo = (lane & 31) * (D * 2) + (lane >> 5) * 8, so = (tb2 * 32 * D + 64 * cb + 16 * kq) * 2;
                const u32x2 a = __builtin_amdgcn_raw_buffer_load_b64(rx, vo, so, 0), c = __builtin_amdgcn_raw_buffer_load_b64(rx, vo + 16, so, 0); res = (u32x4){a.x, a.y, c.x, c.y}; }
            else res = ld16(r, lane * 16, WS_LAYER + L_X2F + (tb2 * 32 + 4 * cb + kq) * KB);
            STAMPW(layer, 25, gemm_stage<8, 4, 2, 99, 128, 9, 0, false, false, false>(wid, lds, acc, b, wa, 8 * 128, acs, -1, pr, sx, sq, L, lds0, layer, r, opq(wl + L_OF + (tb2 * 32 + 8 * kq) * KB), lane));
            STAMPV(layer, 11);
            lane = opqv(lane);
            lds_wait_vec(L, (lane & 1) ? C_ATT : C_XRD + (wid >> 2) * 4, (lane & 1) ? 8u * (layer + 2) : 4u * (4 * layer + 1), r, lane);
            float v[8]; kq_reduce(acc, L, lds0, wid, lane, 4 * layer + 2, r, v);
            resid_add(v, res);
            st16(r, lane * 16, wl + L_X1F + (tb2 * 32 + 4 * cb + kq) * KB, res);
            stats_out(res, r, wl + L_ST1, tb2, 4 * cb + kq, lane);
            STAMPV(layer, 12);
            publish(r, fl + FL_X1 + (tb2 * 32 + 4 * cb + kq) * WSTR, lane, ep);
            STAGE_BAR(3);
            STAMPV(layer, 13);
        }
        {
            lane = opqv(lane);
            wait_flags(r, fl + FL_X1 + tb1 * 32 * WSTR, 0xFFFFu << (16 * kh), lane, ep);
            STAMPV(layer, 14);
            f32x16 acc[4];
#pragma unroll
            for (int t = 0; t < 4; ++t) acc[t] = (f32x16){};
            int wa[4];
            {
                const int q4 = (lane & 15) >> 2;
#pragma unroll
                for (int t = 0; t < 4; ++t) wa[t] = kh * 8192 + (4 * (lane >> 5) + q4) * 256 + (((t ^ (2 * kh)) ^ q4) * 64) + ((lane >> 4) & 1) * 32 + (lane & 3) * 8;
            }
            const unsigned so = opq(wl + L_X1F + (tb1 * 32 + 16 * kh) * KB);
            float sx, sq;
            if (layer == 0) {
                f32x4 pr[4]; f32x16 acs; stats_issue(r, wl + L_ST1, tb1, kh, lane, pr);
                bf16x8 b[4];
                STAMPW(layer, 26, gemm_stage<12, 8, 4, 99, 256, 4, 0, false, true, true>(wid, lds, acc, b, wa, 8 * 256, acs, wid >> 1, pr, sx, sq, L, lds0, layer, r, so, lane));
                lane = opqv(lane);
                cs_store(acs, (LAS float*)(L + LD_CS1) + 128 * kh + 32 * ((wid >> 1) ^ (2 * kh)), lane);
                lds_signal(lds0 + C_CS, lane);
            } else {
                f32x4 pr[4]; f32x16 acs; stats_issue(r, wl + L_ST1, tb1, kh, lane, pr);
                bf16x8 b[RB_F1];
                STAMPW(layer, 26, gemm_stage<12, 8, 4, 99, 256, RB_F1, 0, false, false, true>(wid, lds, acc, b, wa, 8 * 256, acs, -1, pr, sx, sq, L, lds0, layer, r, so, lane));
            }
            STAMPV(layer, 15);
            lane = opqv(lane); const int t32 = lane & 31, h = lane >> 5;
            lds_wait_ge(L, C_XRD + (wid >> 2) * 4, 4u * (4 * layer + 2), r, lane);
            xch_put(L, wid, 0, lane, acc[2], 0); xch_put(L, wid, 1, lane, acc[2], 1); xch_put(L, wid, 2, lane, acc[3], 0); xch_put(L, wid, 3, lane, acc[3], 1);
            if (h == 0) ((LAS f32x2*)(L + LD_ST))[wid * 32 + t32] = (f32x2){sx, sq};
            lds_signal(lds0 + C_XF + wid * 4, lane);
            if (layer == 0) lds_wait_ge(L, C_CS, 14u, r, lane);
            LAS const float* csl = (LAS const float*)(L + LD_CS1) + 64 * kh;
            f32x4 cs[8];
#pragma unroll
            for (int i = 0; i < 8; ++i) cs[i] = *(LAS const f32x4*)(csl + 8 * i + 4 * h) + *(LAS const f32x4*)(csl + 128 + 8 * i + 4 * h);
            lds_wait_ge(L, C_XF + (wid ^ 1) * 4, 4u * layer + 3, r, lane);
            const f32x2 os = ((LAS const f32x2*)(L + LD_ST))[(wid ^ 1) * 32 + t32];
            xch_add(L, wid ^ 1, 0, lane, acc[0], 0); xch_add(L, wid ^ 1, 1, lane, acc[0], 1); xch_add(L, wid ^ 1, 2, lane, acc[1], 0); xch_add(L, wid ^ 1, 3, lane, acc[1], 1);
            lds_signal(lds0 + C_XRD + (wid >> 2) * 4, lane);
            float la, lb; ln_coef(os, sx, sq, la, lb);
            if (mem < 14) {
#pragma unroll
                for (int i = 0; i < 4; ++i) {
                    float hv[8], ex[8];
#pragma unroll
                    for (int e = 0; e < 8; ++e) { hv[e] = la * acc[i >> 1][8 * (i & 1) + e] + lb * cs[2 * i + (e >> 2)][e & 3]; ex[e] = -LOG2E * hv[e]; }
                    SB();
#pragma unroll
                    for (int e = 0; e < 8; ++e) ex[e] = __builtin_amdgcn_exp2f(ex[e]);
                    SB();
#pragma unroll
                    for (int e = 0; e < 8; ++e) ex[e] += 1.f;
                    SB();
#pragma unroll
                    for (int e = 0; e < 8; ++e) ex[e] = __builtin_amdgcn_rcpf(ex[e]);
                    SB();
#pragma unroll
                    for (int e = 0; e < 8; ++e) hv[e] *= ex[e];
                    st16(r, lane * 16, wl + L_HF + (tb1 * 112 + 8 * mem + 4 * kh + i) * KB, pack8(hv));
                    SB();
                }
                STAMPV(layer, 16);
                publish(r, fl + FL_H + (tb1 * 32 + 2 * mem + kh) * WSTR, lane, ep);
            }
            STAGE_BAR(4);
            STAMPV(layer, 17);
        }
        {
            lane = opqv(lane);
            wait_flags(r, fl + FL_H + tb2 * 32 * WSTR, (0x5555555u << (kq >> 1)) & 0xFFFFFFFu, lane, ep);
            STAMPV(layer, 18);
            f32x16 acc[2], acs; bf16x8 b[RB_F2]; f32x4 pr[4]; float sx, sq;
            acc[0] = (f32x16){}; acc[1] = (f32x16){};
            int wa[2]; n64_addr(wa, kq, lane);
            STAMPW(layer, 27, gemm_stage<20, 14, 2, 99, 128, RB_F2, 1, false, false, false>(wid, lds, acc, b, wa, 8 * 128, acs, -1, pr, sx, sq, L, lds0, layer, r, opq(wl + L_HF + (tb2 * 112 + 2 * kq) * KB), lane));
            STAMPV(layer, 19);
            lane = opqv(lane);
            u32x4 res = ld16(r, lane * 16, wl + L_X1F + (tb2 * 32 + 4 * cb + kq) * KB);
            lds_wait_ge(L, C_XRD + (wid >> 2) * 4, 4u * (4 * layer + 3), r, lane);
            float v[8]; kq_reduce(acc, L, lds0, wid, lane, 4 * layer + 4, r, v);
            resid_add(v, res);
            if (layer == 0) {
                st16(r, lane * 16, wl + L_X2F + (tb2 * 32 + 4 * cb + kq) * KB, res);
                stats_out(res, r, wl + L_ST2, tb2, 4 * cb + kq, lane);
                STAMPV(layer, 20);
                publish(r, fl + FL_X2 + (tb2 * 32 + 4 * cb + kq) * WSTR, lane, ep);
                STAGE_BAR(5);
            } else {
                auto s0 = __builtin_amdgcn_permlane32_swap(res.x, res.z, false, false), s1 = __builtin_amdgcn_permlane32_swap(res.y, res.w, false, false);
                unsigned short* dst = p.out + (size_t)(32 * tb2 + (lane & 31)) * D + 64 * cb + 16 * kq + 8 * (lane >> 5);
                *(u32x4*)dst = (u32x4){s0[0], s1[0], s0[1], s1[1]};
            }
            STAMPV(layer, 21);
        }
    }
    STAMP_RT(1); STAMP_DUMP();
}

extern "C" void kernel_launch(void* const* d_in, const int* in_sizes, int n_in, void* d_out, int out_size, void* d_ws, size_t ws_size, hipStream_t stream) {
    Params p;
    p.x = (const unsigned short*)d_in[0]; p.wqkv = (const unsigned short*)d_in[1]; p.wo = (const unsigned short*)d_in[2];
    p.w1 = (const unsigned short*)d_in[4]; p.w2 = (const unsigned short*)d_in[5];
    p.out = (unsigned short*)d_out; p.ws = (unsigned char*)d_ws;
    (void)hipFuncSetAttribute((const void*)k_main, hipFuncAttributeMaxDynamicSharedMemorySize, LDS_BYTES);
    k_main<<<256, NTHR, LDS_BYTES, stream>>>(p);
}
```

```cpp
#include <hip/hip_runtime.h>
#include <hip/hip_bf16.h>
#include <stdint.h>

typedef short bf16x8 __attribute__((ext_vector_type(8)));
typedef float f32x2 __attribute__((ext_vector_type(2)));
typedef float f32x4 __attribute__((ext_vector_type(4)));
typedef float f32x8 __attribute__((ext_vector_type(8)));
typedef float f32x16 __attribute__((ext_vector_type(16)));
typedef unsigned u32x2 __attribute__((ext_vector_type(2)));
typedef unsigned u32x4 __attribute__((ext_vector_type(4)));
typedef __bf16 bf16x2_t __attribute__((ext_vector_type(2)));
#define GAS __attribute__((address_space(1)))
#define LAS __attribute__((address_space(3)))
#define DEV __device__ __forceinline__
#define SB() __builtin_amdgcn_sched_barrier(0)
typedef __amdgpu_buffer_rsrc_t rsrc_t;

constexpr int S = 2048, D = 512, H = 32, F = 1792;
constexpr float EPS = 1e-5f;
constexpr float LOG2E = 1.4426950408889634f;
constexpr float QSCALE = 0.25f * LOG2E;

constexpr unsigned KB = 1024, MB = 1024 * 1024;
constexpr unsigned WS_TMO   = 0;
constexpr unsigned WS_EPOCH = 256;
constexpr unsigned WS_START = 4096;
constexpr unsigned WS_LAYER = 64 * KB;
constexpr unsigned L_KV  = 0;
constexpr unsigned L_OF  = L_KV + 4 * MB;
constexpr unsigned L_X1F = L_OF + 2 * MB;
constexpr unsigned L_HF  = L_X1F + 2 * MB;
constexpr unsigned L_X2F = L_HF + 7 * MB;
constexpr unsigned L_ST1 = L_X2F + 2 * MB;
constexpr unsigned L_ST2 = L_ST1 + 512 * KB;
constexpr unsigned L_SIZE = L_ST2 + 512 * KB;
constexpr unsigned WSTR = 128;
constexpr unsigned WS_FLAGS = 64 * MB;
constexpr unsigned FL_KV = 0, FL_O = FL_KV + 64 * 16 * 2 * WSTR, FL_X1 = FL_O + 64 * 32 * WSTR, FL_H = FL_X1 + 64 * 32 * WSTR, FL_X2 = FL_H + 64 * 32 * WSTR, FL_LAYER = FL_X2 + 64 * 32 * WSTR;
constexpr unsigned WS_END = 72 * MB;
static_assert(WS_LAYER + 2 * L_SIZE <= WS_FLAGS && WS_FLAGS + 2 * FL_LAYER <= WS_END, "workspace map");

constexpr int NCW = 8, NLW = 4, NTHR = 64 * (NCW + NLW);
constexpr int SLOT = 16384, NSLOT = 5;
constexpr int LD_RING = 0;
constexpr int LD_XA = 80 * 1024;
constexpr int LD_K = 96 * 1024;
constexpr int LD_V = 120 * 1024;
constexpr int LD_XB = LD_V;
constexpr int LD_XC0 = LD_K + 16 * 1024, LD_XC1 = LD_V + 16 * 1024;
constexpr int LD_Q = 144 * 1024;
constexpr int LD_MISC = 152 * 1024;
constexpr int LD_CSQ = LD_MISC  , LD_CS1 = LD_MISC + 768  , LD_ONES = LD_MISC + 1792, LD_CTR = LD_MISC + 1856, LD_DUMMY = LD_CTR + 256  ,
              LD_ST = LD_DUMMY + 512  , LD_AB = LD_ST + 2048  , LD_STAMP = LD_AB + 1024;
constexpr int C_FULL = LD_CTR, C_FREE = LD_CTR + 32, C_LOCAL = LD_CTR + 64, C_HALO = LD_CTR + 96, C_XF = LD_CTR + 128, C_XRD = LD_CTR + 160, C_ATT = LD_CTR + 176, C_BAR = LD_CTR + 180, C_CS = LD_CTR + 184;
constexpr int LDS_BYTES = 160 * 1024;
static_assert(LD_STAMP + 8 * 64 * 4 <= LDS_BYTES && LD_CTR % 16 == 0 && LD_ST % 16 == 0, "LDS map");
constexpr int slot_of(int c) { return c % NSLOT; }
constexpr int uses_of(int slot) { return slot == 4 ? 6 : 7; }

struct Params {
    const unsigned short* x; const unsigned short* wqkv; const unsigned short* wo; const unsigned short* w1; const unsigned short* w2;
    unsigned short* out; unsigned char* ws;
};

#define STAMPV(kid, i)
#define STAMPW(kid, i, ...) (void)(__VA_ARGS__)
#define STAMP_RT(i)
#define STAMP_DUMP()
#define STAMP_INIT()

DEV float bf2f(unsigned short b) { return __uint_as_float((unsigned)b << 16); }
DEV float bflo(unsigned u) { return __uint_as_float(u << 16); }
DEV float bfhi(unsigned u) { return __uint_as_float(u & 0xffff0000u); }
DEV unsigned pk2(float lo, float hi) { f32x2 v = {lo, hi}; bf16x2_t r = __builtin_convertvector(v, bf16x2_t); return __builtin_bit_cast(unsigned, r); }
DEV float xsum32(float v) { auto r = __builtin_amdgcn_permlane32_swap(__float_as_uint(v), __float_as_uint(v), false, false); return __uint_as_float(r[0]) + __uint_as_float(r[1]); }
DEV float xmax32(float v) { auto r = __builtin_amdgcn_permlane32_swap(__float_as_uint(v), __float_as_uint(v), false, false); return __builtin_fmaxf(__uint_as_float(r[0]), __uint_as_float(r[1])); }
DEV f32x16 mfma32(bf16x8 a, bf16x8 b, f32x16 c) { return __builtin_amdgcn_mfma_f32_32x32x16_bf16(a, b, c, 0, 0, 0); }
DEV float dot2(unsigned a, unsigned b, float c) { return __builtin_amdgcn_fdot2_f32_bf16(__builtin_bit_cast(bf16x2_t, a), __builtin_bit_cast(bf16x2_t, b), c, false); }
DEV unsigned opq(unsigned x) { asm volatile("" : "+s"(x)); return x; }
DEV int opqv(int x) { asm volatile("" : "+v"(x)); return x; }
DEV unsigned rfl(unsigned v) { return __builtin_amdgcn_readfirstlane(v); }
#define WAITV(n) asm volatile("s_waitcnt vmcnt(" #n ")" ::: "memory")
constexpr int SC1 = 16;
DEV bf16x8 ldf(rsrc_t r, unsigned vo, unsigned so) { return __builtin_bit_cast(bf16x8, __builtin_amdgcn_raw_buffer_load_b128(r, vo, so, SC1)); }
DEV u32x4 ld16(rsrc_t r, unsigned vo, unsigned so) { return __builtin_amdgcn_raw_buffer_load_b128(r, vo, so, SC1); }
DEV u32x4 ldw(rsrc_t r, unsigned vo, unsigned so) { return __builtin_amdgcn_raw_buffer_load_b128(r, vo, so, 0); }
DEV void st16(rsrc_t r, unsigned vo, unsigned so, u32x4 v) { __builtin_amdgcn_raw_buffer_store_b128(v, r, vo, so, SC1); asm volatile("s_nop 1" :: "v"(v) : "memory"); }

DEV void block_map(int b, int& team, int& mem) {
    const int x = b & 7, r = b >> 3;
    team = 4 * (x >> 1) + (r >> 3); mem = 8 * (x & 1) + (r & 7);
}

#define GATE_O 3
#define GATE_F 18
#define GATE_Q 29
#define PACE 0
#define PACE_O 3
#define PACE_F 2
#define PACE_Q 1
#define RB_Q 8
#define RB_F1 6
#define RB_F2 12
#define PRIME_PAUSE 20
#define POLL_SLEEP 1
constexpr unsigned SPIN_CAP = 1u << 15;
DEV void tmo_set(rsrc_t r, int lane) { if (lane == 0) __builtin_amdgcn_raw_buffer_store_b32(1u, r, 0, WS_TMO, SC1); }
DEV void wait_flags(rsrc_t r, unsigned off, unsigned mask, int lane, unsigned ep) {
    bool need = lane < 32 && ((mask >> (lane & 31)) & 1u);
#pragma unroll 1
    for (unsigned spins = 0;; ++spins) {
        if (need) need = __builtin_amdgcn_raw_buffer_load_b32(r, lane * WSTR, off, SC1) != ep;
        if (!__any(need)) break;
        if (spins > SPIN_CAP) { tmo_set(r, lane); break; }
        __builtin_amdgcn_s_sleep(POLL_SLEEP);
    }
    __builtin_amdgcn_fence(__ATOMIC_ACQUIRE, "wavefront");
}
DEV void publish(rsrc_t r, unsigned word_off, int lane, unsigned ep) {
    WAITV(0);
    if (lane == 0) __builtin_amdgcn_raw_buffer_store_b32(ep, r, 0, word_off, SC1);
}
DEV unsigned lds_ld(LAS unsigned char* L, int off) { return *(volatile LAS unsigned*)(L + off); }
DEV int s2v(int x) { int v; asm volatile("v_mov_b32 %0, %1" : "=v"(v) : "s"(x)); return v; }
DEV unsigned lds_ldu(LAS unsigned char* L, int off) { return *(volatile LAS unsigned*)(L + s2v(off)); }
DEV void lds_add1(unsigned addr, int lane) {
    const unsigned a = lane == 0 ? addr : (unsigned)(LD_DUMMY + lane * 4);
    asm volatile("ds_add_u32 %0, %1" :: "v"(a), "v"(1u) : "memory");
}
DEV void lds_signal(unsigned addr, int lane) { lds_add1(addr, lane); }
DEV void lds_wait_ge(LAS unsigned char* L, int off, unsigned target, rsrc_t r, int lane) {
#pragma unroll 1
    for (unsigned spins = 0; rfl(lds_ldu(L, off)) < target; ++spins) {
        if (spins > (SPIN_CAP << 3)) break;
        if (spins > 2) __builtin_amdgcn_s_sleep(1);
    }
    asm volatile("" ::: "memory");
}
DEV void lds_wait_vec(LAS unsigned char* L, int off, unsigned need, rsrc_t r, int lane) {
#pragma unroll 1
    for (unsigned spins = 0; !__all(lds_ld(L, off) >= need); ++spins) {
        if (spins > (SPIN_CAP << 3)) break;
        if (spins > 2) __builtin_amdgcn_s_sleep(1);
    }
    asm volatile("" ::: "memory");
}

template <int C0> DEV int ring_ready(LAS unsigned char* L, int c, int layer, rsrc_t r, int lane) {
    const int ca = C0 + c + (lane & 7), sl = ca % NSLOT;
    const unsigned tgt = 4u * (layer * (sl == 4 ? 6 : 7) + ca / NSLOT + 1);
    unsigned long long ok;
#pragma unroll 1
    for (unsigned spins = 0;; ++spins) {
        ok = __ballot(lds_ld(L, C_FULL + sl * 4) >= tgt);
        if (ok & 1ull) break;
        if (spins > (SPIN_CAP << 3)) { ok = ~0ull; break; }
        if (spins > 2) __builtin_amdgcn_s_sleep(1);
    }
    asm volatile("" ::: "memory");
    const int n = __builtin_ctzll(~ok);
    return c + (n < NSLOT ? n : NSLOT);
}
DEV void stats_issue(rsrc_t r, unsigned st, int tb, int kh, int lane, f32x4 (&pr)[4]) {
    const unsigned so = opq(st + (tb * 16 + 8 * kh) * 512);
#pragma unroll
    for (int i = 0; i < 4; ++i) pr[i] = __builtin_bit_cast(f32x4, ld16(r, ((lane >> 5) * 4 * 32 + (lane & 31)) * 16 + i * 512, so));
}
DEV void stats_half(const f32x4 (&pr)[4], float& mean, float& m2) {
    float ms = 0.f;
#pragma unroll
    for (int i = 0; i < 4; ++i) ms += pr[i][0] + pr[i][2];
    mean = xsum32(ms) * (1.f / 16.f);
    float q = 0.f;
#pragma unroll
    for (int i = 0; i < 4; ++i) { const float d0 = pr[i][0] - mean, d1 = pr[i][2] - mean; q += pr[i][1] + pr[i][3] + 16.f * (d0 * d0 + d1 * d1); }
    m2 = xsum32(q);
}
DEV void stats_out(u32x4 o, rsrc_t r, unsigned st, int tb, int s, int lane) {
    const float q8[8] = {bflo(o.x), bfhi(o.x), bflo(o.y), bfhi(o.y), bflo(o.z), bfhi(o.z), bflo(o.w), bfhi(o.w)};
    const float mean = xsum32(((q8[0] + q8[1]) + (q8[2] + q8[3])) + ((q8[4] + q8[5]) + (q8[6] + q8[7]))) * (1.f / 16.f);
    float q = 0.f;
#pragma unroll
    for (int e = 0; e < 8; ++e) { const float d = q8[e] - mean; q += d * d; }
    q = xsum32(q);
    if (lane < 32) __builtin_amdgcn_raw_buffer_store_b64((u32x2){__float_as_uint(mean), __float_as_uint(q)}, r, lane * 16 + (s & 1) * 8, st + (tb * 16 + (s >> 1)) * 512, SC1);
}

typedef short v4i16 __attribute__((ext_vector_type(4)));
DEV v4i16 trd(LAS const unsigned char* p) { return __builtin_amdgcn_ds_read_tr16_b64_v4i16((LAS v4i16*)p); }
template <int C0, int NCH, int NT, int SWAPV, int P, int RB, int AMODE, bool PRE, bool CS, bool ST>
DEV unsigned gemm_stage(int wid, unsigned char* lds, f32x16 (&acc)[NT], bf16x8 (&b)[RB], const int (&wa)[NT], int tau, f32x16& acs, int cst, const f32x4 (&pr)[4], float& sx, float& sq, LAS unsigned char* L, unsigned lds0, int layer, rsrc_t r, unsigned so, int lane) {
    unsigned twait = 0;
    constexpr int NS = 2 * NCH, JS = 16 * P;
    const unsigned l16 = lane * 16;
#define AOFF(s_) (AMODE == 0 ? (s_) * KB : (((s_) >> 1) * 8 + ((s_) & 1)) * KB)
    if (!PRE) {
#pragma unroll
        for (int s = 0; s < RB - 1; ++s) if (s < NS) b[s] = ldf(r, l16, so + AOFF(s));
    }
    LAS const unsigned char* w0[NT]; LAS const unsigned char* w1[NT];
#pragma unroll
    for (int t = 0; t < NT; ++t) { w0[t] = L + LD_RING + wa[t]; w1[t] = L + opqv(LD_RING + 4 * SLOT + wa[t]); }
    const unsigned fadr = lane == 0 ? lds0 + C_FREE : (unsigned)(LD_DUMMY + lane * 4);
#define WADDR(s_, t_) ((slot_of(C0 + ((s_) >> 1)) == 4 ? w1[t_] : w0[t_] + slot_of(C0 + ((s_) >> 1)) * SLOT) + ((s_) & 1) * JS)
#define WFRAG(s_, t_) __builtin_shufflevector(trd(WADDR(s_, t_)), trd(WADDR(s_, t_) + tau), 0, 1, 2, 3, 4, 5, 6, 7)
    const unsigned one2 = (unsigned)s2v(0x3f803f80);
    const bf16x8 ones = __builtin_bit_cast(bf16x8, (u32x4){one2, one2, one2, one2});
    int ready = ring_ready<C0>(L, 0, layer, r, lane);
    bf16x8 fa[NT];
#pragma unroll
    for (int t = 0; t < NT; ++t) fa[t] = WFRAG(0, t);
    SB();
#pragma unroll
    for (int s = 0; s < NS; ++s) {
        if (!PRE && s + RB - 1 < NS) b[(s + RB - 1) % RB] = ldf(r, l16, so + AOFF(s + RB - 1));
        const int sp = s + 1;
        if (sp < NS && (sp & 1) == 0) { if ((sp >> 1) >= ready) {
            ready = ring_ready<C0>(L, sp >> 1, layer, r, lane);
        } }
        if (CS) {
            u32x4 f = __builtin_bit_cast(u32x4, fa[0]);
#pragma unroll
            for (int t = 1; t < NT; ++t) { const u32x4 g = __builtin_bit_cast(u32x4, fa[t]);
#pragma unroll
                for (int e = 0; e < 4; ++e) f[e] = cst == t ? g[e] : f[e]; }
            if (s == 0) acs = mfma32(__builtin_bit_cast(bf16x8, f), ones, (f32x16){}); else acs = mfma32(__builtin_bit_cast(bf16x8, f), ones, acs);
        }
        SB();
#pragma unroll
        for (int t = 0; t < NT; ++t) {
            if (t >= SWAPV) acc[t] = mfma32(b[s % RB], fa[t], acc[t]); else acc[t] = mfma32(fa[t], b[s % RB], acc[t]);
            if (sp < NS) fa[t] = WFRAG(sp, t);
            SB();
        }
        if (s & 1) asm volatile("ds_add_u32 %0, %1 offset:%2" :: "v"(fadr), "v"(1u), "n"(slot_of(C0 + (s >> 1)) * 4) : "memory");
        if (ST && s == 3) { stats_half(pr, sx, sq); asm volatile("" : "+v"(sx), "+v"(sq)); }
        if (s == 0) STAMPV(layer, C0 == 0 ? 28 : C0 == 8 ? 29 : C0 == 12 ? 30 : 31);
    }
#undef AOFF
#undef WADDR
#undef WFRAG
    return twait;
}
DEV int xch_off(int w, int slot) { return slot < 2 ? LD_XA + w * 4096 + slot * 2048 : slot == 2 ? LD_XB + w * 2048 : (w < 4 ? LD_XC0 + w * 2048 : LD_XC1 + (w - 4) * 2048); }
DEV void xch_put(LAS unsigned char* L, int w, int slot, int lane, const f32x16& a, int half) {
    LAS f32x4* d = (LAS f32x4*)(L + xch_off(w, slot)) + lane;
    d[0] = (f32x4){a[8 * half], a[8 * half + 1], a[8 * half + 2], a[8 * half + 3]}; d[64] = (f32x4){a[8 * half + 4], a[8 * half + 5], a[8 * half + 6], a[8 * half + 7]};
}
DEV void xch_add(LAS unsigned char* L, int w, int slot, int lane, f32x16& a, int half) {
    LAS const f32x4* d = (LAS const f32x4*)(L + xch_off(w, slot)) + lane;
    const f32x4 u = d[0], v = d[64];
#pragma unroll
    for (int e = 0; e < 4; ++e) { a[8 * half + e] += u[e]; a[8 * half + 4 + e] += v[e]; }
}
DEV void ln_coef(f32x2 o, float mean_h, float m2_h, float& la, float& lb) {
    const float mean = 0.5f * (mean_h + o.x), d = mean_h - o.x;
    const float var = (m2_h + o.y + 128.f * d * d) * (1.f / 512.f);
    la = __builtin_amdgcn_rsqf(var + EPS); lb = -mean * la;
}
DEV void kq_reduce(f32x16 (&acc)[2], LAS unsigned char* L, unsigned lds0, int wid, int lane, unsigned seq, rsrc_t r, float (&v)[8]) {
    const int base = wid & 4;
    xch_put(L, wid, 0, lane, acc[0], 1); xch_put(L, wid, 1, lane, acc[1], 0); xch_put(L, wid, 2, lane, acc[1], 1);
    lds_signal(lds0 + C_XF + wid * 4, lane);
    lds_wait_vec(L, C_XF + (base + (lane & 3)) * 4, seq, r, lane);
#pragma unroll
    for (int x = 1; x < 4; ++x) xch_add(L, wid ^ x, x - 1, lane, acc[0], 0);
    lds_signal(lds0 + C_XRD + (wid >> 2) * 4, lane);
#pragma unroll
    for (int e = 0; e < 8; ++e) v[e] = acc[0][e];
}
DEV u32x4 pack8(const float (&f)[8]) { return (u32x4){pk2(f[0], f[1]), pk2(f[2], f[3]), pk2(f[4], f[5]), pk2(f[6], f[7])}; }
DEV void resid_add(const float (&v)[8], u32x4& res) {
    const float f[8] = {bflo(res.x) + v[0], bfhi(res.x) + v[1], bflo(res.y) + v[2], bfhi(res.y) + v[3], bflo(res.z) + v[4], bfhi(res.z) + v[5], bflo(res.w) + v[6], bfhi(res.w) + v[7]};
    res = pack8(f);
}

template <int KH>
DEV void qkv_epi(f32x16 (&acc)[3], float sx, float sq, LAS unsigned char* L, unsigned lds0, int wid, int lane, int layer, int kb, rsrc_t r, unsigned kvo) {
    const int t32 = lane & 31, h = lane >> 5;
    xch_put(L, wid, 0, lane, acc[0], 1 - KH); xch_put(L, wid, 1, lane, acc[1], 1 - KH); xch_put(L, wid, 2, lane, acc[2], 1 - KH);
    lds_signal(lds0 + C_XF + wid * 4, lane);
    if (layer == 0) lds_wait_ge(L, C_CS, 6u, r, lane);
    LAS const float* csl = (LAS const float*)(L + LD_CSQ);
    f32x4 cq[2], ck[2];
#pragma unroll
    for (int u = 0; u < 2; ++u) { cq[u] = *(LAS const f32x4*)(csl + 16 * KH + 8 * u + 4 * h) + *(LAS const f32x4*)(csl + 96 + 16 * KH + 8 * u + 4 * h);
        ck[u] = *(LAS const f32x4*)(csl + 32 + 16 * KH + 8 * u + 4 * h) + *(LAS const f32x4*)(csl + 96 + 32 + 16 * KH + 8 * u + 4 * h); }
    const float cv = csl[64 + t32] + csl[96 + 64 + t32];
    lds_wait_ge(L, C_XF + (wid ^ 1) * 4, 4u * layer + 1, r, lane);
    const f32x2 os = ((LAS const f32x2*)(L + LD_ST))[(wid ^ 1) * 32 + t32];
    xch_add(L, wid ^ 1, 0, lane, acc[0], KH); xch_add(L, wid ^ 1, 1, lane, acc[1], KH); xch_add(L, wid ^ 1, 2, lane, acc[2], KH);
    lds_signal(lds0 + C_XRD + (wid >> 2) * 4, lane);
    float la, lb; ln_coef(os, sx, sq, la, lb);
    LAS f32x2* ab = (LAS f32x2*)(L + LD_AB) + (wid >> 1) * 32;
    if (h == 0) ab[t32] = (f32x2){la, lb};
    f32x4 s01[2], s23[2];
#pragma unroll
    for (int u = 0; u < 2; ++u) { s01[u] = *(LAS const f32x4*)(ab + 16 * KH + 8 * u + 4 * h); s23[u] = *(LAS const f32x4*)(ab + 16 * KH + 8 * u + 4 * h + 2); }
    float v[8];
#pragma unroll
    for (int u = 0; u < 2; ++u)
#pragma unroll
        for (int e = 0; e < 4; ++e) v[4 * u + e] = la * acc[1][8 * KH + 4 * u + e] + lb * ck[u][e];
    const u32x4 ok = pack8(v);
    st16(r, lane * 16, kvo + KH * KB, ok);
    *(LAS u32x4*)(L + LD_K + (kb * 2 + KH) * 1024 + lane * 16) = ok;
#pragma unroll
    for (int u = 0; u < 2; ++u) {
        v[4 * u + 0] = s01[u][0] * acc[2][8 * KH + 4 * u + 0] + s01[u][1] * cv; v[4 * u + 1] = s01[u][2] * acc[2][8 * KH + 4 * u + 1] + s01[u][3] * cv;
        v[4 * u + 2] = s23[u][0] * acc[2][8 * KH + 4 * u + 2] + s23[u][1] * cv; v[4 * u + 3] = s23[u][2] * acc[2][8 * KH + 4 * u + 3] + s23[u][3] * cv; }
    const u32x4 ov = pack8(v);
    st16(r, lane * 16, kvo + (2 + KH) * KB, ov);
    *(LAS u32x4*)(L + LD_V + (kb * 2 + KH) * 1024 + lane * 16) = ov;
    const float qa = la * QSCALE, qb_ = lb * QSCALE;
#pragma unroll
    for (int u = 0; u < 2; ++u)
#pragma unroll
        for (int e = 0; e < 4; ++e) v[4 * u + e] = qa * acc[0][8 * KH + 4 * u + e] + qb_ * cq[u][e];
    *(LAS u32x4*)(L + LD_Q + ((kb - 8) * 2 + KH) * 1024 + lane * 16) = pack8(v);
}
DEV void cs_store(const f32x16& a, LAS float* dst, int lane) {
    if ((lane & 31) == 0) {
#pragma unroll
        for (int u = 0; u < 4; ++u) *(LAS f32x4*)(dst + 8 * u + 4 * (lane >> 5)) = (f32x4){a[4 * u], a[4 * u + 1], a[4 * u + 2], a[4 * u + 3]};
    }
}

#define EXP2(x) __builtin_amdgcn_exp2f(x)
DEV void mask_first(f32x16& s, int q32, int h) {
#pragma unroll
    for (int r = 0; r < 16; ++r) { const int key = (r & 3) + 8 * (r >> 2) + 4 * h; if (key <= q32) s[r] = -INFINITY; }
}
DEV void mask_last(f32x16& s, int q32, int h) {
#pragma unroll
    for (int r = 0; r < 16; ++r) { const int key = (r & 3) + 8 * (r >> 2) + 4 * h; if (key > q32) s[r] = -INFINITY; }
}
DEV float max16(const f32x16& s, float m) {
    float a = __builtin_fmaxf(__builtin_fmaxf(s[0], s[1]), m), b = __builtin_fmaxf(__builtin_fmaxf(s[2], s[3]), s[4]);
    a = __builtin_fmaxf(__builtin_fmaxf(a, s[5]), s[6]); b = __builtin_fmaxf(__builtin_fmaxf(b, s[7]), s[8]);
    a = __builtin_fmaxf(__builtin_fmaxf(a, s[9]), s[10]); b = __builtin_fmaxf(__builtin_fmaxf(b, s[11]), s[12]);
    a = __builtin_fmaxf(__builtin_fmaxf(a, s[13]), s[14]);
    return __builtin_fmaxf(__builtin_fmaxf(a, b), s[15]);
}
template <bool FULL9>
DEV void attn_unit(LAS const unsigned char* L, int team, int qb, int hd, int lane, f32x16& o) {
    const int q32 = lane & 31, h = lane >> 5;
    const bf16x8 qf = *(LAS const bf16x8*)(L + LD_Q + (qb * 2 + hd) * 1024 + lane * 16);
    const int first = FULL9 ? 0 : 8 - 4 * team - qb;
    LAS const unsigned char* kp = L + LD_K + (qb * 2 + hd) * 1024 + lane * 16;
    const bool onel = ((lane >> 4) & 1) != hd;
    const unsigned vstr = onel ? 0u : 2048u;
    LAS const unsigned char* vp = onel ? L + LD_ONES : L + LD_V + qb * 2048 + lane * 16;
    LAS const unsigned char* vq = onel ? L + LD_ONES : L + LD_V + qb * 2048 + 1024 + lane * 16;
    bf16x8 kf[9];
#pragma unroll
    for (int i = 0; i < 9; ++i) if (FULL9 || i >= first) kf[i] = *(LAS const bf16x8*)(kp + i * 2048);
    const f32x16 zero = {};
    float m = -INFINITY;
    if (FULL9) {
        f32x16 sa = mfma32(kf[0], qf, zero), sb;
#pragma unroll
        for (int i = 0; i < 9; ++i) {
            f32x16& cur = (i & 1) ? sb : sa; f32x16& nxt = (i & 1) ? sa : sb;
            if (i + 1 < 9) nxt = mfma32(kf[i + 1], qf, zero);
            SB();
            if (i == 0) mask_first(cur, q32, h);
            if (i == 8) mask_last(cur, q32, h);
            m = max16(cur, m);
            SB();
        }
    } else {
#pragma unroll
        for (int i = 0; i < 9; ++i) if (i >= first) {
            f32x16 s = mfma32(kf[i], qf, zero);
            if (i == 0) mask_first(s, q32, h);
            if (i == 8) mask_last(s, q32, h);
            m = max16(s, m);
        }
    }
    m = xmax32(m);
    f32x16 nm;
#pragma unroll
    for (int r = 0; r < 16; ++r) nm[r] = -m;
    if (FULL9) {
        f32x16 sa = mfma32(kf[0], qf, nm), sb;
        bf16x8 vn0 = *(LAS const bf16x8*)(vp), vn1 = *(LAS const bf16x8*)(vq);
#pragma unroll
        for (int i = 0; i < 9; ++i) {
            f32x16& cur = (i & 1) ? sb : sa; f32x16& nxt = (i & 1) ? sa : sb;
            const bf16x8 v0 = vn0, v1 = vn1;
            if (i + 1 < 9) { vn0 = *(LAS const bf16x8*)(vp + (i + 1) * vstr); vn1 = *(LAS const bf16x8*)(vq + (i + 1) * vstr); }
            if (i + 1 < 9) nxt = mfma32(kf[i + 1], qf, nm);
            SB();
            if (i == 0) mask_first(cur, q32, h);
            if (i == 8) mask_last(cur, q32, h);
            u32x4 p0, p1;
#pragma unroll
            for (int e = 0; e < 4; ++e) p0[e] = pk2(EXP2(cur[2 * e]), EXP2(cur[2 * e + 1]));
            SB();
            if (i == 0) o = mfma32(v0, __builtin_bit_cast(bf16x8, p0), zero); else o = mfma32(v0, __builtin_bit_cast(bf16x8, p0), o);
#pragma unroll
            for (int e = 0; e < 4; ++e) p1[e] = pk2(EXP2(cur[8 + 2 * e]), EXP2(cur[8 + 2 * e + 1]));
            SB();
            o = mfma32(v1, __builtin_bit_cast(bf16x8, p1), o);
            SB();
        }
    } else {
        float z = 0.f; asm volatile("" : "+v"(z));
#pragma unroll
        for (int r = 0; r < 16; ++r) o[r] = z;
#pragma unroll
        for (int i = 0; i < 9; ++i) if (i >= first) {
            f32x16 s = mfma32(kf[i], qf, nm);
            if (i == 0) mask_first(s, q32, h);
            if (i == 8) mask_last(s, q32, h);
            u32x4 p0, p1;
#pragma unroll
            for (int e = 0; e < 4; ++e) { p0[e] = pk2(EXP2(s[2 * e]), EXP2(s[2 * e + 1])); p1[e] = pk2(EXP2(s[8 + 2 * e]), EXP2(s[8 + 2 * e + 1])); }
            const bf16x8 v0 = *(LAS const bf16x8*)(vp + i * vstr), v1 = *(LAS const bf16x8*)(vq + i * vstr);
            o = mfma32(v0, __builtin_bit_cast(bf16x8, p0), o);
            o = mfma32(v1, __builtin_bit_cast(bf16x8, p1), o);
        }
    }
}

struct LoaderCtx { rsrc_t rq, ro, r1, r2; unsigned sq, so_, s1; int lw; };
template <int C, int PC = PACE> DEV void loader_fetch(u32x4 (&buf)[7][4], const LoaderCtx& x, int lane_) {
    constexpr int NI = C < 8 ? 3 : 4;
    const int lane = opqv(lane_), lw = x.lw;
#pragma unroll
    for (int j = 0; j < NI; ++j) {
        if (C < 8) {
            const int i = lw * 192 + j * 64 + lane, row = (i * 683) >> 13, c = i - 12 * row;
            buf[C % 7][j] = ldw(x.rq, ((256 * (row >> 5) + (row & 31)) * 1536 + (c >> 2) * 512 + 8 * (c & 3)) * 2, opq(x.sq + C * 32 * 1536 * 2));
        } else if (C < 12 || C >= 20) {
            const int r8 = lw * 32 + j * 8 + (lane >> 3), c8 = (lane & 7) ^ (((r8 >> 1) & 1) << 2);
            if (C < 12) buf[C % 7][j] = ldw(x.ro, ((128 * (r8 >> 5) + (r8 & 31)) * 512 + 8 * c8) * 2, opq(x.so_ + (C - 8) * 32 * 512 * 2));
            else buf[C % 7][j] = ldw(x.r2, (r8 * 512 + 8 * c8) * 2, opq(x.so_ + (C - 20) * 128 * 512 * 2));
        } else {
            const int r16 = lw * 16 + j * 4 + (lane >> 4), c16 = lane & 15;
            buf[C % 7][j] = ldw(x.r1, ((256 * (r16 >> 5) + (r16 & 31)) * 1792 + (((c16 >> 2) ^ (r16 & 3)) * 32) + 8 * (c16 & 3)) * 2, opq(x.s1 + (C - 12) * 32 * 1792 * 2));
        }
        if (PC) __builtin_amdgcn_s_sleep(PC);
    }
}
template <int C> DEV void loader_step(u32x4 (&buf)[7][4], const LoaderCtx& x, LAS unsigned char* L, unsigned lds0, int lw, int layer, rsrc_t r, int lane) {
    constexpr int NI = C < 8 ? 3 : 4, SL = C % NSLOT;
    const unsigned l16 = lane * 16;
    if (C < 34) {
        lds_wait_ge(L, C_FREE + SL * 4, 8u * (layer * uses_of(SL) + C / NSLOT), r, lane);
        LAS unsigned char* dst = L + LD_RING + SL * SLOT + lw * NI * 1024 + l16;
#pragma unroll
        for (int i = 0; i < NI; ++i) *(LAS u32x4*)(dst + i * 1024) = buf[C % 7][i];
        lds_signal(lds0 + C_FULL + SL * 4, lane);
    }
    constexpr int N = (C + 7) % 35;
    constexpr bool DEFER = (N >= 8 && N <= 14) || C + 7 >= 35;
    if (!DEFER && N != 34) loader_fetch<N>(buf, x, lane);
#define GATE_ON(G) lds_wait_ge(L, C_FREE + ((G) % NSLOT) * 4, 8u * (layer * uses_of((G) % NSLOT) + (G) / NSLOT + 1), r, lane)
    if (C == 7) {
        GATE_ON(GATE_O);
        loader_fetch<8>(buf, x, lane); loader_fetch<9>(buf, x, lane); loader_fetch<10>(buf, x, lane); loader_fetch<11>(buf, x, lane); loader_fetch<12>(buf, x, lane); loader_fetch<13>(buf, x, lane); loader_fetch<14>(buf, x, lane);
    }
    if (C == 34 && layer == 0) {
        GATE_ON(GATE_Q);
        loader_fetch<0>(buf, x, lane); loader_fetch<1>(buf, x, lane); loader_fetch<2>(buf, x, lane); loader_fetch<3>(buf, x, lane); loader_fetch<4>(buf, x, lane); loader_fetch<5>(buf, x, lane); loader_fetch<6>(buf, x, lane);
    }
#undef GATE_ON
    SB();
}
template <int... Cs> struct seq {};
template <int N, int... Cs> struct mkseq : mkseq<N - 1, N - 1, Cs...> {};
template <int... Cs> struct mkseq<0, Cs...> { typedef seq<Cs...> type; };
template <int... Cs> DEV void loader_layer(seq<Cs...>, u32x4 (&buf)[7][4], const LoaderCtx& x, LAS unsigned char* L, unsigned lds0, int lw, int layer, rsrc_t r, int lane) {
    (loader_step<Cs>(buf, x, L, lds0, lw, layer, r, lane), ...);
}
template <int... Cs> DEV void loader_prime(seq<Cs...>, u32x4 (&buf)[7][4], const LoaderCtx& x, int lane) { (loader_fetch<Cs>(buf, x, lane), ...); }
DEV unsigned loader(const Params& p, LAS unsigned char* L, unsigned lds0, int lw, int mem, rsrc_t r, int lane, bool first) {
    const int cb = mem >> 1, mem1 = mem < 14 ? mem : 13;
    LoaderCtx x;
    x.rq = __builtin_amdgcn_make_buffer_rsrc((void*)p.wqkv, 0, D * 3 * D * 2, 0x00020000); x.ro = __builtin_amdgcn_make_buffer_rsrc((void*)p.wo, 0, D * D * 2, 0x00020000);
    x.r1 = __builtin_amdgcn_make_buffer_rsrc((void*)p.w1, 0, D * F * 2, 0x00020000); x.r2 = __builtin_amdgcn_make_buffer_rsrc((void*)p.w2, 0, F * D * 2, 0x00020000);
    x.sq = 32 * mem * 2; x.so_ = 64 * cb * 2; x.s1 = 128 * mem1 * 2; x.lw = lw;
    u32x4 buf[7][4];
    loader_fetch<0>(buf, x, lane); loader_fetch<1>(buf, x, lane);
    __builtin_amdgcn_s_sleep(PRIME_PAUSE);
    loader_fetch<2>(buf, x, lane); loader_fetch<3>(buf, x, lane); loader_fetch<4>(buf, x, lane); loader_fetch<5>(buf, x, lane); loader_fetch<6>(buf, x, lane);
    unsigned ep = 0;
    if (first) { ep = rfl(__builtin_amdgcn_raw_buffer_load_b32(r, 0, WS_EPOCH, SC1)) + 1u; lds_wait_ge(L, C_ATT, 8u, r, lane); if (lane == 0) __builtin_amdgcn_raw_buffer_store_b32(ep, r, 0, WS_START + blockIdx.x * 4, SC1); }
    for (int layer = 0; layer < 2; ++layer) loader_layer(mkseq<35>::type(), buf, x, L, lds0, lw, layer, r, lane);
    return ep;
}

DEV void n64_addr(int (&wa)[2], int kq, int lane) {
    const int q4 = (lane & 15) >> 2;
#pragma unroll
    for (int t = 0; t < 2; ++t) { const int g = (2 * t + ((lane >> 4) & 1)) ^ kq;
        wa[t] = kq * 4096 + (4 * (lane >> 5) + q4) * 128 + (((g >> 1) ^ ((q4 >> 1) & 1)) * 64) + (g & 1) * 32 + (lane & 3) * 8; }
}
#define STAGE_BAR(n) do { lds_signal(lds0 + C_BAR, lane); lds_wait_ge(L, C_BAR, 8u * (5 * layer + (n)), r, lane); } while (0)
__global__ __launch_bounds__(NTHR, 3) void k_main(Params p) {
    extern __shared__ __attribute__((aligned(16))) unsigned char lds[];
    int team, mem; block_map(blockIdx.x, team, mem);
    const int tid = threadIdx.x, wid = rfl(tid >> 6); int lane = tid & 63;
    LAS unsigned char* L = (LAS unsigned char*)lds;
    const unsigned lds0 = (unsigned)(size_t)L;
    const rsrc_t r = __builtin_amdgcn_make_buffer_rsrc(p.ws, 0, WS_END, 0x00020000);
    STAMP_RT(0); STAMP_INIT();
    if (wid >= NCW) {
        if (wid < NCW + 2) {
            const unsigned ln = (blockIdx.x * 2 + (wid - NCW)) * 64 + lane;
            const unsigned char* a = ln < 4096 ? (const unsigned char*)p.wo + ln * 128 : ln < 4096 + 14336 ? (const unsigned char*)p.w1 + (ln - 4096) * 128 : (const unsigned char*)p.w2 + (ln - 4096 - 14336) * 128;
            unsigned sv; asm volatile("s_mov_b32 %0, m0\n\ts_mov_b32 m0, %2\n\ts_nop 2\n\tglobal_load_lds_dword %1, off\n\ts_mov_b32 m0, %0" : "=&s"(sv) : "v"(a), "s"(lds0 + LD_DUMMY) : "memory");
        }
    }

    if (tid < 64) ((LAS unsigned*)(L + LD_CTR))[tid] = 0u;
    if (tid >= 64 && tid < 68) ((LAS unsigned*)(L + LD_ONES))[tid & 3] = 0x3f803f80u;
    __syncthreads();
    STAMPV(0, 0);
    if (wid >= NCW) {
        const int ln = (int)__builtin_amdgcn_mbcnt_hi(~0u, __builtin_amdgcn_mbcnt_lo(~0u, 0u));
        const unsigned ep = loader(p, L, lds0, wid - NCW, mem, r, ln, wid == NCW);
        if (blockIdx.x == 0 && wid == NCW) {
#pragma unroll 1
            for (unsigned spins = 0; spins < SPIN_CAP; ++spins) {
                const u32x4 v = ld16(r, ln * 16, WS_START);
                if (__all(v.x == ep && v.y == ep && v.z == ep && v.w == ep)) { if (ln == 0) __builtin_amdgcn_raw_buffer_store_b32(ep, r, 0, WS_EPOCH, SC1); break; }
                __builtin_amdgcn_s_sleep(8);
            }
        }
        STAMPV(0, 1); STAMP_DUMP(); return;
    }

    const int cb = mem >> 1, kh = wid & 1, kq = wid & 3;
    const int tb1 = 4 * team + (wid >> 1);
    const int tb2 = 4 * team + 2 * (wid >> 2) + (mem & 1);
    const rsrc_t rx = __builtin_amdgcn_make_buffer_rsrc((void*)p.x, 0, S * D * 2, 0x00020000);
    unsigned ep = 0;

    for (int layer = 0; layer < 2; ++layer) {
        const unsigned wl = opq(WS_LAYER + layer * L_SIZE), fl = opq(WS_FLAGS + layer * FL_LAYER);
        if (layer) STAMPV(layer, 0);
        {
            lane = opqv(lane);
            float sx, sq;
            f32x16 acc[3];
#pragma unroll
            for (int t = 0; t < 3; ++t) acc[t] = (f32x16){};
            const unsigned gneed = lane == 0 ? 8u * (layer + 1) : (lane == 1 + (wid >> 2) ? 4u * (4 * layer) : (lane < 3 && layer ? 4u * (4 * layer - 1) : 0u));
            const int goff = lane == 0 ? C_ATT : C_XRD + ((lane - 1) & 1) * 4;
            int wa[3];
            if (layer == 0) {
                bf16x8 b[16]; f32x4 pr[4]; f32x16 acs;
                const unsigned epv = __builtin_amdgcn_raw_buffer_load_b32(r, 0, WS_EPOCH, SC1);
                u32x4 xr[16];
                const unsigned vo = (lane >> 4) * (D * 2) + (lane & 15) * 16, so = opq((tb1 * 32 * D + 256 * kh) * 2);
#pragma unroll
                for (int i = 0; i < 16; ++i) xr[i] = ldw(rx, vo + (i & 7) * 4 * (D * 2) + (i >> 3) * 256, so);
                ep = rfl(epv) + 1u;
                lds_signal(lds0 + C_ATT, lane);
                STAMPV(1, 22);
                LAS unsigned char* xt = L + LD_XA + wid * 8192;
#pragma unroll
                for (int hf = 0; hf < 2; ++hf) {
#pragma unroll
                    for (int j = 0; j < 8; ++j) { const int row = 4 * j + (lane >> 4); *(LAS u32x4*)(xt + row * 256 + (((lane & 15) ^ (row & 15)) * 16)) = xr[8 * hf + j]; }
#pragma unroll
                    for (int s = 0; s < 8; ++s) b[8 * hf + s] = *(LAS const bf16x8*)(xt + (lane & 31) * 256 + (((2 * s + (lane >> 5)) ^ (lane & 15)) * 16));
                }
                STAMPV(layer, 1);
                lds_wait_vec(L, goff, gneed, r, lane);
                float a1[4] = {0.f, 0.f, 0.f, 0.f}, a2[4] = {0.f, 0.f, 0.f, 0.f};
#pragma unroll
                for (int s = 0; s < 16; ++s) { const u32x4 u = __builtin_bit_cast(u32x4, b[s]);
#pragma unroll
                    for (int e = 0; e < 4; ++e) { const float lo = bflo(u[e]), hi = bfhi(u[e]); a1[e] += lo + hi; a2[e] = __builtin_fmaf(lo, lo, __builtin_fmaf(hi, hi, a2[e])); } }
                const float s1 = xsum32((a1[0] + a1[1]) + (a1[2] + a1[3])), s2 = xsum32((a2[0] + a2[1]) + (a2[2] + a2[3]));
                sx = s1 * (1.f / 256.f); sq = __builtin_fmaxf(s2 - s1 * sx, 0.f);
                asm volatile("" : "+v"(sx), "+v"(sq));
                STAMPV(1, 23);
#pragma unroll
                for (int t = 0; t < 3; ++t) wa[t] = kh * 6144 + (8 * (lane >> 5) + ((lane & 15) >> 2)) * 192 + t * 64 + ((lane >> 4) & 1) * 32 + (lane & 3) * 8;
                STAMPW(layer, 24, gemm_stage<0, 8, 3, 2, 192, 16, 0, true, true, false>(wid, lds, acc, b, wa, 4 * 192, acs, wid >> 1, pr, sx, sq, L, lds0, layer, r, 0, lane));
                lane = opqv(lane);
                if ((wid >> 1) < 3) { cs_store(acs, (LAS float*)(L + LD_CSQ) + 96 * kh + 32 * (wid >> 1), lane); lds_signal(lds0 + C_CS, lane); }
            } else {
                bf16x8 b[16]; f32x4 pr[4]; f32x16 acs;
                wait_flags(r, WS_FLAGS + FL_X2 + tb1 * 32 * WSTR, 0xFFFFu << (16 * kh), lane, ep);
                const unsigned so = opq(WS_LAYER + L_X2F + (tb1 * 32 + 16 * kh) * KB);
#pragma unroll
                for (int s = 0; s < 16; ++s) { b[s] = ldf(r, lane * 16, so + s * KB); if (s == 3) stats_issue(r, WS_LAYER + L_ST2, tb1, kh, lane, pr); }
                STAMPV(layer, 1);
                lds_wait_vec(L, goff, gneed, r, lane);
#pragma unroll
                for (int t = 0; t < 3; ++t) wa[t] = kh * 6144 + (4 * (lane >> 5) + ((lane & 15) >> 2)) * 192 + t * 64 + ((lane >> 4) & 1) * 32 + (lane & 3) * 8;
                STAMPW(layer, 24, gemm_stage<0, 8, 3, 2, 192, 16, 0, true, false, true>(wid, lds, acc, b, wa, 8 * 192, acs, -1, pr, sx, sq, L, lds0, layer, r, 0, lane));
            }
            STAMPV(layer, 2);
            lane = opqv(lane);
            if (lane < 32) ((LAS f32x2*)(L + LD_ST))[wid * 32 + lane] = (f32x2){sx, sq};
            const int blk = 4 * team + (wid >> 1), kb = 8 + (wid >> 1);
            const unsigned kvo = wl + L_KV + (blk * 16 + mem) * 4 * KB;
            if (kh == 0) { asm volatile("; kh 0"); qkv_epi<0>(acc, sx, sq, L, lds0, wid, lane, layer, kb, r, kvo); asm volatile("; kh 0 end"); }
            else { asm volatile("; kh 1"); qkv_epi<1>(acc, sx, sq, L, lds0, wid, lane, layer, kb, r, kvo); asm volatile("; kh 1 end"); }
            lds_signal(lds0 + C_LOCAL + (wid >> 1) * 4, lane);
            STAMPV(layer, 3);
            publish(r, fl + FL_KV + ((blk * 16 + mem) * 2 + kh) * WSTR, lane, ep);
            STAGE_BAR(1);
            STAMPV(layer, 4);
        }
        {
            lane = opqv(lane);
            const int gb = 4 * team - 8 + wid;
            if (gb >= 0) {
                wait_flags(r, fl + FL_KV + (gb * 16 + mem) * 2 * WSTR, 3u, lane, ep);
                STAMPV(layer, 5);
                u32x4 kv[4];
                const unsigned so = opq(wl + L_KV + (gb * 16 + mem) * 4 * KB);
#pragma unroll
                for (int i = 0; i < 4; ++i) kv[i] = ld16(r, lane * 16, so + i * KB);
                lds_wait_vec(L, C_XRD + (lane & 1) * 4, 4u * (4 * layer + 1), r, lane);
#pragma unroll
                for (int i = 0; i < 2; ++i) {
                    *(LAS u32x4*)(L + LD_K + (wid * 2 + i) * 1024 + lane * 16) = kv[i];
                    *(LAS u32x4*)(L + LD_V + (wid * 2 + i) * 1024 + lane * 16) = kv[2 + i];
                }
            }
            lds_signal(lds0 + C_HALO + wid * 4, lane);
            const int qb = wid >> 1, hd = wid & 1;
            STAMPV(layer, 6);
            {
                const bool isl = lane < 4, ish = lane >= 8 && lane < 16;
                const unsigned need = isl ? (lane <= qb ? 2u * (layer + 1) : 0u) : (ish && (lane - 8) >= qb ? (unsigned)(layer + 1) : 0u);
                lds_wait_vec(L, C_LOCAL + (lane & 15) * 4, need, r, lane);
            }
            STAMPV(layer, 7);
            f32x16 o;
            if (team >= 2) attn_unit<true>(L, team, qb, hd, lane, o); else attn_unit<false>(L, team, qb, hd, lane, o);
            STAMPV(layer, 8);
            lane = opqv(lane);
            lds_add1(lds0 + C_ATT, lane);
            const float rl = __builtin_amdgcn_rcpf(hd ? o[0] : o[8]);
            float v[8];
#pragma unroll
            for (int e = 0; e < 8; ++e) v[e] = (hd ? o[8 + e] : o[e]) * rl;
            st16(r, lane * 16, wl + L_OF + ((4 * team + qb) * 32 + 2 * mem + hd) * KB, pack8(v));
            publish(r, fl + FL_O + ((4 * team + qb) * 32 + 2 * mem + hd) * WSTR, lane, ep);
            STAGE_BAR(2);
            STAMPV(layer, 9);
        }
        {
            lane = opqv(lane);
            wait_flags(r, fl + FL_O + tb2 * 32 * WSTR, 0xFFu << (8 * kq), lane, ep);
            STAMPV(layer, 10);
            f32x16 acc[2], acs; bf16x8 b[9]; f32x4 pr[4]; float sx, sq;
            acc[0] = (f32x16){}; acc[1] = (f32x16){};
            int wa[2]; n64_addr(wa, kq, lane);
            u32x4 res;
            if (layer == 0) { const unsigned vo = (lane & 31) * (D * 2) + (lane >> 5) * 8, so = (tb2 * 32 * D + 64 * cb + 16 * kq) * 2;
                const u32x2 a = __builtin_amdgcn_raw_buffer_load_b64(rx, vo, so, 0), c = __builtin_amdgcn_raw_buffer_load_b64(rx, vo + 16, so, 0); res = (u32x4){a.x, a.y, c.x, c.y}; }
            else res = ld16(r, lane * 16, WS_LAYER + L_X2F + (tb2 * 32 + 4 * cb + kq) * KB);
            STAMPW(layer, 25, gemm_stage<8, 4, 2, 99, 128, 9, 0, false, false, false>(wid, lds, acc, b, wa, 8 * 128, acs, -1, pr, sx, sq, L, lds0, layer, r, opq(wl + L_OF + (tb2 * 32 + 8 * kq) * KB), lane));
            STAMPV(layer, 11);
            lane = opqv(lane);
            lds_wait_vec(L, (lane & 1) ? C_ATT : C_XRD + (wid >> 2) * 4, (lane & 1) ? 8u * (layer + 2) : 4u * (4 * layer + 1), r, lane);
            float v[8]; kq_reduce(acc, L, lds0, wid, lane, 4 * layer + 2, r, v);
            resid_add(v, res);
            st16(r, lane * 16, wl + L_X1F + (tb2 * 32 + 4 * cb + kq) * KB, res);
            stats_out(res, r, wl + L_ST1, tb2, 4 * cb + kq, lane);
            STAMPV(layer, 12);
            publish(r, fl + FL_X1 + (tb2 * 32 + 4 * cb + kq) * WSTR, lane, ep);
            STAGE_BAR(3);
            STAMPV(layer, 13);
        }
        {
            lane = opqv(lane);
            wait_flags(r, fl + FL_X1 + tb1 * 32 * WSTR, 0xFFFFu << (16 * kh), lane, ep);
            STAMPV(layer, 14);
            f32x16 acc[4];
#pragma unroll
            for (int t = 0; t < 4; ++t) acc[t] = (f32x16){};
            int wa[4];
            {
                const int q4 = (lane & 15) >> 2;
#pragma unroll
                for (int t = 0; t < 4; ++t) wa[t] = kh * 8192 + (4 * (lane >> 5) + q4) * 256 + (((t ^ (2 * kh)) ^ q4) * 64) + ((lane >> 4) & 1) * 32 + (lane & 3) * 8;
            }
            const unsigned so = opq(wl + L_X1F + (tb1 * 32 + 16 * kh) * KB);
            float sx, sq;
            if (layer == 0) {
                f32x4 pr[4]; f32x16 acs; stats_issue(r, wl + L_ST1, tb1, kh, lane, pr);
                bf16x8 b[4];
                STAMPW(layer, 26, gemm_stage<12, 8, 4, 99, 256, 4, 0, false, true, true>(wid, lds, acc, b, wa, 8 * 256, acs, wid >> 1, pr, sx, sq, L, lds0, layer, r, so, lane));
                lane = opqv(lane);
                cs_store(acs, (LAS float*)(L + LD_CS1) + 128 * kh + 32 * ((wid >> 1) ^ (2 * kh)), lane);
                lds_signal(lds0 + C_CS, lane);
            } else {
                f32x4 pr[4]; f32x16 acs; stats_issue(r, wl + L_ST1, tb1, kh, lane, pr);
                bf16x8 b[RB_F1];
                STAMPW(layer, 26, gemm_stage<12, 8, 4, 99, 256, RB_F1, 0, false, false, true>(wid, lds, acc, b, wa, 8 * 256, acs, -1, pr, sx, sq, L, lds0, layer, r, so, lane));
            }
            STAMPV(layer, 15);
            lane = opqv(lane); const int t32 = lane & 31, h = lane >> 5;
            lds_wait_ge(L, C_XRD + (wid >> 2) * 4, 4u * (4 * layer + 2), r, lane);
            xch_put(L, wid, 0, lane, acc[2], 0); xch_put(L, wid, 1, lane, acc[2], 1); xch_put(L, wid, 2, lane, acc[3], 0); xch_put(L, wid, 3, lane, acc[3], 1);
            if (h == 0) ((LAS f32x2*)(L + LD_ST))[wid * 32 + t32] = (f32x2){sx, sq};
            lds_signal(lds0 + C_XF + wid * 4, lane);
            if (layer == 0) lds_wait_ge(L, C_CS, 14u, r, lane);
            LAS const float* csl = (LAS const float*)(L + LD_CS1) + 64 * kh;
            f32x4 cs[8];
#pragma unroll
            for (int i = 0; i < 8; ++i) cs[i] = *(LAS const f32x4*)(csl + 8 * i + 4 * h) + *(LAS const f32x4*)(csl + 128 + 8 * i + 4 * h);
            lds_wait_ge(L, C_XF + (wid ^ 1) * 4, 4u * layer + 3, r, lane);
            const f32x2 os = ((LAS const f32x2*)(L + LD_ST))[(wid ^ 1) * 32 + t32];
            xch_add(L, wid ^ 1, 0, lane, acc[0], 0); xch_add(L, wid ^ 1, 1, lane, acc[0], 1); xch_add(L, wid ^ 1, 2, lane, acc[1], 0); xch_add(L, wid ^ 1, 3, lane, acc[1], 1);
            lds_signal(lds0 + C_XRD + (wid >> 2) * 4, lane);
            float la, lb; ln_coef(os, sx, sq, la, lb);
            if (mem < 14) {
#pragma unroll
                for (int i = 0; i < 4; ++i) {
                    float hv[8], ex[8];
#pragma unroll
                    for (int e = 0; e < 8; ++e) { hv[e] = la * acc[i >> 1][8 * (i & 1) + e] + lb * cs[2 * i + (e >> 2)][e & 3]; ex[e] = -LOG2E * hv[e]; }
                    SB();
#pragma unroll
                    for (int e = 0; e < 8; ++e) ex[e] = __builtin_amdgcn_exp2f(ex[e]);
                    SB();
#pragma unroll
                    for (int e = 0; e < 8; ++e) ex[e] += 1.f;
                    SB();
#pragma unroll
                    for (int e = 0; e < 8; ++e) ex[e] = __builtin_amdgcn_rcpf(ex[e]);
                    SB();
#pragma unroll
                    for (int e = 0; e < 8; ++e) hv[e] *= ex[e];
                    st16(r, lane * 16, wl + L_HF + (tb1 * 112 + 8 * mem + 4 * kh + i) * KB, pack8(hv));
                    SB();
                }
                STAMPV(layer, 16);
                publish(r, fl + FL_H + (tb1 * 32 + 2 * mem + kh) * WSTR, lane, ep);
            }
            STAGE_BAR(4);
            STAMPV(layer, 17);
        }
        {
            lane = opqv(lane);
            wait_flags(r, fl + FL_H + tb2 * 32 * WSTR, (0x5555555u << (kq >> 1)) & 0xFFFFFFFu, lane, ep);
            STAMPV(layer, 18);
            f32x16 acc[2], acs; bf16x8 b[RB_F2]; f32x4 pr[4]; float sx, sq;
            acc[0] = (f32x16){}; acc[1] = (f32x16){};
            int wa[2]; n64_addr(wa, kq, lane);
            STAMPW(layer, 27, gemm_stage<20, 14, 2, 99, 128, RB_F2, 1, false, false, false>(wid, lds, acc, b, wa, 8 * 128, acs, -1, pr, sx, sq, L, lds0, layer, r, opq(wl + L_HF + (tb2 * 112 + 2 * kq) * KB), lane));
            STAMPV(layer, 19);
            lane = opqv(lane);
            u32x4 res = ld16(r, lane * 16, wl + L_X1F + (tb2 * 32 + 4 * cb + kq) * KB);
            lds_wait_ge(L, C_XRD + (wid >> 2) * 4, 4u * (4 * layer + 3), r, lane);
            float v[8]; kq_reduce(acc, L, lds0, wid, lane, 4 * layer + 4, r, v);
            resid_add(v, res);
            if (layer == 0) {
                st16(r, lane * 16, wl + L_X2F + (tb2 * 32 + 4 * cb + kq) * KB, res);
                stats_out(res, r, wl + L_ST2, tb2, 4 * cb + kq, lane);
                STAMPV(layer, 20);
                publish(r, fl + FL_X2 + (tb2 * 32 + 4 * cb + kq) * WSTR, lane, ep);
                STAGE_BAR(5);
            } else {
                auto s0 = __builtin_amdgcn_permlane32_swap(res.x, res.z, false, false), s1 = __builtin_amdgcn_permlane32_swap(res.y, res.w, false, false);
                unsigned short* dst = p.out + (size_t)(32 * tb2 + (lane & 31)) * D + 64 * cb + 16 * kq + 8 * (lane >> 5);
                *(u32x4*)dst = (u32x4){s0[0], s1[0], s0[1], s1[1]};
            }
            STAMPV(layer, 21);
        }
    }
    STAMP_RT(1); STAMP_DUMP();
}

extern "C" void kernel_launch(void* const* d_in, const int* in_sizes, int n_in, void* d_out, int out_size, void* d_ws, size_t ws_size, hipStream_t stream) {
    Params p;
    p.x = (const unsigned short*)d_in[0]; p.wqkv = (const unsigned short*)d_in[1]; p.wo = (const unsigned short*)d_in[2];
    p.w1 = (const unsigned short*)d_in[4]; p.w2 = (const unsigned short*)d_in[5];
    p.out = (unsigned short*)d_out; p.ws = (unsigned char*)d_ws;
    (void)hipFuncSetAttribute((const void*)k_main, hipFuncAttributeMaxDynamicSharedMemorySize, LDS_BYTES);
    k_main<<<256, NTHR, LDS_BYTES, stream>>>(p);
}
```

```cpp
#include <hip/hip_runtime.h>
#include <hip/hip_bf16.h>
#include <stdint.h>

typedef short bf16x8 __attribute__((ext_vector_type(8)));
typedef float f32x2 __attribute__((ext_vector_type(2)));
typedef float f32x4 __attribute__((ext_vector_type(4)));
typedef float f32x8 __attribute__((ext_vector_type(8)));
typedef float f32x16 __attribute__((ext_vector_type(16)));
typedef unsigned u32x2 __attribute__((ext_vector_type(2)));
typedef unsigned u32x4 __attribute__((ext_vector_type(4)));
typedef __bf16 bf16x2_t __attribute__((ext_vector_type(2)));
#define GAS __attribute__((address_space(1)))
#define LAS __attribute__((address_space(3)))
#define DEV __device__ __forceinline__
#define SB() __builtin_amdgcn_sched_barrier(0)
typedef __amdgpu_buffer_rsrc_t rsrc_t;

constexpr int S = 2048, D = 512, H = 32, F = 1792;
constexpr float EPS = 1e-5f;
constexpr float LOG2E = 1.4426950408889634f;
constexpr float QSCALE = 0.25f * LOG2E;

constexpr unsigned KB = 1024, MB = 1024 * 1024;
constexpr unsigned WS_EPOCH = 256;
constexpr unsigned WS_START = 4096;
constexpr unsigned WS_LAYER = 64 * KB;
constexpr unsigned L_KV  = 0;
constexpr unsigned L_OF  = L_KV + 4 * MB;
constexpr unsigned L_X1F = L_OF + 2 * MB;
constexpr unsigned L_HF  = L_X1F + 2 * MB;
constexpr unsigned L_X2F = L_HF + 7 * MB;
constexpr unsigned L_ST1 = L_X2F + 2 * MB;
constexpr unsigned L_ST2 = L_ST1 + 512 * KB;
constexpr unsigned L_SIZE = L_ST2 + 512 * KB;
constexpr unsigned WSTR = 128;
constexpr unsigned WS_FLAGS = 64 * MB;
constexpr unsigned FL_KV = 0, FL_O = FL_KV + 64 * 16 * 2 * WSTR, FL_X1 = FL_O + 64 * 32 * WSTR, FL_H = FL_X1 + 64 * 32 * WSTR, FL_X2 = FL_H + 64 * 32 * WSTR, FL_LAYER = FL_X2 + 64 * 32 * WSTR;
constexpr unsigned WS_END = 72 * MB;
static_assert(WS_LAYER + 2 * L_SIZE <= WS_FLAGS && WS_FLAGS + 2 * FL_LAYER <= WS_END, "workspace map");

constexpr int NCW = 8, NLW = 4, NTHR = 64 * (NCW + NLW);
constexpr int SLOT = 16384, NSLOT = 5;
constexpr int LD_RING = 0;
constexpr int LD_XA = 80 * 1024;
constexpr int LD_K = 96 * 1024;
constexpr int LD_V = 120 * 1024;
constexpr int LD_XB = LD_V;
constexpr int LD_XC0 = LD_K + 16 * 1024, LD_XC1 = LD_V + 16 * 1024;
constexpr int LD_Q = 144 * 1024;
constexpr int LD_MISC = 152 * 1024;
constexpr int LD_CSQ = LD_MISC  , LD_CS1 = LD_MISC + 768  , LD_ONES = LD_MISC + 1792, LD_CTR = LD_MISC + 1856, LD_DUMMY = LD_CTR + 256  ,
              LD_ST = LD_DUMMY + 512  , LD_AB = LD_ST + 2048  , LD_STAMP = LD_AB + 1024;
constexpr int C_FULL = LD_CTR  , C_FREE = LD_CTR + 32  , C_LOCAL = LD_CTR + 64, C_HALO = LD_CTR + 96, C_XF = LD_CTR + 128, C_XRD = LD_CTR + 160, C_ATT = LD_CTR + 176, C_BAR = LD_CTR + 180, C_CS = LD_CTR + 184;
constexpr int LDS_BYTES = 160 * 1024;
static_assert(LD_STAMP + 8 * 64 * 4 <= LDS_BYTES && LD_CTR % 16 == 0 && LD_ST % 16 == 0, "LDS map");
constexpr int slot_of(int c) { return c % NSLOT; }
constexpr int NPOS = 35;

struct Params {
    const unsigned short* x; const unsigned short* wqkv; const unsigned short* wo; const unsigned short* w1; const unsigned short* w2;
    unsigned short* out; unsigned char* ws;
};

#define STAMPV(kid, i)
#define STAMPW(kid, i, ...) (void)(__VA_ARGS__)
#define STAMP_RT(i)
#define STAMP_DUMP()
#define STAMP_INIT()

DEV float bf2f(unsigned short b) { return __uint_as_float((unsigned)b << 16); }
DEV float bflo(unsigned u) { return __uint_as_float(u << 16); }
DEV float bfhi(unsigned u) { return __uint_as_float(u & 0xffff0000u); }
DEV unsigned pk2(float lo, float hi) { f32x2 v = {lo, hi}; bf16x2_t r = __builtin_convertvector(v, bf16x2_t); return __builtin_bit_cast(unsigned, r); }
DEV float xsum32(float v) { auto r = __builtin_amdgcn_permlane32_swap(__float_as_uint(v), __float_as_uint(v), false, false); return __uint_as_float(r[0]) + __uint_as_float(r[1]); }
DEV float xmax32(float v) { auto r = __builtin_amdgcn_permlane32_swap(__float_as_uint(v), __float_as_uint(v), false, false); return __builtin_fmaxf(__uint_as_float(r[0]), __uint_as_float(r[1])); }
DEV f32x16 mfma32(bf16x8 a, bf16x8 b, f32x16 c) { return __builtin_amdgcn_mfma_f32_32x32x16_bf16(a, b, c, 0, 0, 0); }
DEV float dot2(unsigned a, unsigned b, float c) { return __builtin_amdgcn_fdot2_f32_bf16(__builtin_bit_cast(bf16x2_t, a), __builtin_bit_cast(bf16x2_t, b), c, false); }
DEV unsigned opq(unsigned x) { asm volatile("" : "+s"(x)); return x; }
DEV int opqv(int x) { asm volatile("" : "+v"(x)); return x; }
DEV unsigned rfl(unsigned v) { return __builtin_amdgcn_readfirstlane(v); }
#define WAITV(n) asm volatile("s_waitcnt vmcnt(" #n ")" ::: "memory")
constexpr int SC1 = 16;
DEV bf16x8 ldf(rsrc_t r, unsigned vo, unsigned so) { return __builtin_bit_cast(bf16x8, __builtin_amdgcn_raw_buffer_load_b128(r, vo, so, SC1)); }
DEV u32x4 ld16(rsrc_t r, unsigned vo, unsigned so) { return __builtin_amdgcn_raw_buffer_load_b128(r, vo, so, SC1); }
DEV u32x4 ldw(rsrc_t r, unsigned vo, unsigned so) { return __builtin_amdgcn_raw_buffer_load_b128(r, vo, so, 0); }
DEV void st16(rsrc_t r, unsigned vo, unsigned so, u32x4 v) { __builtin_amdgcn_raw_buffer_store_b128(v, r, vo, so, SC1); asm volatile("s_nop 1" :: "v"(v) : "memory"); }

DEV void block_map(int b, int& team, int& mem) {
    const int x = b & 7, r = b >> 3;
    team = 4 * (x >> 1) + (r >> 3); mem = 8 * (x & 1) + (r & 7);
}

#define RB_F1 5
#define RB_F2 12
#define GATE_Q 27
#define WPRIO 3
#define FPRIO 0
#define POLL_SLEEP 1
constexpr unsigned SPIN_CAP = 1u << 15;
DEV void wait_flags(rsrc_t r, unsigned off, unsigned mask, int lane, unsigned ep) {
    bool need = lane < 32 && ((mask >> (lane & 31)) & 1u);
#pragma unroll 1
    for (unsigned spins = 0;; ++spins) {
        if (need) need = __builtin_amdgcn_raw_buffer_load_b32(r, lane * WSTR, off, SC1) != ep;
        if (!__any(need)) break;
        if (spins > SPIN_CAP) break;
        __builtin_amdgcn_s_sleep(POLL_SLEEP);
    }
    __builtin_amdgcn_fence(__ATOMIC_ACQUIRE, "wavefront");
}
DEV void publish(rsrc_t r, unsigned word_off, int lane, unsigned ep) {
    WAITV(0);
    if (lane == 0) __builtin_amdgcn_raw_buffer_store_b32(ep, r, 0, word_off, SC1);
}
DEV unsigned lds_ld(LAS unsigned char* L, int off) { return *(volatile LAS unsigned*)(L + off); }
DEV int s2v(int x) { int v; asm volatile("v_mov_b32 %0, %1" : "=v"(v) : "s"(x)); return v; }
DEV unsigned lds_ldu(LAS unsigned char* L, int off) { return *(volatile LAS unsigned*)(L + s2v(off)); }
DEV void lds_add1(unsigned addr, int lane) {
    const unsigned a = lane == 0 ? addr : (unsigned)(LD_DUMMY + lane * 4);
    asm volatile("ds_add_u32 %0, %1" :: "v"(a), "v"(1u) : "memory");
}
DEV void lds_signal(unsigned addr, int lane) { lds_add1(addr, lane); }
DEV void lds_wait_ge(LAS unsigned char* L, int off, unsigned target, rsrc_t r, int lane) {
#pragma unroll 1
    for (unsigned spins = 0; rfl(lds_ldu(L, off)) < target; ++spins) {
        if (spins > (SPIN_CAP << 3)) break;
        if (spins > 2) __builtin_amdgcn_s_sleep(1);
    }
    asm volatile("" ::: "memory");
}
DEV void lds_wait_vec(LAS unsigned char* L, int off, unsigned need, rsrc_t r, int lane) {
#pragma unroll 1
    for (unsigned spins = 0; !__all(lds_ld(L, off) >= need); ++spins) {
        if (spins > (SPIN_CAP << 3)) break;
        if (spins > 2) __builtin_amdgcn_s_sleep(1);
    }
    asm volatile("" ::: "memory");
}

DEV void lds_set(unsigned addr, unsigned val, int lane) {
    const unsigned a = lane == 0 ? addr : (unsigned)(LD_DUMMY + lane * 4);
    asm volatile("ds_write_b32 %0, %1" :: "v"(a), "v"(val) : "memory");
}
DEV unsigned qmin(unsigned v) {
    v = min(v, (unsigned)__builtin_amdgcn_mov_dpp((int)v, 0xB1, 0xF, 0xF, true));
    return min(v, (unsigned)__builtin_amdgcn_mov_dpp((int)v, 0x4E, 0xF, 0xF, true));
}
DEV unsigned ring_ready(LAS unsigned char* L, unsigned pos, int lane) {
    unsigned n;
#pragma unroll 1
    for (unsigned spins = 0;; ++spins) {
        n = rfl(qmin(lds_ld(L, C_FULL + (lane & 3) * 4)));
        if (n > pos || spins > (SPIN_CAP << 3)) break;
        if (spins > 2) __builtin_amdgcn_s_sleep(1);
    }
    asm volatile("" ::: "memory");
    return n;
}
DEV void stats_issue(rsrc_t r, unsigned st, int tb, int kh, int lane, f32x4 (&pr)[4]) {
    const unsigned so = opq(st + (tb * 16 + 8 * kh) * 512);
#pragma unroll
    for (int i = 0; i < 4; ++i) pr[i] = __builtin_bit_cast(f32x4, ld16(r, ((lane >> 5) * 4 * 32 + (lane & 31)) * 16 + i * 512, so));
}
DEV void stats_half(const f32x4 (&pr)[4], float& mean, float& m2) {
    float ms = 0.f;
#pragma unroll
    for (int i = 0; i < 4; ++i) ms += pr[i][0] + pr[i][2];
    mean = xsum32(ms) * (1.f / 16.f);
    float q = 0.f;
#pragma unroll
    for (int i = 0; i < 4; ++i) { const float d0 = pr[i][0] - mean, d1 = pr[i][2] - mean; q += pr[i][1] + pr[i][3] + 16.f * (d0 * d0 + d1 * d1); }
    m2 = xsum32(q);
}
DEV void stats_out(u32x4 o, rsrc_t r, unsigned st, int tb, int s, int lane) {
    const float q8[8] = {bflo(o.x), bfhi(o.x), bflo(o.y), bfhi(o.y), bflo(o.z), bfhi(o.z), bflo(o.w), bfhi(o.w)};
    const float mean = xsum32(((q8[0] + q8[1]) + (q8[2] + q8[3])) + ((q8[4] + q8[5]) + (q8[6] + q8[7]))) * (1.f / 16.f);
    float q = 0.f;
#pragma unroll
    for (int e = 0; e < 8; ++e) { const float d = q8[e] - mean; q += d * d; }
    q = xsum32(q);
    if (lane < 32) __builtin_amdgcn_raw_buffer_store_b64((u32x2){__float_as_uint(mean), __float_as_uint(q)}, r, lane * 16 + (s & 1) * 8, st + (tb * 16 + (s >> 1)) * 512, SC1);
}

typedef short v4i16 __attribute__((ext_vector_type(4)));
DEV v4i16 trd(LAS const unsigned char* p) { return __builtin_amdgcn_ds_read_tr16_b64_v4i16((LAS v4i16*)p); }
template <int C0, int NCH, int NT, int SWAPV, int P, int RB, int AMODE, bool PRE, bool CS, bool ST>
DEV unsigned gemm_stage(int wid, unsigned char* lds, f32x16 (&acc)[NT], bf16x8 (&b)[RB], const int (&wa)[NT], f32x16& acs, bool cs_on, int cst, const f32x4 (&pr)[4], float& sx, float& sq,
                        LAS unsigned char* L, unsigned lds0, int layer, rsrc_t r, unsigned so, int lane) {
    unsigned twait = 0;
    constexpr int NS = 2 * NCH, JS = 16 * P, TAU = 8 * P;
    const unsigned l16 = lane * 16;
#define AOFF(s_) (AMODE == 0 ? (s_) * KB : (((s_) >> 1) * 8 + ((s_) & 1)) * KB)
    if (!PRE) {
#pragma unroll
        for (int s = 0; s < RB - 1; ++s) if (s < NS) b[s] = ldf(r, l16, so + AOFF(s));
    }
    LAS const unsigned char* w0[NT]; LAS const unsigned char* w1[NT];
#pragma unroll
    for (int t = 0; t < NT; ++t) { w0[t] = L + LD_RING + wa[t]; w1[t] = L + opqv(LD_RING + 4 * SLOT + wa[t]); }
    const unsigned fadr = lane == 0 ? lds0 + C_FREE + wid * 4 : (unsigned)(LD_DUMMY + lane * 4);
    const unsigned pos0 = NPOS * layer + C0;
#define WADDR(s_, t_) ((slot_of(C0 + ((s_) >> 1)) == 4 ? w1[t_] : w0[t_] + slot_of(C0 + ((s_) >> 1)) * SLOT) + ((s_) & 1) * JS)
#define WFRAG(s_, t_) __builtin_shufflevector(trd(WADDR(s_, t_)), trd(WADDR(s_, t_) + TAU), 0, 1, 2, 3, 4, 5, 6, 7)
    bf16x8 ones;
    if (CS) { const unsigned one2 = (unsigned)s2v(0x3f803f80); ones = __builtin_bit_cast(bf16x8, (u32x4){one2, one2, one2, one2});
        const float z = __uint_as_float((unsigned)s2v(0));
#pragma unroll
        for (int e = 0; e < 16; ++e) acs[e] = z; }
    unsigned ready = ring_ready(L, pos0, lane);
    bf16x8 fa[NT];
#pragma unroll
    for (int t = 0; t < NT; ++t) fa[t] = WFRAG(0, t);
    SB();
#pragma unroll
    for (int s = 0; s < NS; ++s) {
        if (!PRE && s + RB - 1 < NS) b[(s + RB - 1) % RB] = ldf(r, l16, so + AOFF(s + RB - 1));
        const int sp = s + 1;
        if (sp < NS && (sp & 1) == 0) { if (pos0 + (sp >> 1) >= ready) {
            ready = ring_ready(L, pos0 + (sp >> 1), lane);
        } }
        if (CS) { if (cs_on) {
            u32x4 f = __builtin_bit_cast(u32x4, fa[0]);
#pragma unroll
            for (int t = 1; t < NT; ++t) { const u32x4 g = __builtin_bit_cast(u32x4, fa[t]);
#pragma unroll
                for (int e = 0; e < 4; ++e) f[e] = cst == t ? g[e] : f[e]; }
            acs = mfma32(__builtin_bit_cast(bf16x8, f), ones, acs);
        } }
        SB();
#pragma unroll
        for (int t = 0; t < NT; ++t) {
            if (t >= SWAPV) acc[t] = mfma32(b[s % RB], fa[t], acc[t]); else acc[t] = mfma32(fa[t], b[s % RB], acc[t]);
            if (sp < NS) fa[t] = WFRAG(sp, t);
            SB();
        }
        if (s & 1) { const unsigned rel = (unsigned)s2v((int)(pos0 + (s >> 1) + 1)); asm volatile("ds_write_b32 %0, %1" :: "v"(fadr), "v"(rel) : "memory"); }
        if (ST && s == 3) { stats_half(pr, sx, sq); asm volatile("" : "+v"(sx), "+v"(sq)); }
        if (s == 0) STAMPV(layer, C0 == 0 ? 28 : C0 == 8 ? 29 : C0 == 12 ? 30 : 31);
    }
#undef AOFF
#undef WADDR
#undef WFRAG
    return twait;
}
DEV int xch_off(int w, int slot) { return slot < 2 ? LD_XA + w * 4096 + slot * 2048 : slot == 2 ? LD_XB + w * 2048 : (w < 4 ? LD_XC0 + w * 2048 : LD_XC1 + (w - 4) * 2048); }
DEV void xch_put(LAS unsigned char* L, int w, int slot, int lane, const f32x16& a, int half) {
    LAS f32x4* d = (LAS f32x4*)(L + xch_off(w, slot)) + lane;
    d[0] = (f32x4){a[8 * half], a[8 * half + 1], a[8 * half + 2], a[8 * half + 3]}; d[64] = (f32x4){a[8 * half + 4], a[8 * half + 5], a[8 * half + 6], a[8 * half + 7]};
}
DEV void xch_add(LAS unsigned char* L, int w, int slot, int lane, f32x16& a, int half) {
    LAS const f32x4* d = (LAS const f32x4*)(L + xch_off(w, slot)) + lane;
    const f32x4 u = d[0], v = d[64];
#pragma unroll
    for (int e = 0; e < 4; ++e) { a[8 * half + e] += u[e]; a[8 * half + 4 + e] += v[e]; }
}
DEV void ln_coef(f32x2 o, float mean_h, float m2_h, float& la, float& lb) {
    const float mean = 0.5f * (mean_h + o.x), d = mean_h - o.x;
    const float var = (m2_h + o.y + 128.f * d * d) * (1.f / 512.f);
    la = __builtin_amdgcn_rsqf(var + EPS); lb = -mean * la;
}
DEV void kq_reduce(f32x16 (&acc)[2], LAS unsigned char* L, unsigned lds0, int wid, int lane, unsigned seq, rsrc_t r, float (&v)[8]) {
    const int base = wid & 4;
    xch_put(L, wid, 0, lane, acc[0], 1); xch_put(L, wid, 1, lane, acc[1], 0); xch_put(L, wid, 2, lane, acc[1], 1);
    lds_signal(lds0 + C_XF + wid * 4, lane);
    lds_wait_vec(L, C_XF + (base + (lane & 3)) * 4, seq, r, lane);
#pragma unroll
    for (int x = 1; x < 4; ++x) xch_add(L, wid ^ x, x - 1, lane, acc[0], 0);
    lds_signal(lds0 + C_XRD + (wid >> 2) * 4, lane);
#pragma unroll
    for (int e = 0; e < 8; ++e) v[e] = acc[0][e];
}
DEV u32x4 pack8(const float (&f)[8]) { return (u32x4){pk2(f[0], f[1]), pk2(f[2], f[3]), pk2(f[4], f[5]), pk2(f[6], f[7])}; }
DEV void resid_add(const float (&v)[8], u32x4& res) {
    const float f[8] = {bflo(res.x) + v[0], bfhi(res.x) + v[1], bflo(res.y) + v[2], bfhi(res.y) + v[3], bflo(res.z) + v[4], bfhi(res.z) + v[5], bflo(res.w) + v[6], bfhi(res.w) + v[7]};
    res = pack8(f);
}

template <int KH>
DEV void qkv_epi(f32x16 (&acc)[3], float sx, float sq, LAS unsigned char* L, unsigned lds0, int wid, int lane, int layer, int kb, rsrc_t r, unsigned kvo) {
    const int t32 = lane & 31, h = lane >> 5;
    xch_put(L, wid, 0, lane, acc[0], 1 - KH); xch_put(L, wid, 1, lane, acc[1], 1 - KH); xch_put(L, wid, 2, lane, acc[2], 1 - KH);
    lds_signal(lds0 + C_XF + wid * 4, lane);
    if (layer == 0) lds_wait_ge(L, C_CS, 6u, r, lane);
    LAS const float* csl = (LAS const float*)(L + LD_CSQ);
    f32x4 cq[2], ck[2];
#pragma unroll
    for (int u = 0; u < 2; ++u) { cq[u] = *(LAS const f32x4*)(csl + 16 * KH + 8 * u + 4 * h) + *(LAS const f32x4*)(csl + 96 + 16 * KH + 8 * u + 4 * h);
        ck[u] = *(LAS const f32x4*)(csl + 32 + 16 * KH + 8 * u + 4 * h) + *(LAS const f32x4*)(csl + 96 + 32 + 16 * KH + 8 * u + 4 * h); }
    const float cv = csl[64 + t32] + csl[96 + 64 + t32];
    lds_wait_ge(L, C_XF + (wid ^ 1) * 4, 4u * layer + 1, r, lane);
    const f32x2 os = ((LAS const f32x2*)(L + LD_ST))[(wid ^ 1) * 32 + t32];
    xch_add(L, wid ^ 1, 0, lane, acc[0], KH); xch_add(L, wid ^ 1, 1, lane, acc[1], KH); xch_add(L, wid ^ 1, 2, lane, acc[2], KH);
    lds_signal(lds0 + C_XRD + (wid >> 2) * 4, lane);
    float la, lb; ln_coef(os, sx, sq, la, lb);
    LAS f32x2* ab = (LAS f32x2*)(L + LD_AB) + (wid >> 1) * 32;
    if (h == 0) ab[t32] = (f32x2){la, lb};
    f32x4 s01[2], s23[2];
#pragma unroll
    for (int u = 0; u < 2; ++u) { s01[u] = *(LAS const f32x4*)(ab + 16 * KH + 8 * u + 4 * h); s23[u] = *(LAS const f32x4*)(ab + 16 * KH + 8 * u + 4 * h + 2); }
    float v[8];
#pragma unroll
    for (int u = 0; u < 2; ++u)
#pragma unroll
        for (int e = 0; e < 4; ++e) v[4 * u + e] = la * acc[1][8 * KH + 4 * u + e] + lb * ck[u][e];
    const u32x4 ok = pack8(v);
    st16(r, lane * 16, kvo + KH * KB, ok);
    *(LAS u32x4*)(L + LD_K + (kb * 2 + KH) * 1024 + lane * 16) = ok;
#pragma unroll
    for (int u = 0; u < 2; ++u) {
        v[4 * u + 0] = s01[u][0] * acc[2][8 * KH + 4 * u + 0] + s01[u][1] * cv; v[4 * u + 1] = s01[u][2] * acc[2][8 * KH + 4 * u + 1] + s01[u][3] * cv;
        v[4 * u + 2] = s23[u][0] * acc[2][8 * KH + 4 * u + 2] + s23[u][1] * cv; v[4 * u + 3] = s23[u][2] * acc[2][8 * KH + 4 * u + 3] + s23[u][3] * cv; }
    const u32x4 ov = pack8(v);
    st16(r, lane * 16, kvo + (2 + KH) * KB, ov);
    *(LAS u32x4*)(L + LD_V + (kb * 2 + KH) * 1024 + lane * 16) = ov;
    const float qa = la * QSCALE, qb_ = lb * QSCALE;
#pragma unroll
    for (int u = 0; u < 2; ++u)
#pragma unroll
        for (int e = 0; e < 4; ++e) v[4 * u + e] = qa * acc[0][8 * KH + 4 * u + e] + qb_ * cq[u][e];
    *(LAS u32x4*)(L + LD_Q + ((kb - 8) * 2 + KH) * 1024 + lane * 16) = pack8(v);
}
DEV void cs_store(const f32x16& a, LAS float* dst, int lane) {
    if ((lane & 31) == 0) {
#pragma unroll
        for (int u = 0; u < 4; ++u) *(LAS f32x4*)(dst + 8 * u + 4 * (lane >> 5)) = (f32x4){a[4 * u], a[4 * u + 1], a[4 * u + 2], a[4 * u + 3]};
    }
}

#define EXP2(x) __builtin_amdgcn_exp2f(x)
DEV void mask_first(f32x16& s, int q32, int h) {
#pragma unroll
    for (int r = 0; r < 16; ++r) { const int key = (r & 3) + 8 * (r >> 2) + 4 * h; if (key <= q32) s[r] = -INFINITY; }
}
DEV void mask_last(f32x16& s, int q32, int h) {
#pragma unroll
    for (int r = 0; r < 16; ++r) { const int key = (r & 3) + 8 * (r >> 2) + 4 * h; if (key > q32) s[r] = -INFINITY; }
}
DEV float max16(const f32x16& s, float m) {
    float a = __builtin_fmaxf(__builtin_fmaxf(s[0], s[1]), m), b = __builtin_fmaxf(__builtin_fmaxf(s[2], s[3]), s[4]);
    a = __builtin_fmaxf(__builtin_fmaxf(a, s[5]), s[6]); b = __builtin_fmaxf(__builtin_fmaxf(b, s[7]), s[8]);
    a = __builtin_fmaxf(__builtin_fmaxf(a, s[9]), s[10]); b = __builtin_fmaxf(__builtin_fmaxf(b, s[11]), s[12]);
    a = __builtin_fmaxf(__builtin_fmaxf(a, s[13]), s[14]);
    return __builtin_fmaxf(__builtin_fmaxf(a, b), s[15]);
}
template <bool FULL9>
DEV void attn_unit(LAS const unsigned char* L, int team, int qb, int hd, int lane, f32x16& o) {
    const int q32 = lane & 31, h = lane >> 5;
    const bf16x8 qf = *(LAS const bf16x8*)(L + LD_Q + (qb * 2 + hd) * 1024 + lane * 16);
    const int first = FULL9 ? 0 : 8 - 4 * team - qb;
    LAS const unsigned char* kp = L + LD_K + (qb * 2 + hd) * 1024 + lane * 16;
    const bool onel = ((lane >> 4) & 1) != hd;
    const unsigned vstr = onel ? 0u : 2048u;
    LAS const unsigned char* vp = onel ? L + LD_ONES : L + LD_V + qb * 2048 + lane * 16;
    LAS const unsigned char* vq = onel ? L + LD_ONES : L + LD_V + qb * 2048 + 1024 + lane * 16;
    bf16x8 kf[9];
#pragma unroll
    for (int i = 0; i < 9; ++i) if (FULL9 || i >= first) kf[i] = *(LAS const bf16x8*)(kp + i * 2048);
    const f32x16 zero = {};
    const int dq = q32 - 4 * h;
    float m = -INFINITY;
    if (FULL9) {
        f32x16 sa = mfma32(kf[0], qf, zero), sb = mfma32(kf[8], qf, zero);
        SB();
#pragma unroll
        for (int r = 0; r < 16; ++r) sa[r] = (r & 3) + 8 * (r >> 2) > dq ? sa[r] : sb[r];
        sb = mfma32(kf[1], qf, zero);
        SB();
        m = max16(sa, m);
        SB();
#pragma unroll
        for (int i = 1; i < 8; ++i) {
            f32x16& cur = (i & 1) ? sb : sa; f32x16& nxt = (i & 1) ? sa : sb;
            if (i + 1 < 8) nxt = mfma32(kf[i + 1], qf, zero);
            SB();
            m = max16(cur, m);
            SB();
        }
    } else {
#pragma unroll
        for (int i = 0; i < 9; ++i) if (i >= first) {
            f32x16 s = mfma32(kf[i], qf, zero);
            if (i == 0) mask_first(s, q32, h);
            if (i == 8) mask_last(s, q32, h);
            m = max16(s, m);
        }
    }
    m = xmax32(m);
    f32x16 nm;
#pragma unroll
    for (int r = 0; r < 16; ++r) nm[r] = -m;
    if (FULL9) {
        f32x16 sa = mfma32(kf[0], qf, nm), sb = mfma32(kf[8], qf, nm);
        const bf16x8 f0 = *(LAS const bf16x8*)(vp), f1 = *(LAS const bf16x8*)(vq), l0 = *(LAS const bf16x8*)(vp + 8 * vstr), l1 = *(LAS const bf16x8*)(vq + 8 * vstr);
        SB();
#pragma unroll
        for (int r = 0; r < 16; ++r) sa[r] = (r & 3) + 8 * (r >> 2) > dq ? sa[r] : sb[r];
        sb = mfma32(kf[1], qf, nm);
        bf16x8 vn0 = *(LAS const bf16x8*)(vp + vstr), vn1 = *(LAS const bf16x8*)(vq + vstr);
        SB();
        {
            u32x4 pf[2], pl[2];
#pragma unroll
            for (int r = 0; r < 16; ++r) sa[r] = EXP2(sa[r]);
            SB();
#pragma unroll
            for (int e = 0; e < 8; ++e) { const int r = 2 * e;
                const unsigned all = pk2(sa[r], sa[r + 1]), fst = pk2((r & 3) + 8 * (r >> 2) > dq ? sa[r] : 0.f, ((r + 1) & 3) + 8 * ((r + 1) >> 2) > dq ? sa[r + 1] : 0.f);
                pf[e >> 2][e & 3] = fst; pl[e >> 2][e & 3] = all ^ fst; }
            SB();
            o = mfma32(f0, __builtin_bit_cast(bf16x8, pf[0]), zero); o = mfma32(f1, __builtin_bit_cast(bf16x8, pf[1]), o);
            o = mfma32(l0, __builtin_bit_cast(bf16x8, pl[0]), o); o = mfma32(l1, __builtin_bit_cast(bf16x8, pl[1]), o);
            SB();
        }
#pragma unroll
        for (int i = 1; i < 8; ++i) {
            f32x16& cur = (i & 1) ? sb : sa; f32x16& nxt = (i & 1) ? sa : sb;
            const bf16x8 v0 = vn0, v1 = vn1;
            if (i + 1 < 8) { vn0 = *(LAS const bf16x8*)(vp + (i + 1) * vstr); vn1 = *(LAS const bf16x8*)(vq + (i + 1) * vstr); nxt = mfma32(kf[i + 1], qf, nm); }
            SB();
            u32x4 p0, p1;
#pragma unroll
            for (int e = 0; e < 4; ++e) p0[e] = pk2(EXP2(cur[2 * e]), EXP2(cur[2 * e + 1]));
            SB();
            o = mfma32(v0, __builtin_bit_cast(bf16x8, p0), o);
#pragma unroll
            for (int e = 0; e < 4; ++e) p1[e] = pk2(EXP2(cur[8 + 2 * e]), EXP2(cur[8 + 2 * e + 1]));
            SB();
            o = mfma32(v1, __builtin_bit_cast(bf16x8, p1), o);
            SB();
        }
    } else {
        float z = 0.f; asm volatile("" : "+v"(z));
#pragma unroll
        for (int r = 0; r < 16; ++r) o[r] = z;
#pragma unroll
        for (int i = 0; i < 9; ++i) if (i >= first) {
            f32x16 s = mfma32(kf[i], qf, nm);
            if (i == 0) mask_first(s, q32, h);
            if (i == 8) mask_last(s, q32, h);
            u32x4 p0, p1;
#pragma unroll
            for (int e = 0; e < 4; ++e) { p0[e] = pk2(EXP2(s[2 * e]), EXP2(s[2 * e + 1])); p1[e] = pk2(EXP2(s[8 + 2 * e]), EXP2(s[8 + 2 * e + 1])); }
            const bf16x8 v0 = *(LAS const bf16x8*)(vp + i * vstr), v1 = *(LAS const bf16x8*)(vq + i * vstr);
            o = mfma32(v0, __builtin_bit_cast(bf16x8, p0), o);
            o = mfma32(v1, __builtin_bit_cast(bf16x8, p1), o);
        }
    }
}

struct Fetch { rsrc_t rs; unsigned so; unsigned v[4]; unsigned vq[4], vo[4], v1[4], v2[4]; };
constexpr unsigned OOB = 0x7FFFFF00u;
DEV void fetch_init(Fetch& f, int lw, int lane) {
#pragma unroll
    for (int j = 0; j < 4; ++j) {
        const int i = lw * 192 + j * 48 + lane, row = (i * 683) >> 13, cc = i - 12 * row;
        f.vq[j] = lane < 48 ? ((256 * (row >> 5) + (row & 31)) * 1536 + (cc >> 2) * 512 + 8 * (cc & 3)) * 2 : OOB;
        const int r16 = lw * 16 + j * 4 + (lane >> 4), c16 = lane & 15;
        f.v1[j] = ((256 * (r16 >> 5) + (r16 & 31)) * 1792 + (((c16 >> 2) ^ (r16 & 3)) * 32) + 8 * (c16 & 3)) * 2;
        const int r8 = lw * 32 + j * 8 + (lane >> 3), c8 = (lane & 7) ^ (((r8 >> 1) & 1) << 2);
        f.vo[j] = ((128 * (r8 >> 5) + (r8 & 31)) * 512 + 8 * c8) * 2;
        f.v2[j] = (r8 * 512 + 8 * c8) * 2;
    }
}
DEV void fetch_pos(Fetch& f, const Params& p, int n, int mem, u32x4 (&b)[4]) {
    const int c = n >= 2 * NPOS ? 34 : n >= NPOS ? n - NPOS : n;
    if (c == 0 || c == 8 || c == 12 || c == 20 || c == 34) {
        const void* base = c == 0 ? (const void*)p.wqkv : c == 8 ? (const void*)p.wo : c == 12 ? (const void*)p.w1 : (const void*)p.w2;
        f.rs = __builtin_amdgcn_make_buffer_rsrc((void*)base, 0, c == 0 ? D * 3 * D * 2 : c == 8 ? D * D * 2 : D * F * 2, 0x00020000);
        f.so = (c == 0 ? 32 * mem : c == 12 ? 128 * (mem < 14 ? mem : 13) : 64 * (mem >> 1)) * 2;
#pragma unroll
        for (int j = 0; j < 4; ++j) f.v[j] = c == 0 ? f.vq[j] : c == 8 ? f.vo[j] : c == 12 ? f.v1[j] : c == 20 ? f.v2[j] : OOB;
    } else f.so += (c < 12 ? (c < 8 ? 32 * 1536 : 32 * 512) : (c < 20 ? 32 * 1792 : 128 * 512)) * 2;
#pragma unroll
    for (int j = 0; j < 4; ++j) { b[j] = ldw(f.rs, f.v[j], f.so); SB(); }
}
constexpr int NSET = 7;
DEV unsigned loader(const Params& p, LAS unsigned char* L, unsigned lds0, int lw, int mem, rsrc_t r, int lane, bool first) {
    u32x4 buf[NSET][4];
    Fetch f; fetch_init(f, lw, lane);
#pragma unroll
    for (int i = 0; i < NSET; ++i) { fetch_pos(f, p, i, mem, buf[i]); SB(); }
    unsigned ep = 0;
    if (first) { ep = rfl(__builtin_amdgcn_raw_buffer_load_b32(r, 0, WS_EPOCH, SC1)) + 1u; lds_wait_ge(L, C_ATT, 8u, r, lane); if (lane == 0) __builtin_amdgcn_raw_buffer_store_b32(ep, r, 0, WS_START + blockIdx.x * 4, SC1); }
#define LOADER_WRITE(n_, i_) do { const int n = (n_), c = n >= NPOS ? n - NPOS : n; \
        if (c < 34) { \
            if (n >= 5) lds_wait_vec(L, C_FREE + (lane & 7) * 4, (unsigned)(n == NPOS + 4 ? n - 9 : n - 4), r, lane);        \
            __builtin_amdgcn_s_setprio(WPRIO); \
            const int js = c < 8 ? 768 : 1024; \
            LAS unsigned char* dst = L + LD_RING + (c % NSLOT) * SLOT + lw * 4 * js + lane * 16; \
            if (c >= 8 || lane < 48) { *(LAS u32x4*)dst = buf[i_][0]; *(LAS u32x4*)(dst + js) = buf[i_][1]; *(LAS u32x4*)(dst + 2 * js) = buf[i_][2]; *(LAS u32x4*)(dst + 3 * js) = buf[i_][3]; } \
        } \
        lds_set(lds0 + C_FULL + lw * 4, (unsigned)s2v(n + 1), lane); __builtin_amdgcn_s_setprio(FPRIO); } while (0)
#pragma unroll 1
    for (int layer = 0; layer < 2; ++layer) {
#pragma unroll 1
        for (int n0 = NPOS * layer; n0 < NPOS * layer + NPOS - NSET; n0 += NSET) {
#pragma unroll
            for (int i = 0; i < NSET; ++i) { LOADER_WRITE(n0 + i, i); fetch_pos(f, p, n0 + i + NSET, mem, buf[i]); }
        }
#pragma unroll
        for (int i = 0; i < NSET; ++i) LOADER_WRITE(NPOS * layer + NPOS - NSET + i, i);
        if (layer == 0) lds_wait_vec(L, C_FREE + (lane & 7) * 4, (unsigned)GATE_Q, r, lane);
#pragma unroll
        for (int i = 0; i < NSET; ++i) fetch_pos(f, p, NPOS * (layer + 1) + i, mem, buf[i]);
    }
#undef LOADER_WRITE
    return ep;
}

DEV void n64_addr(int (&wa)[2], int kq, int lane) {
    const int q4 = (lane & 15) >> 2;
#pragma unroll
    for (int t = 0; t < 2; ++t) { const int g = (2 * t + ((lane >> 4) & 1)) ^ kq;
        wa[t] = kq * 4096 + (4 * (lane >> 5) + q4) * 128 + (((g >> 1) ^ ((q4 >> 1) & 1)) * 64) + (g & 1) * 32 + (lane & 3) * 8; }
}
#define STAGE_BAR(n) do { lds_signal(lds0 + C_BAR, lane); lds_wait_ge(L, C_BAR, 8u * (5 * layer + (n)), r, lane); } while (0)
__global__ __launch_bounds__(NTHR, 3) void k_main(Params p) {
    extern __shared__ __attribute__((aligned(16))) unsigned char lds[];
    int team, mem; block_map(blockIdx.x, team, mem);
    const int tid = threadIdx.x, wid = rfl(tid >> 6); int lane = tid & 63;
    LAS unsigned char* L = (LAS unsigned char*)lds;
    const unsigned lds0 = (unsigned)(size_t)L;
    const rsrc_t r = __builtin_amdgcn_make_buffer_rsrc(p.ws, 0, WS_END, 0x00020000);
    STAMP_RT(0); STAMP_INIT();
    if (tid < 64) ((LAS unsigned*)(L + LD_CTR))[tid] = 0u;
    if (tid >= 64 && tid < 68) ((LAS unsigned*)(L + LD_ONES))[tid & 3] = 0x3f803f80u;
    __syncthreads();
    STAMPV(0, 0);
    if (wid >= NCW) {
        const int ln = (int)__builtin_amdgcn_mbcnt_hi(~0u, __builtin_amdgcn_mbcnt_lo(~0u, 0u));
        const unsigned ep = loader(p, L, lds0, wid - NCW, mem, r, ln, wid == NCW);
        if (blockIdx.x == 0 && wid == NCW) {
#pragma unroll 1
            for (unsigned spins = 0; spins < SPIN_CAP; ++spins) {
                const u32x4 v = ld16(r, ln * 16, WS_START);
                if (__all(v.x == ep && v.y == ep && v.z == ep && v.w == ep)) { if (ln == 0) __builtin_amdgcn_raw_buffer_store_b32(ep, r, 0, WS_EPOCH, SC1); break; }
                __builtin_amdgcn_s_sleep(8);
            }
        }
        STAMPV(0, 1); STAMP_DUMP(); return;
    }

    const int cb = mem >> 1, kh = wid & 1, kq = wid & 3;
    const int tb1 = 4 * team + (wid >> 1);
    const int tb2 = 4 * team + 2 * (wid >> 2) + (mem & 1);
    const rsrc_t rx = __builtin_amdgcn_make_buffer_rsrc((void*)p.x, 0, S * D * 2, 0x00020000);
    unsigned ep = 0;

    for (int layer = 0; layer < 2; ++layer) {
        const unsigned wl = opq(WS_LAYER + layer * L_SIZE), fl = opq(WS_FLAGS + layer * FL_LAYER);
        if (layer) STAMPV(layer, 0);
        {
            lane = opqv(lane);
            float sx, sq;
            f32x16 acc[3], acs; bf16x8 b[16]; f32x4 pr[4];
#pragma unroll
            for (int t = 0; t < 3; ++t) acc[t] = (f32x16){};
            if (layer == 0) {
                const unsigned epv = __builtin_amdgcn_raw_buffer_load_b32(r, 0, WS_EPOCH, SC1);
                u32x4 xr[16];
                const unsigned vo = (lane >> 4) * (D * 2) + (lane & 15) * 16, so = opq((tb1 * 32 * D + 256 * kh) * 2);
#pragma unroll
                for (int i = 0; i < 16; ++i) xr[i] = ldw(rx, vo + (i & 7) * 4 * (D * 2) + (i >> 3) * 256, so);
                ep = rfl(epv) + 1u;
                lds_signal(lds0 + C_ATT, lane);
                STAMPV(1, 22);
                LAS unsigned char* xt = L + LD_XA + wid * 8192;
                float s1 = 0.f, s2 = 0.f;
#pragma unroll
                for (int hf = 0; hf < 2; ++hf) {
#pragma unroll
                    for (int j = 0; j < 8; ++j) { const int row = 4 * j + (lane >> 4); *(LAS u32x4*)(xt + row * 256 + (((lane & 15) ^ (row & 15)) * 16)) = xr[8 * hf + j];
#pragma unroll
                        for (int e = 0; e < 4; ++e) asm volatile("" :: "v"(xr[8 * hf + j][e])); }
#pragma unroll
                    for (int s = 0; s < 8; ++s) {
                        const u32x2 lo = *(LAS const u32x2*)(xt + (lane & 31) * 256 + (((2 * s) ^ (lane & 15)) * 16) + (lane >> 5) * 8), hi = *(LAS const u32x2*)(xt + (lane & 31) * 256 + (((2 * s + 1) ^ (lane & 15)) * 16) + (lane >> 5) * 8);
                        b[8 * hf + s] = __builtin_bit_cast(bf16x8, (u32x4){lo.x, lo.y, hi.x, hi.y});
                        s1 = dot2(lo.x, 0x3f803f80u, dot2(lo.y, 0x3f803f80u, dot2(hi.x, 0x3f803f80u, dot2(hi.y, 0x3f803f80u, s1))));
                        s2 = dot2(lo.x, lo.x, dot2(lo.y, lo.y, dot2(hi.x, hi.x, dot2(hi.y, hi.y, s2))));
                    }
                }
                const float ml = s1 * (1.f / 128.f), m2l = __builtin_fmaxf(s2 - s1 * ml, 0.f) * 0.125f;
#pragma unroll
                for (int i = 0; i < 4; ++i) pr[i] = (f32x4){ml, m2l, ml, m2l};
                STAMPV(1, 23);
            } else {
                wait_flags(r, WS_FLAGS + FL_X2 + tb1 * 32 * WSTR, 0xFFFFu << (16 * kh), lane, ep);
                const unsigned so = opq(WS_LAYER + L_X2F + (tb1 * 32 + 16 * kh) * KB);
#pragma unroll
                for (int s = 0; s < 16; ++s) { b[s] = ldf(r, lane * 16, so + s * KB); if (s == 3) stats_issue(r, WS_LAYER + L_ST2, tb1, kh, lane, pr); }
            }
            STAMPV(layer, 1);
            stats_half(pr, sx, sq); asm volatile("" : "+v"(sx), "+v"(sq));
            {
                const unsigned gneed = lane == 0 ? 8u * (layer + 1) : (lane == 1 + (wid >> 2) ? 4u * (4 * layer) : (lane < 3 && layer ? 4u * (4 * layer - 1) : 0u));
                lds_wait_vec(L, lane == 0 ? C_ATT : C_XRD + ((lane - 1) & 1) * 4, gneed, r, lane);
            }
            int wa[3];
#pragma unroll
            for (int t = 0; t < 3; ++t) wa[t] = kh * 6144 + (4 * (lane >> 5) + ((lane & 15) >> 2)) * 192 + t * 64 + ((lane >> 4) & 1) * 32 + (lane & 3) * 8;
            STAMPW(layer, 24, gemm_stage<0, 8, 3, 2, 192, 16, 0, true, true, false>(wid, lds, acc, b, wa, acs, layer == 0, wid >> 1, pr, sx, sq, L, lds0, layer, r, 0, lane));
            lane = opqv(lane);
            if (layer == 0 && (wid >> 1) < 3) { cs_store(acs, (LAS float*)(L + LD_CSQ) + 96 * kh + 32 * (wid >> 1), lane); lds_signal(lds0 + C_CS, lane); }
            STAMPV(layer, 2);
            lane = opqv(lane);
            if (lane < 32) ((LAS f32x2*)(L + LD_ST))[wid * 32 + lane] = (f32x2){sx, sq};
            const int blk = 4 * team + (wid >> 1), kb = 8 + (wid >> 1);
            const unsigned kvo = wl + L_KV + (blk * 16 + mem) * 4 * KB;
            if (kh == 0) { asm volatile("; kh 0"); qkv_epi<0>(acc, sx, sq, L, lds0, wid, lane, layer, kb, r, kvo); asm volatile("; kh 0 end"); }
            else { asm volatile("; kh 1"); qkv_epi<1>(acc, sx, sq, L, lds0, wid, lane, layer, kb, r, kvo); asm volatile("; kh 1 end"); }
            lds_signal(lds0 + C_LOCAL + (wid >> 1) * 4, lane);
            STAMPV(layer, 3);
            publish(r, fl + FL_KV + ((blk * 16 + mem) * 2 + kh) * WSTR, lane, ep);
            STAGE_BAR(1);
            STAMPV(layer, 4);
        }
        {
            lane = opqv(lane);
            const int gb = 4 * team - 8 + wid;
            if (gb >= 0) {
                wait_flags(r, fl + FL_KV + (gb * 16 + mem) * 2 * WSTR, 3u, lane, ep);
                STAMPV(layer, 5);
                u32x4 kv[4];
                const unsigned so = opq(wl + L_KV + (gb * 16 + mem) * 4 * KB);
#pragma unroll
                for (int i = 0; i < 4; ++i) kv[i] = ld16(r, lane * 16, so + i * KB);
                lds_wait_vec(L, C_XRD + (lane & 1) * 4, 4u * (4 * layer + 1), r, lane);
#pragma unroll
                for (int i = 0; i < 2; ++i) {
                    *(LAS u32x4*)(L + LD_K + (wid * 2 + i) * 1024 + lane * 16) = kv[i];
                    *(LAS u32x4*)(L + LD_V + (wid * 2 + i) * 1024 + lane * 16) = kv[2 + i];
                }
            }
            lds_signal(lds0 + C_HALO + wid * 4, lane);
            const int qb = wid >> 1, hd = wid & 1;
            STAMPV(layer, 6);
            {
                const bool isl = lane < 4, ish = lane >= 8 && lane < 16;
                const unsigned need = isl ? (lane <= qb ? 2u * (layer + 1) : 0u) : (ish && (lane - 8) >= qb ? (unsigned)(layer + 1) : 0u);
                lds_wait_vec(L, C_LOCAL + (lane & 15) * 4, need, r, lane);
            }
            STAMPV(layer, 7);
            f32x16 o;
            if (team >= 2) attn_unit<true>(L, team, qb, hd, lane, o); else attn_unit<false>(L, team, qb, hd, lane, o);
            STAMPV(layer, 8);
            lane = opqv(lane);
            lds_add1(lds0 + C_ATT, lane);
            const float rl = __builtin_amdgcn_rcpf(hd ? o[0] : o[8]);
            float v[8];
#pragma unroll
            for (int e = 0; e < 8; ++e) v[e] = (hd ? o[8 + e] : o[e]) * rl;
            st16(r, lane * 16, wl + L_OF + ((4 * team + qb) * 32 + 2 * mem + hd) * KB, pack8(v));
            publish(r, fl + FL_O + ((4 * team + qb) * 32 + 2 * mem + hd) * WSTR, lane, ep);
            STAGE_BAR(2);
            STAMPV(layer, 9);
        }
        {
            lane = opqv(lane);
            wait_flags(r, fl + FL_O + tb2 * 32 * WSTR, 0xFFu << (8 * kq), lane, ep);
            STAMPV(layer, 10);
            f32x16 acc[2], acs; bf16x8 b[9]; f32x4 pr[4]; float sx, sq;
            acc[0] = (f32x16){}; acc[1] = (f32x16){};
            int wa[2]; n64_addr(wa, kq, lane);
            u32x4 res;
            if (layer == 0) { const unsigned vo = (lane & 31) * (D * 2) + (lane >> 5) * 8, so = (tb2 * 32 * D + 64 * cb + 16 * kq) * 2;
                const u32x2 a = __builtin_amdgcn_raw_buffer_load_b64(rx, vo, so, 0), c = __builtin_amdgcn_raw_buffer_load_b64(rx, vo + 16, so, 0); res = (u32x4){a.x, a.y, c.x, c.y}; }
            else res = ld16(r, lane * 16, WS_LAYER + L_X2F + (tb2 * 32 + 4 * cb + kq) * KB);
            STAMPW(layer, 25, gemm_stage<8, 4, 2, 99, 128, 9, 0, false, false, false>(wid, lds, acc, b, wa, acs, false, 0, pr, sx, sq, L, lds0, layer, r, opq(wl + L_OF + (tb2 * 32 + 8 * kq) * KB), lane));
            STAMPV(layer, 11);
            lane = opqv(lane);
            lds_wait_vec(L, (lane & 1) ? C_ATT : C_XRD + (wid >> 2) * 4, (lane & 1) ? 8u * (layer + 2) : 4u * (4 * layer + 1), r, lane);
            float v[8]; kq_reduce(acc, L, lds0, wid, lane, 4 * layer + 2, r, v);
            resid_add(v, res);
            st16(r, lane * 16, wl + L_X1F + (tb2 * 32 + 4 * cb + kq) * KB, res);
            stats_out(res, r, wl + L_ST1, tb2, 4 * cb + kq, lane);
            STAMPV(layer, 12);
            publish(r, fl + FL_X1 + (tb2 * 32 + 4 * cb + kq) * WSTR, lane, ep);
            STAGE_BAR(3);
            STAMPV(layer, 13);
        }
        {
            lane = opqv(lane);
            wait_flags(r, fl + FL_X1 + tb1 * 32 * WSTR, 0xFFFFu << (16 * kh), lane, ep);
            STAMPV(layer, 14);
            f32x16 acc[4];
#pragma unroll
            for (int t = 0; t < 4; ++t) acc[t] = (f32x16){};
            int wa[4];
            {
                const int q4 = (lane & 15) >> 2;
#pragma unroll
                for (int t = 0; t < 4; ++t) wa[t] = kh * 8192 + (4 * (lane >> 5) + q4) * 256 + (((t ^ (2 * kh)) ^ q4) * 64) + ((lane >> 4) & 1) * 32 + (lane & 3) * 8;
            }
            const unsigned so = opq(wl + L_X1F + (tb1 * 32 + 16 * kh) * KB);
            float sx, sq; f32x4 pr[4]; f32x16 acs; bf16x8 b[RB_F1];
            stats_issue(r, wl + L_ST1, tb1, kh, lane, pr);
            STAMPW(layer, 26, gemm_stage<12, 8, 4, 99, 256, RB_F1, 0, false, true, true>(wid, lds, acc, b, wa, acs, layer == 0, wid >> 1, pr, sx, sq, L, lds0, layer, r, so, lane));
            lane = opqv(lane);
            if (layer == 0) { cs_store(acs, (LAS float*)(L + LD_CS1) + 128 * kh + 32 * ((wid >> 1) ^ (2 * kh)), lane); lds_signal(lds0 + C_CS, lane); }
            STAMPV(layer, 15);
            lane = opqv(lane); const int t32 = lane & 31, h = lane >> 5;
            lds_wait_ge(L, C_XRD + (wid >> 2) * 4, 4u * (4 * layer + 2), r, lane);
            xch_put(L, wid, 0, lane, acc[2], 0); xch_put(L, wid, 1, lane, acc[2], 1); xch_put(L, wid, 2, lane, acc[3], 0); xch_put(L, wid, 3, lane, acc[3], 1);
            if (h == 0) ((LAS f32x2*)(L + LD_ST))[wid * 32 + t32] = (f32x2){sx, sq};
            lds_signal(lds0 + C_XF + wid * 4, lane);
            if (layer == 0) lds_wait_ge(L, C_CS, 14u, r, lane);
            LAS const float* csl = (LAS const float*)(L + LD_CS1) + 64 * kh;
            f32x4 cs[8];
#pragma unroll
            for (int i = 0; i < 8; ++i) cs[i] = *(LAS const f32x4*)(csl + 8 * i + 4 * h) + *(LAS const f32x4*)(csl + 128 + 8 * i + 4 * h);
            lds_wait_ge(L, C_XF + (wid ^ 1) * 4, 4u * layer + 3, r, lane);
            const f32x2 os = ((LAS const f32x2*)(L + LD_ST))[(wid ^ 1) * 32 + t32];
            xch_add(L, wid ^ 1, 0, lane, acc[0], 0); xch_add(L, wid ^ 1, 1, lane, acc[0], 1); xch_add(L, wid ^ 1, 2, lane, acc[1], 0); xch_add(L, wid ^ 1, 3, lane, acc[1], 1);
            lds_signal(lds0 + C_XRD + (wid >> 2) * 4, lane);
            float la, lb; ln_coef(os, sx, sq, la, lb);
            if (mem < 14) {
#pragma unroll
                for (int i = 0; i < 4; ++i) {
                    float hv[8], ex[8];
#pragma unroll
                    for (int e = 0; e < 8; ++e) { hv[e] = la * acc[i >> 1][8 * (i & 1) + e] + lb * cs[2 * i + (e >> 2)][e & 3]; ex[e] = -LOG2E * hv[e]; }
                    SB();
#pragma unroll
                    for (int e = 0; e < 8; ++e) ex[e] = __builtin_amdgcn_exp2f(ex[e]);
                    SB();
#pragma unroll
                    for (int e = 0; e < 8; ++e) ex[e] += 1.f;
                    SB();
#pragma unroll
                    for (int e = 0; e < 8; ++e) ex[e] = __builtin_amdgcn_rcpf(ex[e]);
                    SB();
#pragma unroll
                    for (int e = 0; e < 8; ++e) hv[e] *= ex[e];
                    st16(r, lane * 16, wl + L_HF + (tb1 * 112 + 8 * mem + 4 * kh + i) * KB, pack8(hv));
                    SB();
                }
                STAMPV(layer, 16);
                publish(r, fl + FL_H + (tb1 * 32 + 2 * mem + kh) * WSTR, lane, ep);
            }
            STAGE_BAR(4);
            STAMPV(layer, 17);
        }
        {
            lane = opqv(lane);
            wait_flags(r, fl + FL_H + tb2 * 32 * WSTR, (0x5555555u << (kq >> 1)) & 0xFFFFFFFu, lane, ep);
            STAMPV(layer, 18);
            f32x16 acc[2], acs; bf16x8 b[RB_F2]; f32x4 pr[4]; float sx, sq;
            acc[0] = (f32x16){}; acc[1] = (f32x16){};
            int wa[2]; n64_addr(wa, kq, lane);
            STAMPW(layer, 27, gemm_stage<20, 14, 2, 99, 128, RB_F2, 1, false, false, false>(wid, lds, acc, b, wa, acs, false, 0, pr, sx, sq, L, lds0, layer, r, opq(wl + L_HF + (tb2 * 112 + 2 * kq) * KB), lane));
            STAMPV(layer, 19);
            lane = opqv(lane);
            u32x4 res = ld16(r, lane * 16, wl + L_X1F + (tb2 * 32 + 4 * cb + kq) * KB);
            lds_wait_ge(L, C_XRD + (wid >> 2) * 4, 4u * (4 * layer + 3), r, lane);
            float v[8]; kq_reduce(acc, L, lds0, wid, lane, 4 * layer + 4, r, v);
            resid_add(v, res);
            if (layer == 0) {
                st16(r, lane * 16, wl + L_X2F + (tb2 * 32 + 4 * cb + kq) * KB, res);
                stats_out(res, r, wl + L_ST2, tb2, 4 * cb + kq, lane);
                STAMPV(layer, 20);
                publish(r, fl + FL_X2 + (tb2 * 32 + 4 * cb + kq) * WSTR, lane, ep);
                STAGE_BAR(5);
            } else {
                auto s0 = __builtin_amdgcn_permlane32_swap(res.x, res.z, false, false), s1 = __builtin_amdgcn_permlane32_swap(res.y, res.w, false, false);
                unsigned short* dst = p.out + (size_t)(32 * tb2 + (lane & 31)) * D + 64 * cb + 16 * kq + 8 * (lane >> 5);
                *(u32x4*)dst = (u32x4){s0[0], s1[0], s0[1], s1[1]};
            }
            STAMPV(layer, 21);
        }
    }
    STAMP_RT(1); STAMP_DUMP();
}

extern "C" void kernel_launch(void* const* d_in, const int* in_sizes, int n_in, void* d_out, int out_size, void* d_ws, size_t ws_size, hipStream_t stream) {
    Params p;
    p.x = (const unsigned short*)d_in[0]; p.wqkv = (const unsigned short*)d_in[1]; p.wo = (const unsigned short*)d_in[2];
    p.w1 = (const unsigned short*)d_in[4]; p.w2 = (const unsigned short*)d_in[5];
    p.out = (unsigned short*)d_out; p.ws = (unsigned char*)d_ws;
    (void)hipFuncSetAttribute((const void*)k_main, hipFuncAttributeMaxDynamicSharedMemorySize, LDS_BYTES);
    k_main<<<256, NTHR, LDS_BYTES, stream>>>(p);
}
```

```cpp
#include <hip/hip_runtime.h>
#include <hip/hip_bf16.h>
#include <stdint.h>

typedef short bf16x8 __attribute__((ext_vector_type(8)));
typedef float f32x2 __attribute__((ext_vector_type(2)));
typedef float f32x4 __attribute__((ext_vector_type(4)));
typedef float f32x8 __attribute__((ext_vector_type(8)));
typedef float f32x16 __attribute__((ext_vector_type(16)));
typedef unsigned u32x2 __attribute__((ext_vector_type(2)));
typedef unsigned u32x4 __attribute__((ext_vector_type(4)));
typedef __bf16 bf16x2_t __attribute__((ext_vector_type(2)));
#define GAS __attribute__((address_space(1)))
#define LAS __attribute__((address_space(3)))
#define DEV __device__ __forceinline__
#define SB() __builtin_amdgcn_sched_barrier(0)
typedef __amdgpu_buffer_rsrc_t rsrc_t;

constexpr int S = 2048, D = 512, H = 32, F = 1792;
constexpr float EPS = 1e-5f;
constexpr float LOG2E = 1.4426950408889634f;
constexpr float QSCALE = 0.25f * LOG2E;

constexpr unsigned KB = 1024, MB = 1024 * 1024;
constexpr unsigned WS_EPOCH = 256;
constexpr unsigned WS_START = 4096;
constexpr unsigned WS_LAYER = 64 * KB;
constexpr unsigned L_KV  = 0;
constexpr unsigned L_OF  = L_KV + 4 * MB;
constexpr unsigned L_X1F = L_OF + 2 * MB;
constexpr unsigned L_HF  = L_X1F + 2 * MB;
constexpr unsigned L_X2F = L_HF + 7 * MB;
constexpr unsigned L_ST1 = L_X2F + 2 * MB;
constexpr unsigned L_ST2 = L_ST1 + 512 * KB;
constexpr unsigned L_SIZE = L_ST2 + 512 * KB;
constexpr unsigned WSTR = 128;
constexpr unsigned WS_FLAGS = 64 * MB;
constexpr unsigned FL_KV = 0, FL_O = FL_KV + 64 * 16 * 2 * WSTR, FL_X1 = FL_O + 64 * 32 * WSTR, FL_H = FL_X1 + 64 * 32 * WSTR, FL_X2 = FL_H + 64 * 32 * WSTR, FL_LAYER = FL_X2 + 64 * 32 * WSTR;
constexpr unsigned WS_END = 72 * MB;
static_assert(WS_LAYER + 2 * L_SIZE <= WS_FLAGS && WS_FLAGS + 2 * FL_LAYER <= WS_END, "workspace map");

constexpr int NCW = 8, NLW = 4, NTHR = 64 * (NCW + NLW);
constexpr int SLOT = 16384, NSLOT = 5;
constexpr int LD_RING = 0;
constexpr int LD_XA = 80 * 1024;
constexpr int LD_K = 96 * 1024;
constexpr int LD_V = 120 * 1024;
constexpr int LD_XB = LD_V;
constexpr int LD_XC0 = LD_K + 16 * 1024, LD_XC1 = LD_V + 16 * 1024;
constexpr int LD_Q = 144 * 1024;
constexpr int LD_MISC = 152 * 1024;
constexpr int LD_CSQ = LD_MISC  , LD_CS1 = LD_MISC + 768  , LD_ONES = LD_MISC + 1792, LD_CTR = LD_MISC + 1856, LD_DUMMY = LD_CTR + 256  ,
              LD_ST = LD_DUMMY + 512  , LD_AB = LD_ST + 2048  , LD_STAMP = LD_AB + 1024;
constexpr int C_FULL = LD_CTR  , C_FREE = LD_CTR + 32  , C_LOCAL = LD_CTR + 64, C_HALO = LD_CTR + 96, C_XF = LD_CTR + 128, C_XRD = LD_CTR + 160, C_ATT = LD_CTR + 176, C_BAR = LD_CTR + 180, C_CS = LD_CTR + 184;
constexpr int LDS_BYTES = 160 * 1024;
static_assert(LD_STAMP + 8 * 64 * 4 <= LDS_BYTES && LD_CTR % 16 == 0 && LD_ST % 16 == 0, "LDS map");
constexpr int slot_of(int c) { return c % NSLOT; }
constexpr int NPOS = 35;

struct Params {
    const unsigned short* x; const unsigned short* wqkv; const unsigned short* wo; const unsigned short* w1; const unsigned short* w2;
    unsigned short* out; unsigned char* ws;
};

#define STAMPV(kid, i)
#define STAMPW(kid, i, ...) (void)(__VA_ARGS__)
#define STAMP_RT(i)
#define STAMP_DUMP()
#define STAMP_INIT()

DEV float bf2f(unsigned short b) { return __uint_as_float((unsigned)b << 16); }
DEV float bflo(unsigned u) { return __uint_as_float(u << 16); }
DEV float bfhi(unsigned u) { return __uint_as_float(u & 0xffff0000u); }
DEV unsigned pk2(float lo, float hi) { f32x2 v = {lo, hi}; bf16x2_t r = __builtin_convertvector(v, bf16x2_t); return __builtin_bit_cast(unsigned, r); }
DEV float xsum32(float v) { auto r = __builtin_amdgcn_permlane32_swap(__float_as_uint(v), __float_as_uint(v), false, false); return __uint_as_float(r[0]) + __uint_as_float(r[1]); }
DEV float xmax32(float v) { auto r = __builtin_amdgcn_permlane32_swap(__float_as_uint(v), __float_as_uint(v), false, false); return __builtin_fmaxf(__uint_as_float(r[0]), __uint_as_float(r[1])); }
DEV f32x16 mfma32(bf16x8 a, bf16x8 b, f32x16 c) { return __builtin_amdgcn_mfma_f32_32x32x16_bf16(a, b, c, 0, 0, 0); }
DEV float dot2(unsigned a, unsigned b, float c) { return __builtin_amdgcn_fdot2_f32_bf16(__builtin_bit_cast(bf16x2_t, a), __builtin_bit_cast(bf16x2_t, b), c, false); }
DEV unsigned opq(unsigned x) { asm volatile("" : "+s"(x)); return x; }
DEV int opqv(int x) { asm volatile("" : "+v"(x)); return x; }
DEV unsigned rfl(unsigned v) { return __builtin_amdgcn_readfirstlane(v); }
#define WAITV(n) asm volatile("s_waitcnt vmcnt(" #n ")" ::: "memory")
constexpr int SC1 = 16;
DEV bf16x8 ldf(rsrc_t r, unsigned vo, unsigned so) { return __builtin_bit_cast(bf16x8, __builtin_amdgcn_raw_buffer_load_b128(r, vo, so, SC1)); }
DEV u32x4 ld16(rsrc_t r, unsigned vo, unsigned so) { return __builtin_amdgcn_raw_buffer_load_b128(r, vo, so, SC1); }
DEV u32x4 ldw(rsrc_t r, unsigned vo, unsigned so) { return __builtin_amdgcn_raw_buffer_load_b128(r, vo, so, 0); }
DEV void st16(rsrc_t r, unsigned vo, unsigned so, u32x4 v) { __builtin_amdgcn_raw_buffer_store_b128(v, r, vo, so, SC1); asm volatile("s_nop 1" :: "v"(v) : "memory"); }

DEV void block_map(int b, int& team, int& mem) {
    const int x = b & 7, r = b >> 3;
    team = 4 * (x >> 1) + (r >> 3); mem = 8 * (x & 1) + (r & 7);
}

#define RB_F1 5
#define RB_F2 12
#define GATE_Q 31
#define WPRIO 3
#define FPRIO 0
#define POLL_SLEEP 1
constexpr unsigned SPIN_CAP = 1u << 15;
DEV void wait_flags(rsrc_t r, unsigned off, unsigned mask, int lane, unsigned ep) {
    bool need = lane < 32 && ((mask >> (lane & 31)) & 1u);
#pragma unroll 1
    for (unsigned spins = 0;; ++spins) {
        if (need) need = __builtin_amdgcn_raw_buffer_load_b32(r, lane * WSTR, off, SC1) != ep;
        if (!__any(need)) break;
        if (spins > SPIN_CAP) break;
        __builtin_amdgcn_s_sleep(POLL_SLEEP);
    }
    __builtin_amdgcn_fence(__ATOMIC_ACQUIRE, "wavefront");
}
DEV void publish(rsrc_t r, unsigned word_off, int lane, unsigned ep) {
    WAITV(0);
    if (lane == 0) __builtin_amdgcn_raw_buffer_store_b32(ep, r, 0, word_off, SC1);
}
DEV unsigned lds_ld(LAS unsigned char* L, int off) { return *(volatile LAS unsigned*)(L + off); }
DEV int s2v(int x) { int v; asm volatile("v_mov_b32 %0, %1" : "=v"(v) : "s"(x)); return v; }
DEV unsigned lds_ldu(LAS unsigned char* L, int off) { return *(volatile LAS unsigned*)(L + s2v(off)); }
DEV void lds_add1(unsigned addr, int lane) {
    const unsigned a = lane == 0 ? addr : (unsigned)(LD_DUMMY + lane * 4);
    asm volatile("ds_add_u32 %0, %1" :: "v"(a), "v"(1u) : "memory");
}
DEV void lds_signal(unsigned addr, int lane) { lds_add1(addr, lane); }
DEV void lds_wait_ge(LAS unsigned char* L, int off, unsigned target, rsrc_t r, int lane) {
    if (__builtin_expect(rfl(lds_ldu(L, off)) < target, 0)) {
#pragma unroll 1
        for (unsigned spins = 0; rfl(lds_ldu(L, off)) < target && spins < (SPIN_CAP << 3); ++spins) if (spins > 1) __builtin_amdgcn_s_sleep(1);
    }
    asm volatile("" ::: "memory");
}
DEV void lds_wait_vec(LAS unsigned char* L, int off, unsigned need, rsrc_t r, int lane) {
    if (__builtin_expect(!__all(lds_ld(L, off) >= need), 0)) {
#pragma unroll 1
        for (unsigned spins = 0; !__all(lds_ld(L, off) >= need) && spins < (SPIN_CAP << 3); ++spins) if (spins > 1) __builtin_amdgcn_s_sleep(1);
    }
    asm volatile("" ::: "memory");
}

DEV void lds_set(unsigned addr, unsigned val, int lane) {
    const unsigned a = lane == 0 ? addr : (unsigned)(LD_DUMMY + lane * 4);
    asm volatile("ds_write_b32 %0, %1" :: "v"(a), "v"(val) : "memory");
}
DEV unsigned qmin(unsigned v) {
    v = min(v, (unsigned)__builtin_amdgcn_mov_dpp((int)v, 0xB1, 0xF, 0xF, true));
    return min(v, (unsigned)__builtin_amdgcn_mov_dpp((int)v, 0x4E, 0xF, 0xF, true));
}
DEV unsigned ring_ready(LAS unsigned char* L, unsigned pos, int lane) {
    unsigned n;
#pragma unroll 1
    for (unsigned spins = 0;; ++spins) {
        n = rfl(qmin(lds_ld(L, C_FULL + (lane & 3) * 4)));
        if (n > pos || spins > (SPIN_CAP << 3)) break;
        if (spins > 2) __builtin_amdgcn_s_sleep(1);
    }
    asm volatile("" ::: "memory");
    return n;
}
DEV void stats_issue(rsrc_t r, unsigned st, int tb, int kh, int lane, f32x4 (&pr)[4]) {
    const unsigned so = opq(st + (tb * 16 + 8 * kh) * 512);
#pragma unroll
    for (int i = 0; i < 4; ++i) pr[i] = __builtin_bit_cast(f32x4, ld16(r, ((lane >> 5) * 4 * 32 + (lane & 31)) * 16 + i * 512, so));
}
DEV void stats_half(const f32x4 (&pr)[4], float& mean, float& m2) {
    float ms = 0.f;
#pragma unroll
    for (int i = 0; i < 4; ++i) ms += pr[i][0] + pr[i][2];
    mean = xsum32(ms) * (1.f / 16.f);
    float q = 0.f;
#pragma unroll
    for (int i = 0; i < 4; ++i) { const float d0 = pr[i][0] - mean, d1 = pr[i][2] - mean; q += pr[i][1] + pr[i][3] + 16.f * (d0 * d0 + d1 * d1); }
    m2 = xsum32(q);
}
DEV void stats_out(u32x4 o, rsrc_t r, unsigned st, int tb, int s, int lane) {
    const float q8[8] = {bflo(o.x), bfhi(o.x), bflo(o.y), bfhi(o.y), bflo(o.z), bfhi(o.z), bflo(o.w), bfhi(o.w)};
    const float mean = xsum32(((q8[0] + q8[1]) + (q8[2] + q8[3])) + ((q8[4] + q8[5]) + (q8[6] + q8[7]))) * (1.f / 16.f);
    float q = 0.f;
#pragma unroll
    for (int e = 0; e < 8; ++e) { const float d = q8[e] - mean; q += d * d; }
    q = xsum32(q);
    if (lane < 32) __builtin_amdgcn_raw_buffer_store_b64((u32x2){__float_as_uint(mean), __float_as_uint(q)}, r, lane * 16 + (s & 1) * 8, st + (tb * 16 + (s >> 1)) * 512, SC1);
}

typedef short v4i16 __attribute__((ext_vector_type(4)));
DEV v4i16 trd(LAS const unsigned char* p) { return __builtin_amdgcn_ds_read_tr16_b64_v4i16((LAS v4i16*)p); }
template <int C0, int NCH, int NT, int SWAPV, int P, int RB, int AMODE, bool PRE, bool CS, bool ST>
DEV unsigned gemm_stage(int wid, unsigned char* lds, f32x16 (&acc)[NT], bf16x8 (&b)[RB], const int (&wa)[NT], f32x16& acs, bool cs_on, int cst, const f32x4 (&pr)[4], float& sx, float& sq,
                        LAS unsigned char* L, unsigned lds0, int layer, rsrc_t r, unsigned so, int lane) {
    unsigned twait = 0;
    constexpr int NS = 2 * NCH, JS = 16 * P, TAU = 8 * P;
    const unsigned l16 = lane * 16;
#define AOFF(s_) (AMODE == 0 ? (s_) * KB : (((s_) >> 1) * 8 + ((s_) & 1)) * KB)
    if (!PRE) {
#pragma unroll
        for (int s = 0; s < RB - 1; ++s) if (s < NS) b[s] = ldf(r, l16, so + AOFF(s));
    }
    LAS const unsigned char* w0[NT]; LAS const unsigned char* w1[NT];
#pragma unroll
    for (int t = 0; t < NT; ++t) { w0[t] = L + LD_RING + wa[t]; w1[t] = L + opqv(LD_RING + 4 * SLOT + wa[t]); }
    const unsigned fadr = lane == 0 ? lds0 + C_FREE + wid * 4 : (unsigned)(LD_DUMMY + lane * 4);
    const unsigned pos0 = NPOS * layer + C0;
#define WADDR(s_, t_) ((slot_of(C0 + ((s_) >> 1)) == 4 ? w1[t_] : w0[t_] + slot_of(C0 + ((s_) >> 1)) * SLOT) + ((s_) & 1) * JS)
#define WFRAG(s_, t_) __builtin_shufflevector(trd(WADDR(s_, t_)), trd(WADDR(s_, t_) + TAU), 0, 1, 2, 3, 4, 5, 6, 7)
    bf16x8 ones;
    if (CS) { const unsigned one2 = (unsigned)s2v(0x3f803f80); ones = __builtin_bit_cast(bf16x8, (u32x4){one2, one2, one2, one2});
        const float z = __uint_as_float((unsigned)s2v(0));
#pragma unroll
        for (int e = 0; e < 16; ++e) acs[e] = z; }
    unsigned ready = ring_ready(L, pos0, lane);
    bf16x8 fa[NT];
#pragma unroll
    for (int t = 0; t < NT; ++t) fa[t] = WFRAG(0, t);
    SB();
#pragma unroll
    for (int s = 0; s < NS; ++s) {
        if (!PRE && s + RB - 1 < NS) b[(s + RB - 1) % RB] = ldf(r, l16, so + AOFF(s + RB - 1));
        const int sp = s + 1;
        if (sp < NS && (sp & 1) == 0) { if (__builtin_expect(pos0 + (sp >> 1) >= ready, 0)) {
            ready = ring_ready(L, pos0 + (sp >> 1), lane);
        } }
        if (CS) { if (cs_on) {
            u32x4 f = __builtin_bit_cast(u32x4, fa[0]);
#pragma unroll
            for (int t = 1; t < NT; ++t) { const u32x4 g = __builtin_bit_cast(u32x4, fa[t]);
#pragma unroll
                for (int e = 0; e < 4; ++e) f[e] = cst == t ? g[e] : f[e]; }
            acs = mfma32(__builtin_bit_cast(bf16x8, f), ones, acs);
        } }
        SB();
#pragma unroll
        for (int t = 0; t < NT; ++t) {
            if (t >= SWAPV) acc[t] = mfma32(b[s % RB], fa[t], acc[t]); else acc[t] = mfma32(fa[t], b[s % RB], acc[t]);
            if (sp < NS) fa[t] = WFRAG(sp, t);
            SB();
        }
        if (s & 1) { const unsigned rel = (unsigned)s2v((int)(pos0 + (s >> 1) + 1)); asm volatile("ds_write_b32 %0, %1" :: "v"(fadr), "v"(rel) : "memory"); }
        if (ST && s == 3) { stats_half(pr, sx, sq); asm volatile("" : "+v"(sx), "+v"(sq)); }
        if (s == 0) STAMPV(layer, C0 == 0 ? 28 : C0 == 8 ? 29 : C0 == 12 ? 30 : 31);
    }
#undef AOFF
#undef WADDR
#undef WFRAG
    return twait;
}
DEV int xch_off(int w, int slot) { return slot < 2 ? LD_XA + w * 4096 + slot * 2048 : slot == 2 ? LD_XB + w * 2048 : (w < 4 ? LD_XC0 + w * 2048 : LD_XC1 + (w - 4) * 2048); }
DEV void xch_put(LAS unsigned char* L, int w, int slot, int lane, const f32x16& a, int half) {
    LAS f32x4* d = (LAS f32x4*)(L + xch_off(w, slot)) + lane;
    d[0] = (f32x4){a[8 * half], a[8 * half + 1], a[8 * half + 2], a[8 * half + 3]}; d[64] = (f32x4){a[8 * half + 4], a[8 * half + 5], a[8 * half + 6], a[8 * half + 7]};
}
DEV void xch_add(LAS unsigned char* L, int w, int slot, int lane, f32x16& a, int half) {
    LAS const f32x4* d = (LAS const f32x4*)(L + xch_off(w, slot)) + lane;
    const f32x4 u = d[0], v = d[64];
#pragma unroll
    for (int e = 0; e < 4; ++e) { a[8 * half + e] += u[e]; a[8 * half + 4 + e] += v[e]; }
}
DEV void ln_coef(f32x2 o, float mean_h, float m2_h, float& la, float& lb) {
    const float mean = 0.5f * (mean_h + o.x), d = mean_h - o.x;
    const float var = (m2_h + o.y + 128.f * d * d) * (1.f / 512.f);
    la = __builtin_amdgcn_rsqf(var + EPS); lb = -mean * la;
}
DEV void kq_reduce(f32x16 (&acc)[2], LAS unsigned char* L, unsigned lds0, int wid, int lane, unsigned seq, rsrc_t r, float (&v)[8]) {
    const int base = wid & 4;
    xch_put(L, wid, 0, lane, acc[0], 1); xch_put(L, wid, 1, lane, acc[1], 0); xch_put(L, wid, 2, lane, acc[1], 1);
    lds_signal(lds0 + C_XF + wid * 4, lane);
    lds_wait_vec(L, C_XF + (base + (lane & 3)) * 4, seq, r, lane);
#pragma unroll
    for (int x = 1; x < 4; ++x) xch_add(L, wid ^ x, x - 1, lane, acc[0], 0);
    lds_signal(lds0 + C_XRD + (wid >> 2) * 4, lane);
#pragma unroll
    for (int e = 0; e < 8; ++e) v[e] = acc[0][e];
}
DEV u32x4 pack8(const float (&f)[8]) { return (u32x4){pk2(f[0], f[1]), pk2(f[2], f[3]), pk2(f[4], f[5]), pk2(f[6], f[7])}; }
DEV void resid_add(const float (&v)[8], u32x4& res) {
    const float f[8] = {bflo(res.x) + v[0], bfhi(res.x) + v[1], bflo(res.y) + v[2], bfhi(res.y) + v[3], bflo(res.z) + v[4], bfhi(res.z) + v[5], bflo(res.w) + v[6], bfhi(res.w) + v[7]};
    res = pack8(f);
}

template <int KH>
DEV void qkv_epi(f32x16 (&acc)[3], float sx, float sq, LAS unsigned char* L, unsigned lds0, int wid, int lane, int layer, int kb, rsrc_t r, unsigned kvo) {
    const int t32 = lane & 31, h = lane >> 5;
    xch_put(L, wid, 0, lane, acc[0], 1 - KH); xch_put(L, wid, 1, lane, acc[1], 1 - KH); xch_put(L, wid, 2, lane, acc[2], 1 - KH);
    lds_signal(lds0 + C_XF + wid * 4, lane);
    if (layer == 0) lds_wait_ge(L, C_CS, 6u, r, lane);
    LAS const float* csl = (LAS const float*)(L + LD_CSQ);
    f32x4 cq[2], ck[2];
#pragma unroll
    for (int u = 0; u < 2; ++u) { cq[u] = *(LAS const f32x4*)(csl + 16 * KH + 8 * u + 4 * h) + *(LAS const f32x4*)(csl + 96 + 16 * KH + 8 * u + 4 * h);
        ck[u] = *(LAS const f32x4*)(csl + 32 + 16 * KH + 8 * u + 4 * h) + *(LAS const f32x4*)(csl + 96 + 32 + 16 * KH + 8 * u + 4 * h); }
    const float cv = csl[64 + t32] + csl[96 + 64 + t32];
    lds_wait_ge(L, C_XF + (wid ^ 1) * 4, 4u * layer + 1, r, lane);
    const f32x2 os = ((LAS const f32x2*)(L + LD_ST))[(wid ^ 1) * 32 + t32];
    xch_add(L, wid ^ 1, 0, lane, acc[0], KH); xch_add(L, wid ^ 1, 1, lane, acc[1], KH); xch_add(L, wid ^ 1, 2, lane, acc[2], KH);
    lds_signal(lds0 + C_XRD + (wid >> 2) * 4, lane);
    float la, lb; ln_coef(os, sx, sq, la, lb);
    LAS f32x2* ab = (LAS f32x2*)(L + LD_AB) + (wid >> 1) * 32;
    if (h == 0) ab[t32] = (f32x2){la, lb};
    f32x4 s01[2], s23[2];
#pragma unroll
    for (int u = 0; u < 2; ++u) { s01[u] = *(LAS const f32x4*)(ab + 16 * KH + 8 * u + 4 * h); s23[u] = *(LAS const f32x4*)(ab + 16 * KH + 8 * u + 4 * h + 2); }
    float v[8];
#pragma unroll
    for (int u = 0; u < 2; ++u)
#pragma unroll
        for (int e = 0; e < 4; ++e) v[4 * u + e] = la * acc[1][8 * KH + 4 * u + e] + lb * ck[u][e];
    const u32x4 ok = pack8(v);
    st16(r, lane * 16, kvo + KH * KB, ok);
    *(LAS u32x4*)(L + LD_K + (kb * 2 + KH) * 1024 + lane * 16) = ok;
#pragma unroll
    for (int u = 0; u < 2; ++u) {
        v[4 * u + 0] = s01[u][0] * acc[2][8 * KH + 4 * u + 0] + s01[u][1] * cv; v[4 * u + 1] = s01[u][2] * acc[2][8 * KH + 4 * u + 1] + s01[u][3] * cv;
        v[4 * u + 2] = s23[u][0] * acc[2][8 * KH + 4 * u + 2] + s23[u][1] * cv; v[4 * u + 3] = s23[u][2] * acc[2][8 * KH + 4 * u + 3] + s23[u][3] * cv; }
    const u32x4 ov = pack8(v);
    st16(r, lane * 16, kvo + (2 + KH) * KB, ov);
    *(LAS u32x4*)(L + LD_V + (kb * 2 + KH) * 1024 + lane * 16) = ov;
    const float qa = la * QSCALE, qb_ = lb * QSCALE;
#pragma unroll
    for (int u = 0; u < 2; ++u)
#pragma unroll
        for (int e = 0; e < 4; ++e) v[4 * u + e] = qa * acc[0][8 * KH + 4 * u + e] + qb_ * cq[u][e];
    *(LAS u32x4*)(L + LD_Q + ((kb - 8) * 2 + KH) * 1024 + lane * 16) = pack8(v);
}
DEV void cs_store(const f32x16& a, LAS float* dst, int lane) {
    if ((lane & 31) == 0) {
#pragma unroll
        for (int u = 0; u < 4; ++u) *(LAS f32x4*)(dst + 8 * u + 4 * (lane >> 5)) = (f32x4){a[4 * u], a[4 * u + 1], a[4 * u + 2], a[4 * u + 3]};
    }
}

#define EXP2(x) __builtin_amdgcn_exp2f(x)
DEV void mask_first(f32x16& s, int q32, int h) {
#pragma unroll
    for (int r = 0; r < 16; ++r) { const int key = (r & 3) + 8 * (r >> 2) + 4 * h; if (key <= q32) s[r] = -INFINITY; }
}
DEV void mask_last(f32x16& s, int q32, int h) {
#pragma unroll
    for (int r = 0; r < 16; ++r) { const int key = (r & 3) + 8 * (r >> 2) + 4 * h; if (key > q32) s[r] = -INFINITY; }
}
DEV float max16(const f32x16& s, float m) {
    float a = __builtin_fmaxf(__builtin_fmaxf(s[0], s[1]), m), b = __builtin_fmaxf(__builtin_fmaxf(s[2], s[3]), s[4]);
    a = __builtin_fmaxf(__builtin_fmaxf(a, s[5]), s[6]); b = __builtin_fmaxf(__builtin_fmaxf(b, s[7]), s[8]);
    a = __builtin_fmaxf(__builtin_fmaxf(a, s[9]), s[10]); b = __builtin_fmaxf(__builtin_fmaxf(b, s[11]), s[12]);
    a = __builtin_fmaxf(__builtin_fmaxf(a, s[13]), s[14]);
    return __builtin_fmaxf(__builtin_fmaxf(a, b), s[15]);
}
template <bool FULL9>
DEV void attn_unit(LAS const unsigned char* L, int team, int qb, int hd, int lane, f32x16& o) {
    const int q32 = lane & 31, h = lane >> 5;
    const bf16x8 qf = *(LAS const bf16x8*)(L + LD_Q + (qb * 2 + hd) * 1024 + lane * 16);
    const int first = FULL9 ? 0 : 8 - 4 * team - qb;
    LAS const unsigned char* kp = L + LD_K + (qb * 2 + hd) * 1024 + lane * 16;
    const bool onel = ((lane >> 4) & 1) != hd;
    const unsigned vstr = onel ? 0u : 2048u;
    LAS const unsigned char* vp = onel ? L + LD_ONES : L + LD_V + qb * 2048 + lane * 16;
    LAS const unsigned char* vq = onel ? L + LD_ONES : L + LD_V + qb * 2048 + 1024 + lane * 16;
    bf16x8 kf[9];
#pragma unroll
    for (int i = 0; i < 9; ++i) if (FULL9 || i >= first) kf[i] = *(LAS const bf16x8*)(kp + i * 2048);
    const f32x16 zero = {};
    const int dq = q32 - 4 * h;
    float m = -INFINITY;
    if (FULL9) {
        f32x16 sa = mfma32(kf[0], qf, zero), sb = mfma32(kf[8], qf, zero);
        SB();
#pragma unroll
        for (int r = 0; r < 16; ++r) sa[r] = (r & 3) + 8 * (r >> 2) > dq ? sa[r] : sb[r];
        sb = mfma32(kf[1], qf, zero);
        SB();
        m = max16(sa, m);
        SB();
#pragma unroll
        for (int i = 1; i < 8; ++i) {
            f32x16& cur = (i & 1) ? sb : sa; f32x16& nxt = (i & 1) ? sa : sb;
            if (i + 1 < 8) nxt = mfma32(kf[i + 1], qf, zero);
            SB();
            m = max16(cur, m);
            SB();
        }
    } else {
#pragma unroll
        for (int i = 0; i < 9; ++i) if (i >= first) {
            f32x16 s = mfma32(kf[i], qf, zero);
            if (i == 0) mask_first(s, q32, h);
            if (i == 8) mask_last(s, q32, h);
            m = max16(s, m);
        }
    }
    m = xmax32(m);
    f32x16 nm;
#pragma unroll
    for (int r = 0; r < 16; ++r) nm[r] = -m;
    if (FULL9) {
        f32x16 sa = mfma32(kf[0], qf, nm), sb = mfma32(kf[8], qf, nm);
        const bf16x8 f0 = *(LAS const bf16x8*)(vp), f1 = *(LAS const bf16x8*)(vq), l0 = *(LAS const bf16x8*)(vp + 8 * vstr), l1 = *(LAS const bf16x8*)(vq + 8 * vstr);
        SB();
#pragma unroll
        for (int r = 0; r < 16; ++r) sa[r] = (r & 3) + 8 * (r >> 2) > dq ? sa[r] : sb[r];
        sb = mfma32(kf[1], qf, nm);
        bf16x8 vn0 = *(LAS const bf16x8*)(vp + vstr), vn1 = *(LAS const bf16x8*)(vq + vstr);
        SB();
        {
            u32x4 pf[2], pl[2];
#pragma unroll
            for (int r = 0; r < 16; ++r) sa[r] = EXP2(sa[r]);
            SB();
#pragma unroll
            for (int e = 0; e < 8; ++e) { const int r = 2 * e;
                const unsigned all = pk2(sa[r], sa[r + 1]), fst = pk2((r & 3) + 8 * (r >> 2) > dq ? sa[r] : 0.f, ((r + 1) & 3) + 8 * ((r + 1) >> 2) > dq ? sa[r + 1] : 0.f);
                pf[e >> 2][e & 3] = fst; pl[e >> 2][e & 3] = all ^ fst; }
            SB();
            o = mfma32(f0, __builtin_bit_cast(bf16x8, pf[0]), zero); o = mfma32(f1, __builtin_bit_cast(bf16x8, pf[1]), o);
            o = mfma32(l0, __builtin_bit_cast(bf16x8, pl[0]), o); o = mfma32(l1, __builtin_bit_cast(bf16x8, pl[1]), o);
            SB();
        }
#pragma unroll
        for (int i = 1; i < 8; ++i) {
            f32x16& cur = (i & 1) ? sb : sa; f32x16& nxt = (i & 1) ? sa : sb;
            const bf16x8 v0 = vn0, v1 = vn1;
            if (i + 1 < 8) { vn0 = *(LAS const bf16x8*)(vp + (i + 1) * vstr); vn1 = *(LAS const bf16x8*)(vq + (i + 1) * vstr); nxt = mfma32(kf[i + 1], qf, nm); }
            SB();
            u32x4 p0, p1;
#pragma unroll
            for (int e = 0; e < 4; ++e) p0[e] = pk2(EXP2(cur[2 * e]), EXP2(cur[2 * e + 1]));
            SB();
            o = mfma32(v0, __builtin_bit_cast(bf16x8, p0), o);
#pragma unroll
            for (int e = 0; e < 4; ++e) p1[e] = pk2(EXP2(cur[8 + 2 * e]), EXP2(cur[8 + 2 * e + 1]));
            SB();
            o = mfma32(v1, __builtin_bit_cast(bf16x8, p1), o);
            SB();
        }
    } else {
        float z = 0.f; asm volatile("" : "+v"(z));
#pragma unroll
        for (int r = 0; r < 16; ++r) o[r] = z;
#pragma unroll
        for (int i = 0; i < 9; ++i) if (i >= first) {
            f32x16 s = mfma32(kf[i], qf, nm);
            if (i == 0) mask_first(s, q32, h);
            if (i == 8) mask_last(s, q32, h);
            u32x4 p0, p1;
#pragma unroll
            for (int e = 0; e < 4; ++e) { p0[e] = pk2(EXP2(s[2 * e]), EXP2(s[2 * e + 1])); p1[e] = pk2(EXP2(s[8 + 2 * e]), EXP2(s[8 + 2 * e + 1])); }
            const bf16x8 v0 = *(LAS const bf16x8*)(vp + i * vstr), v1 = *(LAS const bf16x8*)(vq + i * vstr);
            o = mfma32(v0, __builtin_bit_cast(bf16x8, p0), o);
            o = mfma32(v1, __builtin_bit_cast(bf16x8, p1), o);
        }
    }
}

struct Fetch { rsrc_t rs; unsigned so; unsigned v[4]; unsigned vq[4], vo[4], v1[4], v2[4]; };
constexpr unsigned OOB = 0x7FFFFF00u;
DEV void fetch_init(Fetch& f, int lw, int lane) {
#pragma unroll
    for (int j = 0; j < 4; ++j) {
        const int i = lw * 192 + j * 48 + lane, row = (i * 683) >> 13, cc = i - 12 * row;
        f.vq[j] = lane < 48 ? ((256 * (row >> 5) + (row & 31)) * 1536 + (cc >> 2) * 512 + 8 * (cc & 3)) * 2 : OOB;
        const int r16 = lw * 16 + j * 4 + (lane >> 4), c16 = lane & 15;
        f.v1[j] = ((256 * (r16 >> 5) + (r16 & 31)) * 1792 + (((c16 >> 2) ^ (r16 & 3)) * 32) + 8 * (c16 & 3)) * 2;
        const int r8 = lw * 32 + j * 8 + (lane >> 3), c8 = (lane & 7) ^ (((r8 >> 1) & 1) << 2);
        f.vo[j] = ((128 * (r8 >> 5) + (r8 & 31)) * 512 + 8 * c8) * 2;
        f.v2[j] = (r8 * 512 + 8 * c8) * 2;
    }
}
DEV void fetch_pos(Fetch& f, const Params& p, int n, int mem, u32x4 (&b)[4]) {
    const int c = n >= 2 * NPOS ? 34 : n >= NPOS ? n - NPOS : n;
    if (c == 0 || c == 8 || c == 12 || c == 20 || c == 34) {
        const void* base = c == 0 ? (const void*)p.wqkv : c == 8 ? (const void*)p.wo : c == 12 ? (const void*)p.w1 : (const void*)p.w2;
        f.rs = __builtin_amdgcn_make_buffer_rsrc((void*)base, 0, c == 0 ? D * 3 * D * 2 : c == 8 ? D * D * 2 : D * F * 2, 0x00020000);
        f.so = (c == 0 ? 32 * mem : c == 12 ? 128 * (mem < 14 ? mem : 13) : 64 * (mem >> 1)) * 2;
#pragma unroll
        for (int j = 0; j < 4; ++j) f.v[j] = c == 0 ? f.vq[j] : c == 8 ? f.vo[j] : c == 12 ? f.v1[j] : c == 20 ? f.v2[j] : OOB;
    } else f.so += (c < 12 ? (c < 8 ? 32 * 1536 : 32 * 512) : (c < 20 ? 32 * 1792 : 128 * 512)) * 2;
#pragma unroll
    for (int j = 0; j < 4; ++j) { b[j] = ldw(f.rs, f.v[j], f.so); SB(); }
}
constexpr int NSET = 7;
DEV unsigned loader(const Params& p, LAS unsigned char* L, unsigned lds0, int lw, int mem, rsrc_t r, int lane, bool first) {
    u32x4 buf[NSET][4];
    Fetch f; fetch_init(f, lw, lane);
#pragma unroll
    for (int i = 0; i < NSET; ++i) { fetch_pos(f, p, i, mem, buf[i]); SB(); }
    unsigned ep = 0;
    if (first) { ep = rfl(__builtin_amdgcn_raw_buffer_load_b32(r, 0, WS_EPOCH, SC1)) + 1u; lds_wait_ge(L, C_ATT, 8u, r, lane); if (lane == 0) __builtin_amdgcn_raw_buffer_store_b32(ep, r, 0, WS_START + blockIdx.x * 4, SC1); }
#define LOADER_WRITE(n_, i_) do { const int n = (n_), c = n >= NPOS ? n - NPOS : n; \
        if (c < 34) { \
            if (n >= 5) lds_wait_vec(L, C_FREE + (lane & 7) * 4, (unsigned)(n == NPOS + 4 ? n - 9 : n - 4), r, lane);        \
            __builtin_amdgcn_s_setprio(WPRIO); \
            const int js = c < 8 ? 768 : 1024; \
            LAS unsigned char* dst = L + LD_RING + (c % NSLOT) * SLOT + lw * 4 * js + lane * 16; \
            if (c >= 8 || lane < 48) { *(LAS u32x4*)dst = buf[i_][0]; *(LAS u32x4*)(dst + js) = buf[i_][1]; *(LAS u32x4*)(dst + 2 * js) = buf[i_][2]; *(LAS u32x4*)(dst + 3 * js) = buf[i_][3]; } \
        } \
        lds_set(lds0 + C_FULL + lw * 4, (unsigned)s2v(n + 1), lane); __builtin_amdgcn_s_setprio(FPRIO); } while (0)
#pragma unroll 1
    for (int layer = 0; layer < 2; ++layer) {
#pragma unroll 1
        for (int n0 = NPOS * layer; n0 < NPOS * layer + NPOS - NSET; n0 += NSET) {
#pragma unroll
            for (int i = 0; i < NSET; ++i) { LOADER_WRITE(n0 + i, i); fetch_pos(f, p, n0 + i + NSET, mem, buf[i]); }
        }
#pragma unroll
        for (int i = 0; i < NSET; ++i) LOADER_WRITE(NPOS * layer + NPOS - NSET + i, i);
        if (layer == 0) lds_wait_vec(L, C_FREE + (lane & 7) * 4, (unsigned)GATE_Q, r, lane);
#pragma unroll
        for (int i = 0; i < NSET; ++i) fetch_pos(f, p, NPOS * (layer + 1) + i, mem, buf[i]);
    }
#undef LOADER_WRITE
    return ep;
}

DEV void n64_addr(int (&wa)[2], int kq, int lane) {
    const int q4 = (lane & 15) >> 2;
#pragma unroll
    for (int t = 0; t < 2; ++t) { const int g = (2 * t + ((lane >> 4) & 1)) ^ kq;
        wa[t] = kq * 4096 + (4 * (lane >> 5) + q4) * 128 + (((g >> 1) ^ ((q4 >> 1) & 1)) * 64) + (g & 1) * 32 + (lane & 3) * 8; }
}
#define STAGE_BAR(n) do { lds_signal(lds0 + C_BAR, lane); lds_wait_ge(L, C_BAR, 8u * (5 * layer + (n)), r, lane); } while (0)
__global__ __launch_bounds__(NTHR, 3) void k_main(Params p) {
    extern __shared__ __attribute__((aligned(16))) unsigned char lds[];
    int team, mem; block_map(blockIdx.x, team, mem);
    const int tid = threadIdx.x, wid = rfl(tid >> 6); int lane = tid & 63;
    LAS unsigned char* L = (LAS unsigned char*)lds;
    const unsigned lds0 = (unsigned)(size_t)L;
    const rsrc_t r = __builtin_amdgcn_make_buffer_rsrc(p.ws, 0, WS_END, 0x00020000);
    STAMP_RT(0); STAMP_INIT();
    if (tid < 64) ((LAS unsigned*)(L + LD_CTR))[tid] = 0u;
    if (tid >= 64 && tid < 68) ((LAS unsigned*)(L + LD_ONES))[tid & 3] = 0x3f803f80u;
    __syncthreads();
    STAMPV(0, 0);
    if (wid >= NCW) {
        const int ln = (int)__builtin_amdgcn_mbcnt_hi(~0u, __builtin_amdgcn_mbcnt_lo(~0u, 0u));
        const unsigned ep = loader(p, L, lds0, wid - NCW, mem, r, ln, wid == NCW);
        if (blockIdx.x == 0 && wid == NCW) {
#pragma unroll 1
            for (unsigned spins = 0; spins < SPIN_CAP; ++spins) {
                const u32x4 v = ld16(r, ln * 16, WS_START);
                if (__all(v.x == ep && v.y == ep && v.z == ep && v.w == ep)) { if (ln == 0) __builtin_amdgcn_raw_buffer_store_b32(ep, r, 0, WS_EPOCH, SC1); break; }
                __builtin_amdgcn_s_sleep(8);
            }
        }
        STAMPV(0, 1); STAMP_DUMP(); return;
    }

    const int cb = mem >> 1, kh = wid & 1, kq = wid & 3;
    const int tb1 = 4 * team + (wid >> 1);
    const int tb2 = 4 * team + 2 * (wid >> 2) + (mem & 1);
    const rsrc_t rx = __builtin_amdgcn_make_buffer_rsrc((void*)p.x, 0, S * D * 2, 0x00020000);
    unsigned ep = 0;

    for (int layer = 0; layer < 2; ++layer) {
        const unsigned wl = opq(WS_LAYER + layer * L_SIZE), fl = opq(WS_FLAGS + layer * FL_LAYER);
        if (layer) STAMPV(layer, 0);
        {
            lane = opqv(lane);
            float sx, sq;
            f32x16 acc[3], acs; bf16x8 b[16]; f32x4 pr[4];
#pragma unroll
            for (int t = 0; t < 3; ++t) acc[t] = (f32x16){};
            if (layer == 0) {
                const unsigned epv = __builtin_amdgcn_raw_buffer_load_b32(r, 0, WS_EPOCH, SC1);
                u32x4 xr[16];
                const unsigned vo = (lane >> 4) * (D * 2) + (lane & 15) * 16, so = opq((tb1 * 32 * D + 256 * kh) * 2);
#pragma unroll
                for (int i = 0; i < 16; ++i) xr[i] = ldw(rx, vo + (i & 7) * 4 * (D * 2) + (i >> 3) * 256, so);
                ep = rfl(epv) + 1u;
                lds_signal(lds0 + C_ATT, lane);
                STAMPV(1, 22);
                LAS unsigned char* xt = L + LD_XA + wid * 8192;
                float s1 = 0.f, s2 = 0.f;
#pragma unroll
                for (int hf = 0; hf < 2; ++hf) {
#pragma unroll
                    for (int j = 0; j < 8; ++j) { const int row = 4 * j + (lane >> 4); *(LAS u32x4*)(xt + row * 256 + (((lane & 15) ^ (row & 15)) * 16)) = xr[8 * hf + j];
#pragma unroll
                        for (int e = 0; e < 4; ++e) asm volatile("" :: "v"(xr[8 * hf + j][e])); }
#pragma unroll
                    for (int s = 0; s < 8; ++s) {
                        const u32x2 lo = *(LAS const u32x2*)(xt + (lane & 31) * 256 + (((2 * s) ^ (lane & 15)) * 16) + (lane >> 5) * 8), hi = *(LAS const u32x2*)(xt + (lane & 31) * 256 + (((2 * s + 1) ^ (lane & 15)) * 16) + (lane >> 5) * 8);
                        b[8 * hf + s] = __builtin_bit_cast(bf16x8, (u32x4){lo.x, lo.y, hi.x, hi.y});
                        s1 = dot2(lo.x, 0x3f803f80u, dot2(lo.y, 0x3f803f80u, dot2(hi.x, 0x3f803f80u, dot2(hi.y, 0x3f803f80u, s1))));
                        s2 = dot2(lo.x, lo.x, dot2(lo.y, lo.y, dot2(hi.x, hi.x, dot2(hi.y, hi.y, s2))));
                    }
                }
                const float ml = s1 * (1.f / 128.f), m2l = __builtin_fmaxf(s2 - s1 * ml, 0.f) * 0.125f;
#pragma unroll
                for (int i = 0; i < 4; ++i) pr[i] = (f32x4){ml, m2l, ml, m2l};
                STAMPV(1, 23);
            } else {
                wait_flags(r, WS_FLAGS + FL_X2 + tb1 * 32 * WSTR, 0xFFFFu << (16 * kh), lane, ep);
                const unsigned so = opq(WS_LAYER + L_X2F + (tb1 * 32 + 16 * kh) * KB);
#pragma unroll
                for (int s = 0; s < 16; ++s) { b[s] = ldf(r, lane * 16, so + s * KB); if (s == 3) stats_issue(r, WS_LAYER + L_ST2, tb1, kh, lane, pr); }
            }
            STAMPV(layer, 1);
            stats_half(pr, sx, sq); asm volatile("" : "+v"(sx), "+v"(sq));
            {
                const unsigned gneed = lane == 0 ? 8u * (layer + 1) : (lane == 1 + (wid >> 2) ? 4u * (4 * layer) : (lane < 3 && layer ? 4u * (4 * layer - 1) : 0u));
                lds_wait_vec(L, lane == 0 ? C_ATT : C_XRD + ((lane - 1) & 1) * 4, gneed, r, lane);
            }
            int wa[3];
#pragma unroll
            for (int t = 0; t < 3; ++t) wa[t] = kh * 6144 + (4 * (lane >> 5) + ((lane & 15) >> 2)) * 192 + t * 64 + ((lane >> 4) & 1) * 32 + (lane & 3) * 8;
            STAMPW(layer, 24, gemm_stage<0, 8, 3, 2, 192, 16, 0, true, true, false>(wid, lds, acc, b, wa, acs, layer == 0, wid >> 1, pr, sx, sq, L, lds0, layer, r, 0, lane));
            lane = opqv(lane);
            if (layer == 0 && (wid >> 1) < 3) { cs_store(acs, (LAS float*)(L + LD_CSQ) + 96 * kh + 32 * (wid >> 1), lane); lds_signal(lds0 + C_CS, lane); }
            STAMPV(layer, 2);
            lane = opqv(lane);
            if (lane < 32) ((LAS f32x2*)(L + LD_ST))[wid * 32 + lane] = (f32x2){sx, sq};
            const int blk = 4 * team + (wid >> 1), kb = 8 + (wid >> 1);
            const unsigned kvo = wl + L_KV + (blk * 16 + mem) * 4 * KB;
            if (kh == 0) { asm volatile("; kh 0"); qkv_epi<0>(acc, sx, sq, L, lds0, wid, lane, layer, kb, r, kvo); asm volatile("; kh 0 end"); }
            else { asm volatile("; kh 1"); qkv_epi<1>(acc, sx, sq, L, lds0, wid, lane, layer, kb, r, kvo); asm volatile("; kh 1 end"); }
            lds_signal(lds0 + C_LOCAL + (wid >> 1) * 4, lane);
            STAMPV(layer, 3);
            publish(r, fl + FL_KV + ((blk * 16 + mem) * 2 + kh) * WSTR, lane, ep);
            STAGE_BAR(1);
            STAMPV(layer, 4);
        }
        {
            lane = opqv(lane);
            const int gb = 4 * team - 8 + wid;
            if (gb >= 0) {
                wait_flags(r, fl + FL_KV + (gb * 16 + mem) * 2 * WSTR, 3u, lane, ep);
                STAMPV(layer, 5);
                u32x4 kv[4];
                const unsigned so = opq(wl + L_KV + (gb * 16 + mem) * 4 * KB);
#pragma unroll
                for (int i = 0; i < 4; ++i) kv[i] = ld16(r, lane * 16, so + i * KB);
                lds_wait_vec(L, C_XRD + (lane & 1) * 4, 4u * (4 * layer + 1), r, lane);
#pragma unroll
                for (int i = 0; i < 2; ++i) {
                    *(LAS u32x4*)(L + LD_K + (wid * 2 + i) * 1024 + lane * 16) = kv[i];
                    *(LAS u32x4*)(L + LD_V + (wid * 2 + i) * 1024 + lane * 16) = kv[2 + i];
                }
            }
            lds_signal(lds0 + C_HALO + wid * 4, lane);
            const int qb = wid >> 1, hd = wid & 1;
            STAMPV(layer, 6);
            {
                const bool isl = lane < 4, ish = lane >= 8 && lane < 16;
                const unsigned need = isl ? (lane <= qb ? 2u * (layer + 1) : 0u) : (ish && (lane - 8) >= qb ? (unsigned)(layer + 1) : 0u);
                lds_wait_vec(L, C_LOCAL + (lane & 15) * 4, need, r, lane);
            }
            STAMPV(layer, 7);
            f32x16 o;
            if (__builtin_expect(team >= 2, 1)) attn_unit<true>(L, team, qb, hd, lane, o); else attn_unit<false>(L, team, qb, hd, lane, o);
            STAMPV(layer, 8);
            lane = opqv(lane);
            lds_add1(lds0 + C_ATT, lane);
            const float rl = __builtin_amdgcn_rcpf(hd ? o[0] : o[8]);
            float v[8];
#pragma unroll
            for (int e = 0; e < 8; ++e) v[e] = (hd ? o[8 + e] : o[e]) * rl;
            st16(r, lane * 16, wl + L_OF + ((4 * team + qb) * 32 + 2 * mem + hd) * KB, pack8(v));
            publish(r, fl + FL_O + ((4 * team + qb) * 32 + 2 * mem + hd) * WSTR, lane, ep);
            STAGE_BAR(2);
            STAMPV(layer, 9);
        }
        {
            lane = opqv(lane);
            wait_flags(r, fl + FL_O + tb2 * 32 * WSTR, 0xFFu << (8 * kq), lane, ep);
            STAMPV(layer, 10);
            f32x16 acc[2], acs; bf16x8 b[9]; f32x4 pr[4]; float sx, sq;
            acc[0] = (f32x16){}; acc[1] = (f32x16){};
            int wa[2]; n64_addr(wa, kq, lane);
            u32x4 res;
            if (layer == 0) { const unsigned vo = (lane & 31) * (D * 2) + (lane >> 5) * 8, so = (tb2 * 32 * D + 64 * cb + 16 * kq) * 2;
                const u32x2 a = __builtin_amdgcn_raw_buffer_load_b64(rx, vo, so, 0), c = __builtin_amdgcn_raw_buffer_load_b64(rx, vo + 16, so, 0); res = (u32x4){a.x, a.y, c.x, c.y}; }
            else res = ld16(r, lane * 16, WS_LAYER + L_X2F + (tb2 * 32 + 4 * cb + kq) * KB);
            STAMPW(layer, 25, gemm_stage<8, 4, 2, 99, 128, 9, 0, false, false, false>(wid, lds, acc, b, wa, acs, false, 0, pr, sx, sq, L, lds0, layer, r, opq(wl + L_OF + (tb2 * 32 + 8 * kq) * KB), lane));
            STAMPV(layer, 11);
            lane = opqv(lane);
            lds_wait_vec(L, (lane & 1) ? C_ATT : C_XRD + (wid >> 2) * 4, (lane & 1) ? 8u * (layer + 2) : 4u * (4 * layer + 1), r, lane);
            float v[8]; kq_reduce(acc, L, lds0, wid, lane, 4 * layer + 2, r, v);
            resid_add(v, res);
            st16(r, lane * 16, wl + L_X1F + (tb2 * 32 + 4 * cb + kq) * KB, res);
            stats_out(res, r, wl + L_ST1, tb2, 4 * cb + kq, lane);
            STAMPV(layer, 12);
            publish(r, fl + FL_X1 + (tb2 * 32 + 4 * cb + kq) * WSTR, lane, ep);
            STAGE_BAR(3);
            STAMPV(layer, 13);
        }
        {
            lane = opqv(lane);
            wait_flags(r, fl + FL_X1 + tb1 * 32 * WSTR, 0xFFFFu << (16 * kh), lane, ep);
            STAMPV(layer, 14);
            f32x16 acc[4];
#pragma unroll
            for (int t = 0; t < 4; ++t) acc[t] = (f32x16){};
            int wa[4];
            {
                const int q4 = (lane & 15) >> 2;
#pragma unroll
                for (int t = 0; t < 4; ++t) wa[t] = kh * 8192 + (4 * (lane >> 5) + q4) * 256 + (((t ^ (2 * kh)) ^ q4) * 64) + ((lane >> 4) & 1) * 32 + (lane & 3) * 8;
            }
            const unsigned so = opq(wl + L_X1F + (tb1 * 32 + 16 * kh) * KB);
            float sx, sq; f32x4 pr[4]; f32x16 acs; bf16x8 b[RB_F1];
            stats_issue(r, wl + L_ST1, tb1, kh, lane, pr);
            STAMPW(layer, 26, gemm_stage<12, 8, 4, 99, 256, RB_F1, 0, false, true, true>(wid, lds, acc, b, wa, acs, layer == 0, wid >> 1, pr, sx, sq, L, lds0, layer, r, so, lane));
            lane = opqv(lane);
            if (layer == 0) { cs_store(acs, (LAS float*)(L + LD_CS1) + 128 * kh + 32 * ((wid >> 1) ^ (2 * kh)), lane); lds_signal(lds0 + C_CS, lane); }
            STAMPV(layer, 15);
            lane = opqv(lane); const int t32 = lane & 31, h = lane >> 5;
            lds_wait_ge(L, C_XRD + (wid >> 2) * 4, 4u * (4 * layer + 2), r, lane);
            xch_put(L, wid, 0, lane, acc[2], 0); xch_put(L, wid, 1, lane, acc[2], 1); xch_put(L, wid, 2, lane, acc[3], 0); xch_put(L, wid, 3, lane, acc[3], 1);
            if (h == 0) ((LAS f32x2*)(L + LD_ST))[wid * 32 + t32] = (f32x2){sx, sq};
            lds_signal(lds0 + C_XF + wid * 4, lane);
            if (layer == 0) lds_wait_ge(L, C_CS, 14u, r, lane);
            LAS const float* csl = (LAS const float*)(L + LD_CS1) + 64 * kh;
            f32x4 cs[8];
#pragma unroll
            for (int i = 0; i < 8; ++i) cs[i] = *(LAS const f32x4*)(csl + 8 * i + 4 * h) + *(LAS const f32x4*)(csl + 128 + 8 * i + 4 * h);
            lds_wait_ge(L, C_XF + (wid ^ 1) * 4, 4u * layer + 3, r, lane);
            const f32x2 os = ((LAS const f32x2*)(L + LD_ST))[(wid ^ 1) * 32 + t32];
            xch_add(L, wid ^ 1, 0, lane, acc[0], 0); xch_add(L, wid ^ 1, 1, lane, acc[0], 1); xch_add(L, wid ^ 1, 2, lane, acc[1], 0); xch_add(L, wid ^ 1, 3, lane, acc[1], 1);
            lds_signal(lds0 + C_XRD + (wid >> 2) * 4, lane);
            float la, lb; ln_coef(os, sx, sq, la, lb);
            if (mem < 14) {
#pragma unroll
                for (int i = 0; i < 4; ++i) {
                    float hv[8], ex[8];
#pragma unroll
                    for (int e = 0; e < 8; ++e) { hv[e] = la * acc[i >> 1][8 * (i & 1) + e] + lb * cs[2 * i + (e >> 2)][e & 3]; ex[e] = -LOG2E * hv[e]; }
                    SB();
#pragma unroll
                    for (int e = 0; e < 8; ++e) ex[e] = __builtin_amdgcn_exp2f(ex[e]);
                    SB();
#pragma unroll
                    for (int e = 0; e < 8; ++e) ex[e] += 1.f;
                    SB();
#pragma unroll
                    for (int e = 0; e < 8; ++e) ex[e] = __builtin_amdgcn_rcpf(ex[e]);
                    SB();
#pragma unroll
                    for (int e = 0; e < 8; ++e) hv[e] *= ex[e];
                    st16(r, lane * 16, wl + L_HF + (tb1 * 112 + 8 * mem + 4 * kh + i) * KB, pack8(hv));
                    SB();
                }
                STAMPV(layer, 16);
                publish(r, fl + FL_H + (tb1 * 32 + 2 * mem + kh) * WSTR, lane, ep);
            }
            STAGE_BAR(4);
            STAMPV(layer, 17);
        }
        {
            lane = opqv(lane);
            wait_flags(r, fl + FL_H + tb2 * 32 * WSTR, (0x5555555u << (kq >> 1)) & 0xFFFFFFFu, lane, ep);
            STAMPV(layer, 18);
            f32x16 acc[2], acs; bf16x8 b[RB_F2]; f32x4 pr[4]; float sx, sq;
            acc[0] = (f32x16){}; acc[1] = (f32x16){};
            int wa[2]; n64_addr(wa, kq, lane);
            STAMPW(layer, 27, gemm_stage<20, 14, 2, 99, 128, RB_F2, 1, false, false, false>(wid, lds, acc, b, wa, acs, false, 0, pr, sx, sq, L, lds0, layer, r, opq(wl + L_HF + (tb2 * 112 + 2 * kq) * KB), lane));
            STAMPV(layer, 19);
            lane = opqv(lane);
            u32x4 res = ld16(r, lane * 16, wl + L_X1F + (tb2 * 32 + 4 * cb + kq) * KB);
            lds_wait_ge(L, C_XRD + (wid >> 2) * 4, 4u * (4 * layer + 3), r, lane);
            float v[8]; kq_reduce(acc, L, lds0, wid, lane, 4 * layer + 4, r, v);
            resid_add(v, res);
            if (layer == 0) {
                st16(r, lane * 16, wl + L_X2F + (tb2 * 32 + 4 * cb + kq) * KB, res);
                stats_out(res, r, wl + L_ST2, tb2, 4 * cb + kq, lane);
                STAMPV(layer, 20);
                publish(r, fl + FL_X2 + (tb2 * 32 + 4 * cb + kq) * WSTR, lane, ep);
                STAGE_BAR(5);
            } else {
                auto s0 = __builtin_amdgcn_permlane32_swap(res.x, res.z, false, false), s1 = __builtin_amdgcn_permlane32_swap(res.y, res.w, false, false);
                unsigned short* dst = p.out + (size_t)(32 * tb2 + (lane & 31)) * D + 64 * cb + 16 * kq + 8 * (lane >> 5);
                *(u32x4*)dst = (u32x4){s0[0], s1[0], s0[1], s1[1]};
            }
            STAMPV(layer, 21);
        }
    }
    STAMP_RT(1); STAMP_DUMP();
}

extern "C" void kernel_launch(void* const* d_in, const int* in_sizes, int n_in, void* d_out, int out_size, void* d_ws, size_t ws_size, hipStream_t stream) {
    Params p;
    p.x = (const unsigned short*)d_in[0]; p.wqkv = (const unsigned short*)d_in[1]; p.wo = (const unsigned short*)d_in[2];
    p.w1 = (const unsigned short*)d_in[4]; p.w2 = (const unsigned short*)d_in[5];
    p.out = (unsigned short*)d_out; p.ws = (unsigned char*)d_ws;
    (void)hipFuncSetAttribute((const void*)k_main, hipFuncAttributeMaxDynamicSharedMemorySize, LDS_BYTES);
    k_main<<<256, NTHR, LDS_BYTES, stream>>>(p);
}
```

```cpp
#include <hip/hip_runtime.h>
#include <hip/hip_bf16.h>
#include <stdint.h>

typedef short bf16x8 __attribute__((ext_vector_type(8)));
typedef float f32x2 __attribute__((ext_vector_type(2)));
typedef float f32x4 __attribute__((ext_vector_type(4)));
typedef float f32x8 __attribute__((ext_vector_type(8)));
typedef float f32x16 __attribute__((ext_vector_type(16)));
typedef unsigned u32x2 __attribute__((ext_vector_type(2)));
typedef unsigned u32x4 __attribute__((ext_vector_type(4)));
typedef __bf16 bf16x2_t __attribute__((ext_vector_type(2)));
#define GAS __attribute__((address_space(1)))
#define LAS __attribute__((address_space(3)))
#define DEV __device__ __forceinline__
#define SB() __builtin_amdgcn_sched_barrier(0)
typedef __amdgpu_buffer_rsrc_t rsrc_t;

constexpr int S = 2048, D = 512, H = 32, F = 1792;
constexpr float EPS = 1e-5f;
constexpr float LOG2E = 1.4426950408889634f;
constexpr float QSCALE = 0.25f * LOG2E;

constexpr unsigned KB = 1024, MB = 1024 * 1024;
constexpr unsigned WS_CSQ   = 64 * KB;
constexpr unsigned WS_CS1   = WS_CSQ + 192 * KB;
constexpr unsigned WS_WQKV  = 512 * KB;
constexpr unsigned WS_WO    = WS_WQKV + 1536 * KB;
constexpr unsigned WS_W1    = WS_WO + 512 * KB;
constexpr unsigned WS_W2    = WS_W1 + 1792 * KB;
constexpr unsigned WS_XF0   = WS_W2 + 1792 * KB;
constexpr unsigned WS_ST0   = WS_XF0 + 2 * MB;
constexpr unsigned WS_LAYER = WS_ST0 + 512 * KB;
constexpr unsigned L_KV  = 0;
constexpr unsigned L_OF  = L_KV + 4 * MB;
constexpr unsigned L_X1F = L_OF + 2 * MB;
constexpr unsigned L_HF  = L_X1F + 2 * MB;
constexpr unsigned L_X2F = L_HF + 7 * MB;
constexpr unsigned L_ST1 = L_X2F + 2 * MB;
constexpr unsigned L_ST2 = L_ST1 + 512 * KB;
constexpr unsigned L_SIZE = L_ST2 + 512 * KB;
constexpr unsigned WSTR = 128;
constexpr unsigned WS_FLAGS = 64 * MB;
constexpr unsigned FL_KV = 0, FL_O = FL_KV + 64 * 16 * 2 * WSTR, FL_X1 = FL_O + 64 * 32 * WSTR, FL_H = FL_X1 + 64 * 32 * WSTR, FL_X2 = FL_H + 64 * 32 * WSTR, FL_LAYER = FL_X2 + 64 * 32 * WSTR;
constexpr unsigned WS_END = 72 * MB;
static_assert(WS_LAYER + 2 * L_SIZE <= WS_FLAGS && WS_FLAGS + 2 * FL_LAYER <= WS_END, "workspace map");

constexpr int NCW = 8, NLW = 4, NTHR = 64 * (NCW + NLW);
constexpr int SLOT = 16384, NSLOT = 5;
constexpr int LD_RING = 0;
constexpr int LD_XA = 80 * 1024;
constexpr int LD_K = 96 * 1024;
constexpr int LD_V = 120 * 1024;
constexpr int LD_XB = LD_V;
constexpr int LD_XC0 = LD_K + 16 * 1024, LD_XC1 = LD_V + 16 * 1024;
constexpr int LD_Q = 144 * 1024;
constexpr int LD_MISC = 152 * 1024;
constexpr int LD_CSQ = LD_MISC  , LD_CS1 = LD_MISC + 768  , LD_ONES = LD_MISC + 1792, LD_CTR = LD_MISC + 1856, LD_DUMMY = LD_CTR + 256  ,
              LD_ST = LD_DUMMY + 512  , LD_AB = LD_ST + 2048  , LD_STAMP = LD_AB + 1024;
constexpr int C_FULL = LD_CTR  , C_FREE = LD_CTR + 32  , C_LOCAL = LD_CTR + 64, C_HALO = LD_CTR + 96, C_XF = LD_CTR + 128, C_XRD = LD_CTR + 160, C_ATT = LD_CTR + 176, C_BAR = LD_CTR + 180, C_CS = LD_CTR + 184;
constexpr int LDS_BYTES = 160 * 1024;
static_assert(LD_STAMP + 8 * 64 * 4 <= LDS_BYTES && LD_CTR % 16 == 0 && LD_ST % 16 == 0, "LDS map");
constexpr int slot_of(int c) { return c % NSLOT; }
constexpr int NPOS = 35;

struct Params {
    const unsigned short* x; const unsigned short* wqkv; const unsigned short* wo; const unsigned short* w1; const unsigned short* w2;
    unsigned short* out; unsigned char* ws;
};

#define STAMPV(kid, i)
#define STAMPW(kid, i, ...) (void)(__VA_ARGS__)
#define STAMP_RT(i)
#define STAMP_DUMP()
#define STAMP_INIT()

DEV float bf2f(unsigned short b) { return __uint_as_float((unsigned)b << 16); }
DEV float bflo(unsigned u) { return __uint_as_float(u << 16); }
DEV float bfhi(unsigned u) { return __uint_as_float(u & 0xffff0000u); }
DEV unsigned pk2(float lo, float hi) { f32x2 v = {lo, hi}; bf16x2_t r = __builtin_convertvector(v, bf16x2_t); return __builtin_bit_cast(unsigned, r); }
DEV float xsum32(float v) { auto r = __builtin_amdgcn_permlane32_swap(__float_as_uint(v), __float_as_uint(v), false, false); return __uint_as_float(r[0]) + __uint_as_float(r[1]); }
DEV float xmax32(float v) { auto r = __builtin_amdgcn_permlane32_swap(__float_as_uint(v), __float_as_uint(v), false, false); return __builtin_fmaxf(__uint_as_float(r[0]), __uint_as_float(r[1])); }
DEV f32x16 mfma32(bf16x8 a, bf16x8 b, f32x16 c) { return __builtin_amdgcn_mfma_f32_32x32x16_bf16(a, b, c, 0, 0, 0); }
DEV float dot2(unsigned a, unsigned b, float c) { return __builtin_amdgcn_fdot2_f32_bf16(__builtin_bit_cast(bf16x2_t, a), __builtin_bit_cast(bf16x2_t, b), c, false); }
DEV unsigned opq(unsigned x) { asm volatile("" : "+s"(x)); return x; }
DEV int opqv(int x) { asm volatile("" : "+v"(x)); return x; }
DEV unsigned rfl(unsigned v) { return __builtin_amdgcn_readfirstlane(v); }
#define WAITV(n) asm volatile("s_waitcnt vmcnt(" #n ")" ::: "memory")
constexpr int SC1 = 16;
DEV bf16x8 ldf(rsrc_t r, unsigned vo, unsigned so) { return __builtin_bit_cast(bf16x8, __builtin_amdgcn_raw_buffer_load_b128(r, vo, so, SC1)); }
DEV u32x4 ld16(rsrc_t r, unsigned vo, unsigned so) { return __builtin_amdgcn_raw_buffer_load_b128(r, vo, so, SC1); }
DEV u32x4 ldw(rsrc_t r, unsigned vo, unsigned so) { return __builtin_amdgcn_raw_buffer_load_b128(r, vo, so, 0); }
DEV void st16(rsrc_t r, unsigned vo, unsigned so, u32x4 v) { __builtin_amdgcn_raw_buffer_store_b128(v, r, vo, so, SC1); asm volatile("s_nop 1" :: "v"(v) : "memory"); }

DEV void block_map(int b, int& team, int& mem) {
    const int x = b & 7, r = b >> 3;
    team = 2 * x + (r >> 4); mem = r & 15;
}

typedef short v4i16 __attribute__((ext_vector_type(4)));
DEV v4i16 trd(LAS const unsigned char* p) { return __builtin_amdgcn_ds_read_tr16_b64_v4i16((LAS v4i16*)p); }
constexpr int PREP_W = 2816, PREP_X = 512;
__global__ __launch_bounds__(256) void k_prep(Params p) {
    __shared__ __attribute__((aligned(16))) unsigned char tile[4][4096];
    const int lane = threadIdx.x & 63, wv = threadIdx.x >> 6, id = blockIdx.x * 4 + wv;
    LAS unsigned char* T = (LAS unsigned char*)tile[wv];
    for (unsigned i = blockIdx.x * 256 + threadIdx.x; i < 2 * FL_LAYER / 16; i += gridDim.x * 256) ((u32x4*)(p.ws + WS_FLAGS))[i] = (u32x4){0u, 0u, 0u, 0u};
    if (id < PREP_W) {
        const unsigned short* W; int ld, s, g, ty;
        if (id < 768) { ty = 0; W = p.wqkv; ld = 1536; s = id / 24; g = id % 24; }
        else if (id < 1024) { ty = 1; W = p.wo; ld = 512; s = (id - 768) >> 3; g = (id - 768) & 7; }
        else if (id < 1920) { ty = 2; W = p.w1; ld = 1792; s = (id - 1024) / 28; g = (id - 1024) % 28; }
        else { ty = 3; W = p.w2; ld = 512; s = (id - 1920) >> 3; g = (id - 1920) & 7; }
        const unsigned short* src = W + (size_t)(16 * s + (lane >> 3)) * ld + 64 * g + 8 * (lane & 7);
        const u32x4 a = *(const u32x4*)src, b = *(const u32x4*)(src + 8 * ld);
        *(LAS u32x4*)(T + lane * 16) = a; *(LAS u32x4*)(T + 1024 + lane * 16) = b;
        const int h = lane >> 5, nsub = (lane >> 4) & 1, row = 4 * h + ((lane & 15) >> 2);
        const int kq = ty == 1 ? s >> 3 : (s >> 1) & 3;
#pragma unroll
        for (int t = 0; t < 2; ++t) {
            const int grp = (ty & 1) ? (2 * t + nsub) ^ kq : 2 * t + nsub;
            LAS const unsigned char* a0 = T + row * 128 + grp * 32 + (lane & 3) * 8;
            const v4i16 lo = trd(a0), hi = trd(a0 + 8 * 128);
            const u32x2 l2 = __builtin_bit_cast(u32x2, lo), h2 = __builtin_bit_cast(u32x2, hi);
            const u32x4 o = {l2.x, l2.y, h2.x, h2.y};
            unsigned dst; float* cs = nullptr;
            if (ty == 0) { const int part = g >> 3, mem = 2 * (g & 7) + t, kh = s >> 4, c = (s & 15) >> 1, j = s & 1;
                dst = WS_WQKV + (mem * 96 + c * 12 + kh * 6 + j * 3 + part) * KB; cs = (float*)(p.ws + WS_CSQ) + (mem * 32 + s) * 96 + part * 32; }
            else if (ty == 1) { const int c = (s & 7) >> 1, j = s & 1; dst = WS_WO + (g * 64 + c * 16 + kq * 4 + j * 2 + t) * KB; }
            else if (ty == 2) { const int mem = g >> 1, T4 = 2 * (g & 1) + t, kh = s >> 4, c = (s & 15) >> 1, j = s & 1;
                dst = WS_W1 + (mem * 128 + c * 16 + kh * 8 + j * 4 + (T4 ^ (2 * kh))) * KB; cs = (float*)(p.ws + WS_CS1) + (mem * 32 + s) * 128 + T4 * 32; }
            else { const int c = s >> 3, j = s & 1; dst = WS_W2 + (g * 224 + c * 16 + kq * 4 + j * 2 + t) * KB; }
            *(u32x4*)(p.ws + dst + lane * 16) = o;
            if (cs) { const float sum = xsum32(((bflo(o.x) + bfhi(o.x)) + (bflo(o.y) + bfhi(o.y))) + ((bflo(o.z) + bfhi(o.z)) + (bflo(o.w) + bfhi(o.w)))); if (h == 0) cs[lane] = sum; }
        }
    } else if (id < PREP_W + PREP_X) {
        const int i = id - PREP_W, tb = i >> 3, g = i & 7;
        const unsigned short* src = p.x + (size_t)(32 * tb + (lane >> 3)) * D + 64 * g + 8 * (lane & 7);
        u32x4 v[4];
#pragma unroll
        for (int j = 0; j < 4; ++j) v[j] = *(const u32x4*)(src + 8 * j * D);
#pragma unroll
        for (int j = 0; j < 4; ++j) { const int row = 8 * j + (lane >> 3); *(LAS u32x4*)(T + row * 128 + (((lane & 7) ^ (row & 7)) * 16)) = v[j]; }
        const int t32 = lane & 31, h = lane >> 5;
#pragma unroll
        for (int s = 0; s < 4; ++s) {
            const u32x2 a = *(LAS const u32x2*)(T + t32 * 128 + (((2 * s) ^ (t32 & 7)) * 16) + h * 8), b = *(LAS const u32x2*)(T + t32 * 128 + (((2 * s + 1) ^ (t32 & 7)) * 16) + h * 8);
            const int st = 4 * g + s;
            *(u32x4*)(p.ws + WS_XF0 + (tb * 32 + st) * KB + lane * 16) = (u32x4){a.x, a.y, b.x, b.y};
            const float f[8] = {bflo(a.x), bfhi(a.x), bflo(a.y), bfhi(a.y), bflo(b.x), bfhi(b.x), bflo(b.y), bfhi(b.y)};
            const float mean = xsum32(((f[0] + f[1]) + (f[2] + f[3])) + ((f[4] + f[5]) + (f[6] + f[7]))) * (1.f / 16.f); float q = 0.f;
#pragma unroll
            for (int e = 0; e < 8; ++e) { const float d = f[e] - mean; q += d * d; }
            q = xsum32(q);
            if (h == 0) *(f32x2*)(p.ws + WS_ST0 + ((tb * 16 + (st >> 1)) * 32 + t32) * 16 + (st & 1) * 8) = (f32x2){mean, q};
        }
    }
}

#define RB_Q 12
#define RB_F1 8
#define RB_F2 12
#define GATE_Q 31
#define WPRIO 3
#define FPRIO 0
#define POLL_SLEEP 1
constexpr unsigned SPIN_CAP = 1u << 15;
DEV void wait_flags(rsrc_t r, unsigned off, unsigned mask, int lane) {
    bool need = lane < 32 && ((mask >> (lane & 31)) & 1u);
#pragma unroll 1
    for (unsigned spins = 0;; ++spins) {
        if (need) need = __builtin_amdgcn_raw_buffer_load_b32(r, lane * WSTR, off, SC1) == 0u;
        if (!__any(need)) break;
        if (spins > SPIN_CAP) break;
        __builtin_amdgcn_s_sleep(POLL_SLEEP);
    }
    __builtin_amdgcn_fence(__ATOMIC_ACQUIRE, "wavefront");
}
DEV void publish(rsrc_t r, unsigned word_off, int lane) {
    WAITV(0);
    if (lane == 0) __builtin_amdgcn_raw_buffer_store_b32(1u, r, 0, word_off, SC1);
}
DEV unsigned lds_ld(LAS unsigned char* L, int off) { return *(volatile LAS unsigned*)(L + off); }
DEV int s2v(int x) { int v; asm volatile("v_mov_b32 %0, %1" : "=v"(v) : "s"(x)); return v; }
DEV unsigned lds_ldu(LAS unsigned char* L, int off) { return *(volatile LAS unsigned*)(L + s2v(off)); }
DEV void lds_add1(unsigned addr, int lane) {
    const unsigned a = lane == 0 ? addr : (unsigned)(LD_DUMMY + lane * 4);
    asm volatile("ds_add_u32 %0, %1" :: "v"(a), "v"(1u) : "memory");
}
DEV void lds_signal(unsigned addr, int lane) { lds_add1(addr, lane); }
DEV void lds_wait_ge(LAS unsigned char* L, int off, unsigned target, rsrc_t r, int lane) {
    if (__builtin_expect(rfl(lds_ldu(L, off)) < target, 0)) {
#pragma unroll 1
        for (unsigned spins = 0; rfl(lds_ldu(L, off)) < target && spins < (SPIN_CAP << 3); ++spins) if (spins > 1) __builtin_amdgcn_s_sleep(1);
    }
    asm volatile("" ::: "memory");
}
DEV void lds_wait_vec(LAS unsigned char* L, int off, unsigned need, rsrc_t r, int lane) {
    if (__builtin_expect(!__all(lds_ld(L, off) >= need), 0)) {
#pragma unroll 1
        for (unsigned spins = 0; !__all(lds_ld(L, off) >= need) && spins < (SPIN_CAP << 3); ++spins) if (spins > 1) __builtin_amdgcn_s_sleep(1);
    }
    asm volatile("" ::: "memory");
}

DEV void lds_set(unsigned addr, unsigned val, int lane) {
    const unsigned a = lane == 0 ? addr : (unsigned)(LD_DUMMY + lane * 4);
    asm volatile("ds_write_b32 %0, %1" :: "v"(a), "v"(val) : "memory");
}
DEV unsigned qmin(unsigned v) {
    v = min(v, (unsigned)__builtin_amdgcn_mov_dpp((int)v, 0xB1, 0xF, 0xF, true));
    return min(v, (unsigned)__builtin_amdgcn_mov_dpp((int)v, 0x4E, 0xF, 0xF, true));
}
DEV unsigned ring_ready(LAS unsigned char* L, unsigned pos, int lane) {
    unsigned n;
#pragma unroll 1
    for (unsigned spins = 0;; ++spins) {
        n = rfl(qmin(lds_ld(L, C_FULL + (lane & 3) * 4)));
        if (n > pos || spins > (SPIN_CAP << 3)) break;
        if (spins > 2) __builtin_amdgcn_s_sleep(1);
    }
    asm volatile("" ::: "memory");
    return n;
}
DEV void stats_issue(rsrc_t r, unsigned st, int tb, int kh, int lane, f32x4 (&pr)[4]) {
    const unsigned so = opq(st + (tb * 16 + 8 * kh) * 512);
#pragma unroll
    for (int i = 0; i < 4; ++i) pr[i] = __builtin_bit_cast(f32x4, ld16(r, ((lane >> 5) * 4 * 32 + (lane & 31)) * 16 + i * 512, so));
}
DEV void stats_half(const f32x4 (&pr)[4], float& mean, float& m2) {
    float ms = 0.f;
#pragma unroll
    for (int i = 0; i < 4; ++i) ms += pr[i][0] + pr[i][2];
    mean = xsum32(ms) * (1.f / 16.f);
    float q = 0.f;
#pragma unroll
    for (int i = 0; i < 4; ++i) { const float d0 = pr[i][0] - mean, d1 = pr[i][2] - mean; q += pr[i][1] + pr[i][3] + 16.f * (d0 * d0 + d1 * d1); }
    m2 = xsum32(q);
}
DEV void stats_out(u32x4 o, rsrc_t r, unsigned st, int tb, int s, int lane) {
    const float q8[8] = {bflo(o.x), bfhi(o.x), bflo(o.y), bfhi(o.y), bflo(o.z), bfhi(o.z), bflo(o.w), bfhi(o.w)};
    const float mean = xsum32(((q8[0] + q8[1]) + (q8[2] + q8[3])) + ((q8[4] + q8[5]) + (q8[6] + q8[7]))) * (1.f / 16.f);
    float q = 0.f;
#pragma unroll
    for (int e = 0; e < 8; ++e) { const float d = q8[e] - mean; q += d * d; }
    q = xsum32(q);
    if (lane < 32) __builtin_amdgcn_raw_buffer_store_b64((u32x2){__float_as_uint(mean), __float_as_uint(q)}, r, lane * 16 + (s & 1) * 8, st + (tb * 16 + (s >> 1)) * 512, SC1);
}

template <int C0, int NCH, int NT, int SWAPV, int RB, int AMODE, bool ST>
DEV unsigned gemm_stage(int wid, unsigned char* lds, f32x16 (&acc)[NT], int wsub, float& sx, float& sq,
                        LAS unsigned char* L, unsigned lds0, int layer, rsrc_t r, unsigned so, unsigned st, int tb, int kh, int lane) {
    unsigned twait = 0;
    constexpr int NS = 2 * NCH, JS = NT * 1024;
    const unsigned l16 = lane * 16;
    bf16x8 b[RB]; f32x4 pl[4];
#define AOFF(s_) (AMODE == 0 ? (s_) * KB : (((s_) >> 1) * 8 + ((s_) & 1)) * KB)
#pragma unroll
    for (int s = 0; s < RB - 1; ++s) { if (s < NS) b[s] = ldf(r, l16, so + AOFF(s)); if (ST && s == 1) stats_issue(r, st, tb, kh, lane, pl); }
    LAS const unsigned char* w0 = L + LD_RING + wsub + l16;
    LAS const unsigned char* w1 = L + opqv(LD_RING + 4 * SLOT + wsub + l16);
    const unsigned fadr = lane == 0 ? lds0 + C_FREE + wid * 4 : (unsigned)(LD_DUMMY + lane * 4);
    const unsigned pos0 = NPOS * layer + C0;
#define WADDR(s_) ((slot_of(C0 + ((s_) >> 1)) == 4 ? w1 : w0 + slot_of(C0 + ((s_) >> 1)) * SLOT) + ((s_) & 1) * JS)
    unsigned ready = ring_ready(L, pos0, lane);
    bf16x8 fa[NT];
#pragma unroll
    for (int t = 0; t < NT; ++t) fa[t] = *(LAS const bf16x8*)(WADDR(0) + t * 1024);
    SB();
#pragma unroll
    for (int s = 0; s < NS; ++s) {
        if (s + RB - 1 < NS) b[(s + RB - 1) % RB] = ldf(r, l16, so + AOFF(s + RB - 1));
        const int sp = s + 1;
        if (sp < NS && (sp & 1) == 0) { if (__builtin_expect(pos0 + (sp >> 1) >= ready, 0)) {
            ready = ring_ready(L, pos0 + (sp >> 1), lane);
        } }
        SB();
#pragma unroll
        for (int t = 0; t < NT; ++t) {
            if (t >= SWAPV) acc[t] = mfma32(b[s % RB], fa[t], acc[t]); else acc[t] = mfma32(fa[t], b[s % RB], acc[t]);
            if (sp < NS) fa[t] = *(LAS const bf16x8*)(WADDR(sp) + t * 1024);
            SB();
        }
        if (s & 1) { const unsigned rel = (unsigned)s2v((int)(pos0 + (s >> 1) + 1)); asm volatile("ds_write_b32 %0, %1" :: "v"(fadr), "v"(rel) : "memory"); }
        if (ST && s == 3) { stats_half(pl, sx, sq); asm volatile("" : "+v"(sx), "+v"(sq)); }
        if (s == 0) STAMPV(layer, C0 == 0 ? 28 : C0 == 8 ? 29 : C0 == 12 ? 30 : 31);
    }
#undef AOFF
#undef WADDR
    return twait;
}
DEV int xch_off(int w, int slot) { return slot < 2 ? LD_XA + w * 4096 + slot * 2048 : slot == 2 ? LD_XB + w * 2048 : (w < 4 ? LD_XC0 + w * 2048 : LD_XC1 + (w - 4) * 2048); }
DEV void xch_put(LAS unsigned char* L, int w, int slot, int lane, const f32x16& a, int half) {
    LAS f32x4* d = (LAS f32x4*)(L + xch_off(w, slot)) + lane;
    d[0] = (f32x4){a[8 * half], a[8 * half + 1], a[8 * half + 2], a[8 * half + 3]}; d[64] = (f32x4){a[8 * half + 4], a[8 * half + 5], a[8 * half + 6], a[8 * half + 7]};
}
DEV void xch_add(LAS unsigned char* L, int w, int slot, int lane, f32x16& a, int half) {
    LAS const f32x4* d = (LAS const f32x4*)(L + xch_off(w, slot)) + lane;
    const f32x4 u = d[0], v = d[64];
#pragma unroll
    for (int e = 0; e < 4; ++e) { a[8 * half + e] += u[e]; a[8 * half + 4 + e] += v[e]; }
}
DEV void ln_coef(f32x2 o, float mean_h, float m2_h, float& la, float& lb) {
    const float mean = 0.5f * (mean_h + o.x), d = mean_h - o.x;
    const float var = (m2_h + o.y + 128.f * d * d) * (1.f / 512.f);
    la = __builtin_amdgcn_rsqf(var + EPS); lb = -mean * la;
}
DEV void kq_reduce(f32x16 (&acc)[2], LAS unsigned char* L, unsigned lds0, int wid, int lane, unsigned seq, rsrc_t r, float (&v)[8]) {
    const int base = wid & 4;
    xch_put(L, wid, 0, lane, acc[0], 1); xch_put(L, wid, 1, lane, acc[1], 0); xch_put(L, wid, 2, lane, acc[1], 1);
    lds_signal(lds0 + C_XF + wid * 4, lane);
    lds_wait_vec(L, C_XF + (base + (lane & 3)) * 4, seq, r, lane);
#pragma unroll
    for (int x = 1; x < 4; ++x) xch_add(L, wid ^ x, x - 1, lane, acc[0], 0);
    lds_signal(lds0 + C_XRD + (wid >> 2) * 4, lane);
#pragma unroll
    for (int e = 0; e < 8; ++e) v[e] = acc[0][e];
}
DEV u32x4 pack8(const float (&f)[8]) { return (u32x4){pk2(f[0], f[1]), pk2(f[2], f[3]), pk2(f[4], f[5]), pk2(f[6], f[7])}; }
DEV void resid_add(const float (&v)[8], u32x4& res) {
    const float f[8] = {bflo(res.x) + v[0], bfhi(res.x) + v[1], bflo(res.y) + v[2], bfhi(res.y) + v[3], bflo(res.z) + v[4], bfhi(res.z) + v[5], bflo(res.w) + v[6], bfhi(res.w) + v[7]};
    res = pack8(f);
}

template <int KH>
DEV void qkv_epi(f32x16 (&acc)[3], float sx, float sq, LAS unsigned char* L, unsigned lds0, int wid, int lane, int layer, int kb, rsrc_t r, unsigned kvo) {
    const int t32 = lane & 31, h = lane >> 5;
    xch_put(L, wid, 0, lane, acc[0], 1 - KH); xch_put(L, wid, 1, lane, acc[1], 1 - KH); xch_put(L, wid, 2, lane, acc[2], 1 - KH);
    lds_signal(lds0 + C_XF + wid * 4, lane);
    LAS const float* csl = (LAS const float*)(L + LD_CSQ);
    f32x4 cq[2], ck[2];
#pragma unroll
    for (int u = 0; u < 2; ++u) { cq[u] = *(LAS const f32x4*)(csl + 16 * KH + 8 * u + 4 * h); ck[u] = *(LAS const f32x4*)(csl + 32 + 16 * KH + 8 * u + 4 * h); }
    const float cv = csl[64 + t32];
    lds_wait_ge(L, C_XF + (wid ^ 1) * 4, 4u * layer + 1, r, lane);
    const f32x2 os = ((LAS const f32x2*)(L + LD_ST))[(wid ^ 1) * 32 + t32];
    xch_add(L, wid ^ 1, 0, lane, acc[0], KH); xch_add(L, wid ^ 1, 1, lane, acc[1], KH); xch_add(L, wid ^ 1, 2, lane, acc[2], KH);
    lds_signal(lds0 + C_XRD + (wid >> 2) * 4, lane);
    float la, lb; ln_coef(os, sx, sq, la, lb);
    LAS f32x2* ab = (LAS f32x2*)(L + LD_AB) + (wid >> 1) * 32;
    if (h == 0) ab[t32] = (f32x2){la, lb};
    f32x4 s01[2], s23[2];
#pragma unroll
    for (int u = 0; u < 2; ++u) { s01[u] = *(LAS const f32x4*)(ab + 16 * KH + 8 * u + 4 * h); s23[u] = *(LAS const f32x4*)(ab + 16 * KH + 8 * u + 4 * h + 2); }
    float v[8];
#pragma unroll
    for (int u = 0; u < 2; ++u)
#pragma unroll
        for (int e = 0; e < 4; ++e) v[4 * u + e] = la * acc[1][8 * KH + 4 * u + e] + lb * ck[u][e];
    const u32x4 ok = pack8(v);
    st16(r, lane * 16, kvo + KH * KB, ok);
    *(LAS u32x4*)(L + LD_K + (kb * 2 + KH) * 1024 + lane * 16) = ok;
#pragma unroll
    for (int u = 0; u < 2; ++u) {
        v[4 * u + 0] = s01[u][0] * acc[2][8 * KH + 4 * u + 0] + s01[u][1] * cv; v[4 * u + 1] = s01[u][2] * acc[2][8 * KH + 4 * u + 1] + s01[u][3] * cv;
        v[4 * u + 2] = s23[u][0] * acc[2][8 * KH + 4 * u + 2] + s23[u][1] * cv; v[4 * u + 3] = s23[u][2] * acc[2][8 * KH + 4 * u + 3] + s23[u][3] * cv; }
    const u32x4 ov = pack8(v);
    st16(r, lane * 16, kvo + (2 + KH) * KB, ov);
    *(LAS u32x4*)(L + LD_V + (kb * 2 + KH) * 1024 + lane * 16) = ov;
    const float qa = la * QSCALE, qb_ = lb * QSCALE;
#pragma unroll
    for (int u = 0; u < 2; ++u)
#pragma unroll
        for (int e = 0; e < 4; ++e) v[4 * u + e] = qa * acc[0][8 * KH + 4 * u + e] + qb_ * cq[u][e];
    *(LAS u32x4*)(L + LD_Q + ((kb - 8) * 2 + KH) * 1024 + lane * 16) = pack8(v);
}
#define EXP2(x) __builtin_amdgcn_exp2f(x)
DEV void mask_first(f32x16& s, int q32, int h) {
#pragma unroll
    for (int r = 0; r < 16; ++r) { const int key = (r & 3) + 8 * (r >> 2) + 4 * h; if (key <= q32) s[r] = -INFINITY; }
}
DEV void mask_last(f32x16& s, int q32, int h) {
#pragma unroll
    for (int r = 0; r < 16; ++r) { const int key = (r & 3) + 8 * (r >> 2) + 4 * h; if (key > q32) s[r] = -INFINITY; }
}
DEV float max16(const f32x16& s, float m) {
    float a = __builtin_fmaxf(__builtin_fmaxf(s[0], s[1]), m), b = __builtin_fmaxf(__builtin_fmaxf(s[2], s[3]), s[4]);
    a = __builtin_fmaxf(__builtin_fmaxf(a, s[5]), s[6]); b = __builtin_fmaxf(__builtin_fmaxf(b, s[7]), s[8]);
    a = __builtin_fmaxf(__builtin_fmaxf(a, s[9]), s[10]); b = __builtin_fmaxf(__builtin_fmaxf(b, s[11]), s[12]);
    a = __builtin_fmaxf(__builtin_fmaxf(a, s[13]), s[14]);
    return __builtin_fmaxf(__builtin_fmaxf(a, b), s[15]);
}
template <bool FULL9>
DEV void attn_unit(LAS const unsigned char* L, int team, int qb, int hd, int lane, f32x16& o) {
    const int q32 = lane & 31, h = lane >> 5;
    const bf16x8 qf = *(LAS const bf16x8*)(L + LD_Q + (qb * 2 + hd) * 1024 + lane * 16);
    const int first = FULL9 ? 0 : 8 - 4 * team - qb;
    LAS const unsigned char* kp = L + LD_K + (qb * 2 + hd) * 1024 + lane * 16;
    const bool onel = ((lane >> 4) & 1) != hd;
    const unsigned vstr = onel ? 0u : 2048u;
    LAS const unsigned char* vp = onel ? L + LD_ONES : L + LD_V + qb * 2048 + lane * 16;
    LAS const unsigned char* vq = onel ? L + LD_ONES : L + LD_V + qb * 2048 + 1024 + lane * 16;
    bf16x8 kf[9];
#pragma unroll
    for (int i = 0; i < 9; ++i) if (FULL9 || i >= first) kf[i] = *(LAS const bf16x8*)(kp + i * 2048);
    const f32x16 zero = {};
    const int dq = q32 - 4 * h;
    float m = -INFINITY;
    if (FULL9) {
        f32x16 sa = mfma32(kf[0], qf, zero), sb = mfma32(kf[8], qf, zero);
        SB();
#pragma unroll
        for (int r = 0; r < 16; ++r) sa[r] = (r & 3) + 8 * (r >> 2) > dq ? sa[r] : sb[r];
        sb = mfma32(kf[1], qf, zero);
        SB();
        m = max16(sa, m);
        SB();
#pragma unroll
        for (int i = 1; i < 8; ++i) {
            f32x16& cur = (i & 1) ? sb : sa; f32x16& nxt = (i & 1) ? sa : sb;
            if (i + 1 < 8) nxt = mfma32(kf[i + 1], qf, zero);
            SB();
            m = max16(cur, m);
            SB();
        }
    } else {
#pragma unroll
        for (int i = 0; i < 9; ++i) if (i >= first) {
            f32x16 s = mfma32(kf[i], qf, zero);
            if (i == 0) mask_first(s, q32, h);
            if (i == 8) mask_last(s, q32, h);
            m = max16(s, m);
        }
    }
    m = xmax32(m);
    f32x16 nm;
#pragma unroll
    for (int r = 0; r < 16; ++r) nm[r] = -m;
    if (FULL9) {
        f32x16 sa = mfma32(kf[0], qf, nm), sb = mfma32(kf[8], qf, nm);
        const bf16x8 f0 = *(LAS const bf16x8*)(vp), f1 = *(LAS const bf16x8*)(vq), l0 = *(LAS const bf16x8*)(vp + 8 * vstr), l1 = *(LAS const bf16x8*)(vq + 8 * vstr);
        SB();
#pragma unroll
        for (int r = 0; r < 16; ++r) sa[r] = (r & 3) + 8 * (r >> 2) > dq ? sa[r] : sb[r];
        sb = mfma32(kf[1], qf, nm);
        bf16x8 vn0 = *(LAS const bf16x8*)(vp + vstr), vn1 = *(LAS const bf16x8*)(vq + vstr);
        SB();
        {
            u32x4 pf[2], pl[2];
#pragma unroll
            for (int r = 0; r < 16; ++r) sa[r] = EXP2(sa[r]);
            SB();
#pragma unroll
            for (int e = 0; e < 8; ++e) { const int r = 2 * e;
                const unsigned all = pk2(sa[r], sa[r + 1]), fst = pk2((r & 3) + 8 * (r >> 2) > dq ? sa[r] : 0.f, ((r + 1) & 3) + 8 * ((r + 1) >> 2) > dq ? sa[r + 1] : 0.f);
                pf[e >> 2][e & 3] = fst; pl[e >> 2][e & 3] = all ^ fst; }
            SB();
            o = mfma32(f0, __builtin_bit_cast(bf16x8, pf[0]), zero); o = mfma32(f1, __builtin_bit_cast(bf16x8, pf[1]), o);
            o = mfma32(l0, __builtin_bit_cast(bf16x8, pl[0]), o); o = mfma32(l1, __builtin_bit_cast(bf16x8, pl[1]), o);
            SB();
        }
#pragma unroll
        for (int i = 1; i < 8; ++i) {
            f32x16& cur = (i & 1) ? sb : sa; f32x16& nxt = (i & 1) ? sa : sb;
            const bf16x8 v0 = vn0, v1 = vn1;
            if (i + 1 < 8) { vn0 = *(LAS const bf16x8*)(vp + (i + 1) * vstr); vn1 = *(LAS const bf16x8*)(vq + (i + 1) * vstr); nxt = mfma32(kf[i + 1], qf, nm); }
            SB();
            u32x4 p0, p1;
#pragma unroll
            for (int e = 0; e < 4; ++e) p0[e] = pk2(EXP2(cur[2 * e]), EXP2(cur[2 * e + 1]));
            SB();
            o = mfma32(v0, __builtin_bit_cast(bf16x8, p0), o);
#pragma unroll
            for (int e = 0; e < 4; ++e) p1[e] = pk2(EXP2(cur[8 + 2 * e]), EXP2(cur[8 + 2 * e + 1]));
            SB();
            o = mfma32(v1, __builtin_bit_cast(bf16x8, p1), o);
            SB();
        }
    } else {
        float z = 0.f; asm volatile("" : "+v"(z));
#pragma unroll
        for (int r = 0; r < 16; ++r) o[r] = z;
#pragma unroll
        for (int i = 0; i < 9; ++i) if (i >= first) {
            f32x16 s = mfma32(kf[i], qf, nm);
            if (i == 0) mask_first(s, q32, h);
            if (i == 8) mask_last(s, q32, h);
            u32x4 p0, p1;
#pragma unroll
            for (int e = 0; e < 4; ++e) { p0[e] = pk2(EXP2(s[2 * e]), EXP2(s[2 * e + 1])); p1[e] = pk2(EXP2(s[8 + 2 * e]), EXP2(s[8 + 2 * e + 1])); }
            const bf16x8 v0 = *(LAS const bf16x8*)(vp + i * vstr), v1 = *(LAS const bf16x8*)(vq + i * vstr);
            o = mfma32(v0, __builtin_bit_cast(bf16x8, p0), o);
            o = mfma32(v1, __builtin_bit_cast(bf16x8, p1), o);
        }
    }
}

struct Fetch { unsigned so, v3, vl; };
constexpr unsigned OOB = 0x7FFFFF00u;
DEV void fetch_pos(Fetch& f, rsrc_t r, int n, int lw, int mem, int lane, u32x4 (&b)[4]) {
    const int c = n >= 2 * NPOS ? 34 : n >= NPOS ? n - NPOS : n;
    if (c == 0 || c == 8 || c == 12 || c == 20 || c == 34) {
        f.so = c == 0 ? WS_WQKV + (mem * 96 + lw * 3) * KB : c == 8 ? WS_WO + ((mem >> 1) * 64 + lw * 4) * KB : c == 12 ? WS_W1 + ((mem < 14 ? mem : 13) * 128 + lw * 4) * KB : WS_W2 + ((mem >> 1) * 224 + lw * 4) * KB;
        f.vl = c == 34 ? OOB : lane * 16; f.v3 = c == 34 || c == 0 ? OOB : lane * 16 + 3072;
    } else f.so += (c < 8 ? 12 : 16) * KB;
    b[0] = ldw(r, f.vl, f.so); SB(); b[1] = ldw(r, f.vl + 1024, f.so); SB(); b[2] = ldw(r, f.vl + 2048, f.so); SB(); b[3] = ldw(r, f.v3, f.so); SB();
}
constexpr int NSET = 7;
DEV void loader(LAS unsigned char* L, unsigned lds0, int lw, int mem, rsrc_t r, int lane) {
    u32x4 buf[NSET][4];
    Fetch f;
#pragma unroll
    for (int i = 0; i < NSET; ++i) { fetch_pos(f, r, i, lw, mem, lane, buf[i]); SB(); }
#define LOADER_WRITE(n_, i_) do { const int n = (n_), c = n >= NPOS ? n - NPOS : n; \
        if (c < 34) { \
            if (n >= 5) lds_wait_vec(L, C_FREE + (lane & 7) * 4, (unsigned)(n == NPOS + 4 ? n - 9 : n - 4), r, lane);        \
            __builtin_amdgcn_s_setprio(WPRIO); \
            LAS unsigned char* dst = L + LD_RING + (c % NSLOT) * SLOT + lw * (c < 8 ? 3072 : 4096) + lane * 16; \
            *(LAS u32x4*)dst = buf[i_][0]; *(LAS u32x4*)(dst + 1024) = buf[i_][1]; *(LAS u32x4*)(dst + 2048) = buf[i_][2]; if (c >= 8) *(LAS u32x4*)(dst + 3072) = buf[i_][3]; \
        } \
        lds_set(lds0 + C_FULL + lw * 4, (unsigned)s2v(n + 1), lane); __builtin_amdgcn_s_setprio(FPRIO); } while (0)
#pragma unroll 1
    for (int layer = 0; layer < 2; ++layer) {
#pragma unroll 1
        for (int n0 = NPOS * layer; n0 < NPOS * layer + NPOS - NSET; n0 += NSET) {
#pragma unroll
            for (int i = 0; i < NSET; ++i) { LOADER_WRITE(n0 + i, i); fetch_pos(f, r, n0 + i + NSET, lw, mem, lane, buf[i]); }
        }
#pragma unroll
        for (int i = 0; i < NSET; ++i) LOADER_WRITE(NPOS * layer + NPOS - NSET + i, i);
        if (layer == 0) lds_wait_vec(L, C_FREE + (lane & 7) * 4, (unsigned)GATE_Q, r, lane);
#pragma unroll
        for (int i = 0; i < NSET; ++i) fetch_pos(f, r, NPOS * (layer + 1) + i, lw, mem, lane, buf[i]);
    }
#undef LOADER_WRITE
}

#define STAGE_BAR(n) do { lds_signal(lds0 + C_BAR, lane); lds_wait_ge(L, C_BAR, 8u * (5 * layer + (n)), r, lane); } while (0)
__global__ __launch_bounds__(NTHR, 3) void k_main(Params p) {
    extern __shared__ __attribute__((aligned(16))) unsigned char lds[];
    int team, mem; block_map(blockIdx.x, team, mem);
    const int tid = threadIdx.x, wid = rfl(tid >> 6); int lane = tid & 63;
    LAS unsigned char* L = (LAS unsigned char*)lds;
    const unsigned lds0 = (unsigned)(size_t)L;
    const rsrc_t r = __builtin_amdgcn_make_buffer_rsrc(p.ws, 0, WS_END, 0x00020000);
    STAMP_RT(0); STAMP_INIT();
    if (tid < 64) ((LAS unsigned*)(L + LD_CTR))[tid] = 0u;
    if (tid >= 64 && tid < 68) ((LAS unsigned*)(L + LD_ONES))[tid & 3] = 0x3f803f80u;
    __syncthreads();
    STAMPV(0, 0);
    if (wid >= NCW) { loader(L, lds0, wid - NCW, mem, r, (int)__builtin_amdgcn_mbcnt_hi(~0u, __builtin_amdgcn_mbcnt_lo(~0u, 0u))); STAMPV(0, 1); STAMP_DUMP(); return; }

    const int cb = mem >> 1, kh = wid & 1, kq = wid & 3;
    const int tb1 = 4 * team + (wid >> 1);
    const int tb2 = 4 * team + 2 * (wid >> 2) + (mem & 1);
    {
        float c[8];
        const bool q = tid < 96; const int col = q ? tid : tid - 128;
        if (q || (tid >= 128 && tid < 256)) {
            const float* src = q ? (const float*)(p.ws + WS_CSQ) + mem * 32 * 96 + col : (const float*)(p.ws + WS_CS1) + (mem < 14 ? mem : 13) * 32 * 128 + col;
            const int st = q ? 96 : 128; float sum = 0.f;
#pragma unroll
            for (int o = 0; o < 4; ++o) {
#pragma unroll
                for (int i = 0; i < 8; ++i) c[i] = src[(8 * o + i) * st];
                sum += ((c[0] + c[1]) + (c[2] + c[3])) + ((c[4] + c[5]) + (c[6] + c[7])); }
            ((LAS float*)(L + (q ? LD_CSQ : LD_CS1)))[col] = sum;
        }
        lds_signal(lds0 + C_ATT, lane);
    }

    for (int layer = 0; layer < 2; ++layer) {
        const unsigned wl = opq(WS_LAYER + layer * L_SIZE), fl = opq(WS_FLAGS + layer * FL_LAYER);
        if (layer) STAMPV(layer, 0);
        {
            lane = opqv(lane);
            float sx, sq;
            f32x16 acc[3];
#pragma unroll
            for (int t = 0; t < 3; ++t) acc[t] = (f32x16){};
            if (layer > 0) wait_flags(r, WS_FLAGS + FL_X2 + tb1 * 32 * WSTR, 0xFFFFu << (16 * kh), lane);
            STAMPV(layer, 1);
            STAMPW(layer, 24, gemm_stage<0, 8, 3, 2, RB_Q, 0, true>(wid, lds, acc, kh * 6144, sx, sq, L, lds0, layer, r, opq((layer == 0 ? WS_XF0 : WS_LAYER + L_X2F) + (tb1 * 32 + 16 * kh) * KB),
                                                                   layer == 0 ? WS_ST0 : WS_LAYER + L_ST2, tb1, kh, lane));
            lane = opqv(lane);
            {
                const unsigned gneed = lane == 0 ? 8u * (layer + 1) : (lane == 1 + (wid >> 2) ? 4u * (4 * layer) : (lane < 3 && layer ? 4u * (4 * layer - 1) : 0u));
                lds_wait_vec(L, lane == 0 ? C_ATT : C_XRD + ((lane - 1) & 1) * 4, gneed, r, lane);
            }
            STAMPV(layer, 2);
            lane = opqv(lane);
            if (lane < 32) ((LAS f32x2*)(L + LD_ST))[wid * 32 + lane] = (f32x2){sx, sq};
            const int blk = 4 * team + (wid >> 1), kb = 8 + (wid >> 1);
            const unsigned kvo = wl + L_KV + (blk * 16 + mem) * 4 * KB;
            if (kh == 0) { asm volatile("; kh 0"); qkv_epi<0>(acc, sx, sq, L, lds0, wid, lane, layer, kb, r, kvo); asm volatile("; kh 0 end"); }
            else { asm volatile("; kh 1"); qkv_epi<1>(acc, sx, sq, L, lds0, wid, lane, layer, kb, r, kvo); asm volatile("; kh 1 end"); }
            lds_signal(lds0 + C_LOCAL + (wid >> 1) * 4, lane);
            STAMPV(layer, 3);
            publish(r, fl + FL_KV + ((blk * 16 + mem) * 2 + kh) * WSTR, lane);
            STAGE_BAR(1);
            STAMPV(layer, 4);
        }
        {
            lane = opqv(lane);
            const int gb = 4 * team - 8 + wid;
            if (gb >= 0) {
                wait_flags(r, fl + FL_KV + (gb * 16 + mem) * 2 * WSTR, 3u, lane);
                STAMPV(layer, 5);
                u32x4 kv[4];
                const unsigned so = opq(wl + L_KV + (gb * 16 + mem) * 4 * KB);
#pragma unroll
                for (int i = 0; i < 4; ++i) kv[i] = ld16(r, lane * 16, so + i * KB);
                lds_wait_vec(L, C_XRD + (lane & 1) * 4, 4u * (4 * layer + 1), r, lane);
#pragma unroll
                for (int i = 0; i < 2; ++i) {
                    *(LAS u32x4*)(L + LD_K + (wid * 2 + i) * 1024 + lane * 16) = kv[i];
                    *(LAS u32x4*)(L + LD_V + (wid * 2 + i) * 1024 + lane * 16) = kv[2 + i];
                }
            }
            lds_signal(lds0 + C_HALO + wid * 4, lane);
            const int qb = wid >> 1, hd = wid & 1;
            STAMPV(layer, 6);
            {
                const bool isl = lane < 4, ish = lane >= 8 && lane < 16;
                const unsigned need = isl ? (lane <= qb ? 2u * (layer + 1) : 0u) : (ish && (lane - 8) >= qb ? (unsigned)(layer + 1) : 0u);
                lds_wait_vec(L, C_LOCAL + (lane & 15) * 4, need, r, lane);
            }
            STAMPV(layer, 7);
            f32x16 o;
            if (__builtin_expect(team >= 2, 1)) attn_unit<true>(L, team, qb, hd, lane, o); else attn_unit<false>(L, team, qb, hd, lane, o);
            STAMPV(layer, 8);
            lane = opqv(lane);
            lds_add1(lds0 + C_ATT, lane);
            const float rl = __builtin_amdgcn_rcpf(hd ? o[0] : o[8]);
            float v[8];
#pragma unroll
            for (int e = 0; e < 8; ++e) v[e] = (hd ? o[8 + e] : o[e]) * rl;
            st16(r, lane * 16, wl + L_OF + ((4 * team + qb) * 32 + 2 * mem + hd) * KB, pack8(v));
            publish(r, fl + FL_O + ((4 * team + qb) * 32 + 2 * mem + hd) * WSTR, lane);
            STAGE_BAR(2);
            STAMPV(layer, 9);
        }
        {
            lane = opqv(lane);
            wait_flags(r, fl + FL_O + tb2 * 32 * WSTR, 0xFFu << (8 * kq), lane);
            STAMPV(layer, 10);
            f32x16 acc[2]; float sx, sq;
            acc[0] = (f32x16){}; acc[1] = (f32x16){};
            u32x4 res = ld16(r, lane * 16, (layer == 0 ? WS_XF0 : WS_LAYER + L_X2F) + (tb2 * 32 + 4 * cb + kq) * KB);
            STAMPW(layer, 25, gemm_stage<8, 4, 2, 99, 9, 0, false>(wid, lds, acc, kq * 4096, sx, sq, L, lds0, layer, r, opq(wl + L_OF + (tb2 * 32 + 8 * kq) * KB), 0, 0, 0, lane));
            STAMPV(layer, 11);
            lane = opqv(lane);
            lds_wait_vec(L, (lane & 1) ? C_ATT : C_XRD + (wid >> 2) * 4, (lane & 1) ? 8u * (layer + 2) : 4u * (4 * layer + 1), r, lane);
            float v[8]; kq_reduce(acc, L, lds0, wid, lane, 4 * layer + 2, r, v);
            resid_add(v, res);
            st16(r, lane * 16, wl + L_X1F + (tb2 * 32 + 4 * cb + kq) * KB, res);
            stats_out(res, r, wl + L_ST1, tb2, 4 * cb + kq, lane);
            STAMPV(layer, 12);
            publish(r, fl + FL_X1 + (tb2 * 32 + 4 * cb + kq) * WSTR, lane);
            STAGE_BAR(3);
            STAMPV(layer, 13);
        }
        {
            lane = opqv(lane);
            wait_flags(r, fl + FL_X1 + tb1 * 32 * WSTR, 0xFFFFu << (16 * kh), lane);
            STAMPV(layer, 14);
            f32x16 acc[4]; float sx, sq;
#pragma unroll
            for (int t = 0; t < 4; ++t) acc[t] = (f32x16){};
            STAMPW(layer, 26, gemm_stage<12, 8, 4, 99, RB_F1, 0, true>(wid, lds, acc, kh * 8192, sx, sq, L, lds0, layer, r, opq(wl + L_X1F + (tb1 * 32 + 16 * kh) * KB), wl + L_ST1, tb1, kh, lane));
            STAMPV(layer, 15);
            lane = opqv(lane); const int t32 = lane & 31, h = lane >> 5;
            lds_wait_ge(L, C_XRD + (wid >> 2) * 4, 4u * (4 * layer + 2), r, lane);
            xch_put(L, wid, 0, lane, acc[2], 0); xch_put(L, wid, 1, lane, acc[2], 1); xch_put(L, wid, 2, lane, acc[3], 0); xch_put(L, wid, 3, lane, acc[3], 1);
            if (h == 0) ((LAS f32x2*)(L + LD_ST))[wid * 32 + t32] = (f32x2){sx, sq};
            lds_signal(lds0 + C_XF + wid * 4, lane);
            LAS const float* csl = (LAS const float*)(L + LD_CS1) + 64 * kh;
            f32x4 cs[8];
#pragma unroll
            for (int i = 0; i < 8; ++i) cs[i] = *(LAS const f32x4*)(csl + 8 * i + 4 * h);
            lds_wait_ge(L, C_XF + (wid ^ 1) * 4, 4u * layer + 3, r, lane);
            const f32x2 os = ((LAS const f32x2*)(L + LD_ST))[(wid ^ 1) * 32 + t32];
            xch_add(L, wid ^ 1, 0, lane, acc[0], 0); xch_add(L, wid ^ 1, 1, lane, acc[0], 1); xch_add(L, wid ^ 1, 2, lane, acc[1], 0); xch_add(L, wid ^ 1, 3, lane, acc[1], 1);
            lds_signal(lds0 + C_XRD + (wid >> 2) * 4, lane);
            float la, lb; ln_coef(os, sx, sq, la, lb);
            if (mem < 14) {
#pragma unroll
                for (int i = 0; i < 4; ++i) {
                    float hv[8], ex[8];
#pragma unroll
                    for (int e = 0; e < 8; ++e) { hv[e] = la * acc[i >> 1][8 * (i & 1) + e] + lb * cs[2 * i + (e >> 2)][e & 3]; ex[e] = -LOG2E * hv[e]; }
                    SB();
#pragma unroll
                    for (int e = 0; e < 8; ++e) ex[e] = __builtin_amdgcn_exp2f(ex[e]);
                    SB();
#pragma unroll
                    for (int e = 0; e < 8; ++e) ex[e] += 1.f;
                    SB();
#pragma unroll
                    for (int e = 0; e < 8; ++e) ex[e] = __builtin_amdgcn_rcpf(ex[e]);
                    SB();
#pragma unroll
                    for (int e = 0; e < 8; ++e) hv[e] *= ex[e];
                    st16(r, lane * 16, wl + L_HF + (tb1 * 112 + 8 * mem + 4 * kh + i) * KB, pack8(hv));
                    SB();
                }
                STAMPV(layer, 16);
                publish(r, fl + FL_H + (tb1 * 32 + 2 * mem + kh) * WSTR, lane);
            }
            STAGE_BAR(4);
            STAMPV(layer, 17);
        }
        {
            lane = opqv(lane);
            wait_flags(r, fl + FL_H + tb2 * 32 * WSTR, (0x5555555u << (kq >> 1)) & 0xFFFFFFFu, lane);
            STAMPV(layer, 18);
            f32x16 acc[2]; float sx, sq;
            acc[0] = (f32x16){}; acc[1] = (f32x16){};
            STAMPW(layer, 27, gemm_stage<20, 14, 2, 99, RB_F2, 1, false>(wid, lds, acc, kq * 4096, sx, sq, L, lds0, layer, r, opq(wl + L_HF + (tb2 * 112 + 2 * kq) * KB), 0, 0, 0, lane));
            STAMPV(layer, 19);
            lane = opqv(lane);
            u32x4 res = ld16(r, lane * 16, wl + L_X1F + (tb2 * 32 + 4 * cb + kq) * KB);
            lds_wait_ge(L, C_XRD + (wid >> 2) * 4, 4u * (4 * layer + 3), r, lane);
            float v[8]; kq_reduce(acc, L, lds0, wid, lane, 4 * layer + 4, r, v);
            resid_add(v, res);
            if (layer == 0) {
                st16(r, lane * 16, wl + L_X2F + (tb2 * 32 + 4 * cb + kq) * KB, res);
                stats_out(res, r, wl + L_ST2, tb2, 4 * cb + kq, lane);
                STAMPV(layer, 20);
                publish(r, fl + FL_X2 + (tb2 * 32 + 4 * cb + kq) * WSTR, lane);
                STAGE_BAR(5);
            } else {
                auto s0 = __builtin_amdgcn_permlane32_swap(res.x, res.z, false, false), s1 = __builtin_amdgcn_permlane32_swap(res.y, res.w, false, false);
                unsigned short* dst = p.out + (size_t)(32 * tb2 + (lane & 31)) * D + 64 * cb + 16 * kq + 8 * (lane >> 5);
                *(u32x4*)dst = (u32x4){s0[0], s1[0], s0[1], s1[1]};
            }
            STAMPV(layer, 21);
        }
    }
    STAMP_RT(1); STAMP_DUMP();
}

extern "C" void kernel_launch(void* const* d_in, const int* in_sizes, int n_in, void* d_out, int out_size, void* d_ws, size_t ws_size, hipStream_t stream) {
    Params p;
    p.x = (const unsigned short*)d_in[0]; p.wqkv = (const unsigned short*)d_in[1]; p.wo = (const unsigned short*)d_in[2];
    p.w1 = (const unsigned short*)d_in[4]; p.w2 = (const unsigned short*)d_in[5];
    p.out = (unsigned short*)d_out; p.ws = (unsigned char*)d_ws;
    (void)hipFuncSetAttribute((const void*)k_main, hipFuncAttributeMaxDynamicSharedMemorySize, LDS_BYTES);
    k_prep<<<(PREP_W + PREP_X) / 4, 256, 0, stream>>>(p);
    k_main<<<256, NTHR, LDS_BYTES, stream>>>(p);
}
```

```cpp
#include <hip/hip_runtime.h>
#include <hip/hip_bf16.h>
#include <stdint.h>

typedef short bf16x8 __attribute__((ext_vector_type(8)));
typedef float f32x2 __attribute__((ext_vector_type(2)));
typedef float f32x4 __attribute__((ext_vector_type(4)));
typedef float f32x8 __attribute__((ext_vector_type(8)));
typedef float f32x16 __attribute__((ext_vector_type(16)));
typedef unsigned u32x2 __attribute__((ext_vector_type(2)));
typedef unsigned u32x4 __attribute__((ext_vector_type(4)));
typedef __bf16 bf16x2_t __attribute__((ext_vector_type(2)));
#define GAS __attribute__((address_space(1)))
#define LAS __attribute__((address_space(3)))
#define DEV __device__ __forceinline__
#define SB() __builtin_amdgcn_sched_barrier(0)
typedef __amdgpu_buffer_rsrc_t rsrc_t;

constexpr int S = 2048, D = 512, H = 32, F = 1792;
constexpr float EPS = 1e-5f;
constexpr float LOG2E = 1.4426950408889634f;
constexpr float QSCALE = 0.25f * LOG2E;

constexpr unsigned KB = 1024, MB = 1024 * 1024;
constexpr unsigned WS_CSQ   = 64 * KB;
constexpr unsigned WS_CS1   = WS_CSQ + 192 * KB;
constexpr unsigned WS_WQKV  = 512 * KB;
constexpr unsigned WS_WO    = WS_WQKV + 1536 * KB;
constexpr unsigned WS_W1    = WS_WO + 512 * KB;
constexpr unsigned WS_W2    = WS_W1 + 1792 * KB;
constexpr unsigned WS_XF0   = WS_W2 + 1792 * KB;
constexpr unsigned WS_ST0   = WS_XF0 + 2 * MB;
constexpr unsigned WS_LAYER = WS_ST0 + 512 * KB;
constexpr unsigned L_KV  = 0;
constexpr unsigned L_OF  = L_KV + 4 * MB;
constexpr unsigned L_X1F = L_OF + 2 * MB;
constexpr unsigned L_HF  = L_X1F + 2 * MB;
constexpr unsigned L_X2F = L_HF + 7 * MB;
constexpr unsigned L_ST1 = L_X2F + 2 * MB;
constexpr unsigned L_ST2 = L_ST1 + 512 * KB;
constexpr unsigned L_SIZE = L_ST2 + 512 * KB;
constexpr unsigned WSTR = 128;
constexpr unsigned WS_FLAGS = 64 * MB;
constexpr unsigned FL_KV = 0, FL_O = FL_KV + 64 * 16 * WSTR, FL_X1 = FL_O + 64 * 32 * WSTR, FL_H = FL_X1 + 64 * 16 * WSTR, FL_X2 = FL_H + 64 * 16 * WSTR, FL_LAYER = FL_X2 + 64 * 16 * WSTR;
constexpr unsigned WS_END = 72 * MB;
static_assert(WS_LAYER + 2 * L_SIZE <= WS_FLAGS && WS_FLAGS + 2 * FL_LAYER <= WS_END, "workspace map");

constexpr int NCW = 4, NLW = 4, NTHR = 64 * (NCW + NLW);
constexpr int SLOT = 16384, NSLOT = 5;
constexpr int LD_RING = 0;
constexpr int LD_XCH = 80 * 1024;
constexpr int LD_K = 96 * 1024;
constexpr int LD_V = 120 * 1024;
constexpr int LD_Q = 144 * 1024;
constexpr int LD_MISC = 152 * 1024;
constexpr int LD_CSQ = LD_MISC  , LD_CS1 = LD_MISC + 512  , LD_ONES = LD_MISC + 1024, LD_CTR = LD_MISC + 1088, LD_DUMMY = LD_CTR + 256  ,
              LD_AB = LD_DUMMY + 512  , LD_STAMP = LD_AB + 1024;
constexpr int C_FULL = LD_CTR  , C_FREE = LD_CTR + 16  , C_XF = LD_CTR + 32  , C_LOCAL = LD_CTR + 48, C_HALO = LD_CTR + 52, C_ATT = LD_CTR + 56, C_BAR = LD_CTR + 60;
constexpr int LDS_BYTES = 160 * 1024;
static_assert(LD_STAMP + 8 * 64 * 4 <= LDS_BYTES && LD_CTR % 16 == 0 && LD_AB % 16 == 0, "LDS map");
constexpr int slot_of(int c) { return c % NSLOT; }
constexpr int NPOS = 35;

struct Params {
    const unsigned short* x; const unsigned short* wqkv; const unsigned short* wo; const unsigned short* w1; const unsigned short* w2;
    unsigned short* out; unsigned char* ws;
};

#define STAMPV(kid, i)
#define STAMPW(kid, i, ...) (void)(__VA_ARGS__)
#define STAMP_RT(i)
#define STAMP_DUMP()
#define STAMP_INIT()

DEV float bf2f(unsigned short b) { return __uint_as_float((unsigned)b << 16); }
DEV float bflo(unsigned u) { return __uint_as_float(u << 16); }
DEV float bfhi(unsigned u) { return __uint_as_float(u & 0xffff0000u); }
DEV unsigned pk2(float lo, float hi) { f32x2 v = {lo, hi}; bf16x2_t r = __builtin_convertvector(v, bf16x2_t); return __builtin_bit_cast(unsigned, r); }
DEV float xsum32(float v) { auto r = __builtin_amdgcn_permlane32_swap(__float_as_uint(v), __float_as_uint(v), false, false); return __uint_as_float(r[0]) + __uint_as_float(r[1]); }
DEV float xmax32(float v) { auto r = __builtin_amdgcn_permlane32_swap(__float_as_uint(v), __float_as_uint(v), false, false); return __builtin_fmaxf(__uint_as_float(r[0]), __uint_as_float(r[1])); }
DEV f32x16 mfma32(bf16x8 a, bf16x8 b, f32x16 c) { return __builtin_amdgcn_mfma_f32_32x32x16_bf16(a, b, c, 0, 0, 0); }
DEV float dot2(unsigned a, unsigned b, float c) { return __builtin_amdgcn_fdot2_f32_bf16(__builtin_bit_cast(bf16x2_t, a), __builtin_bit_cast(bf16x2_t, b), c, false); }
DEV unsigned opq(unsigned x) { asm volatile("" : "+s"(x)); return x; }
DEV int opqv(int x) { asm volatile("" : "+v"(x)); return x; }
DEV unsigned rfl(unsigned v) { return __builtin_amdgcn_readfirstlane(v); }
#define WAITV(n) asm volatile("s_waitcnt vmcnt(" #n ")" ::: "memory")
constexpr int SC1 = 16;
DEV bf16x8 ldf(rsrc_t r, unsigned vo, unsigned so) { return __builtin_bit_cast(bf16x8, __builtin_amdgcn_raw_buffer_load_b128(r, vo, so, SC1)); }
DEV u32x4 ld16(rsrc_t r, unsigned vo, unsigned so) { return __builtin_amdgcn_raw_buffer_load_b128(r, vo, so, SC1); }
DEV u32x4 ldw(rsrc_t r, unsigned vo, unsigned so) { return __builtin_amdgcn_raw_buffer_load_b128(r, vo, so, 0); }
DEV void st16(rsrc_t r, unsigned vo, unsigned so, u32x4 v) { __builtin_amdgcn_raw_buffer_store_b128(v, r, vo, so, SC1); asm volatile("s_nop 1" :: "v"(v) : "memory"); }

DEV void block_map(int b, int& team, int& mem) {
    const int x = b & 7, r = b >> 3;
    team = 4 * (x >> 1) + (r >> 3); mem = 8 * (x & 1) + (r & 7);
}

typedef short v4i16 __attribute__((ext_vector_type(4)));
DEV v4i16 trd(LAS const unsigned char* p) { return __builtin_amdgcn_ds_read_tr16_b64_v4i16((LAS v4i16*)p); }
constexpr int PREP_W = 2816, PREP_X = 512;
__global__ __launch_bounds__(256) void k_prep(Params p) {
    __shared__ __attribute__((aligned(16))) unsigned char tile[4][4096];
    const int lane = threadIdx.x & 63, wv = threadIdx.x >> 6, id = blockIdx.x * 4 + wv;
    LAS unsigned char* T = (LAS unsigned char*)tile[wv];
    for (unsigned i = blockIdx.x * 256 + threadIdx.x; i < 2 * FL_LAYER / 16; i += gridDim.x * 256) ((u32x4*)(p.ws + WS_FLAGS))[i] = (u32x4){0u, 0u, 0u, 0u};
    if (id < PREP_W) {
        const unsigned short* W; int ld, s, g, ty;
        if (id < 768) { ty = 0; W = p.wqkv; ld = 1536; s = id / 24; g = id % 24; }
        else if (id < 1024) { ty = 1; W = p.wo; ld = 512; s = (id - 768) >> 3; g = (id - 768) & 7; }
        else if (id < 1920) { ty = 2; W = p.w1; ld = 1792; s = (id - 1024) / 28; g = (id - 1024) % 28; }
        else { ty = 3; W = p.w2; ld = 512; s = (id - 1920) >> 3; g = (id - 1920) & 7; }
        const unsigned short* src = W + (size_t)(16 * s + (lane >> 3)) * ld + 64 * g + 8 * (lane & 7);
        const u32x4 a = *(const u32x4*)src, b = *(const u32x4*)(src + 8 * ld);
        *(LAS u32x4*)(T + lane * 16) = a; *(LAS u32x4*)(T + 1024 + lane * 16) = b;
        const int h = lane >> 5, nsub = (lane >> 4) & 1, row = 4 * h + ((lane & 15) >> 2);
        const int kh = ty == 1 ? s >> 4 : (s >> 2) & 1;
#pragma unroll
        for (int t = 0; t < 2; ++t) {
            LAS const unsigned char* a0 = T + row * 128 + (2 * t + nsub) * 32 + (lane & 3) * 8;
            const v4i16 lo = trd(a0), hi = trd(a0 + 8 * 128);
            const u32x2 l2 = __builtin_bit_cast(u32x2, lo), h2 = __builtin_bit_cast(u32x2, hi);
            const u32x4 o = {l2.x, l2.y, h2.x, h2.y};
            unsigned dst; float* cs = nullptr;
            if (ty == 0) { const int part = g >> 3, mem = 2 * (g & 7) + t;
                dst = WS_WQKV + (mem * 96 + s * 3 + part) * KB; cs = (float*)(p.ws + WS_CSQ) + (mem * 32 + s) * 96 + part * 32; }
            else if (ty == 1) dst = WS_WO + (g * 64 + ((s & 15) >> 2) * 16 + kh * 8 + (s & 3) * 2 + (t ^ kh)) * KB;
            else if (ty == 2) { const int mem = g >> 1, T4 = 2 * (g & 1) + t;
                dst = WS_W1 + (mem * 128 + s * 4 + T4) * KB; cs = (float*)(p.ws + WS_CS1) + (mem * 32 + s) * 128 + T4 * 32; }
            else dst = WS_W2 + (g * 224 + (s >> 3) * 16 + kh * 8 + (s & 3) * 2 + (t ^ kh)) * KB;
            *(u32x4*)(p.ws + dst + lane * 16) = o;
            if (cs) { const float sum = xsum32(((bflo(o.x) + bfhi(o.x)) + (bflo(o.y) + bfhi(o.y))) + ((bflo(o.z) + bfhi(o.z)) + (bflo(o.w) + bfhi(o.w)))); if (h == 0) cs[lane] = sum; }
        }
    } else if (id < PREP_W + PREP_X) {
        const int i = id - PREP_W, tb = i >> 3, g = i & 7;
        const unsigned short* src = p.x + (size_t)(32 * tb + (lane >> 3)) * D + 64 * g + 8 * (lane & 7);
        u32x4 v[4];
#pragma unroll
        for (int j = 0; j < 4; ++j) v[j] = *(const u32x4*)(src + 8 * j * D);
#pragma unroll
        for (int j = 0; j < 4; ++j) { const int row = 8 * j + (lane >> 3); *(LAS u32x4*)(T + row * 128 + (((lane & 7) ^ (row & 7)) * 16)) = v[j]; }
        const int t32 = lane & 31, h = lane >> 5;
#pragma unroll
        for (int s = 0; s < 4; ++s) {
            const u32x2 a = *(LAS const u32x2*)(T + t32 * 128 + (((2 * s) ^ (t32 & 7)) * 16) + h * 8), b = *(LAS const u32x2*)(T + t32 * 128 + (((2 * s + 1) ^ (t32 & 7)) * 16) + h * 8);
            const int st = 4 * g + s;
            *(u32x4*)(p.ws + WS_XF0 + (tb * 32 + st) * KB + lane * 16) = (u32x4){a.x, a.y, b.x, b.y};
            const float f[8] = {bflo(a.x), bfhi(a.x), bflo(a.y), bfhi(a.y), bflo(b.x), bfhi(b.x), bflo(b.y), bfhi(b.y)};
            const float mean = xsum32(((f[0] + f[1]) + (f[2] + f[3])) + ((f[4] + f[5]) + (f[6] + f[7]))) * (1.f / 16.f); float q = 0.f;
#pragma unroll
            for (int e = 0; e < 8; ++e) { const float d = f[e] - mean; q += d * d; }
            q = xsum32(q);
            if (h == 0) *(f32x2*)(p.ws + WS_ST0 + ((tb * 16 + (st >> 1)) * 32 + t32) * 16 + (st & 1) * 8) = (f32x2){mean, q};
        }
    }
}

#define RB_Q 8
#define RB_O 12
#define RB_F1 8
#define RB_F2 12
#define PD_Q 2
#define PD_F1 2
#define PD_2 3
#define GATE_Q 31
#define WPRIO 0
#define TIGHT 1
#define POLL_DELAY 10
#define LOADPOS 1
#define LAZY 2
#define POLL_SLEEP 1
constexpr unsigned SPIN_CAP = 1u << 15;
DEV void wait_flags(rsrc_t r, unsigned off, unsigned mask, int lane) {
    bool need = lane < 32 && ((mask >> (lane & 31)) & 1u);
    if (POLL_DELAY) __builtin_amdgcn_s_sleep(POLL_DELAY);
#pragma unroll 1
    for (unsigned spins = 0;; ++spins) {
        if (need) need = __builtin_amdgcn_raw_buffer_load_b32(r, lane * WSTR, off, SC1) == 0u;
        if (!__any(need) || spins > SPIN_CAP) break;
        __builtin_amdgcn_s_sleep(POLL_SLEEP);
    }
    __builtin_amdgcn_fence(__ATOMIC_ACQUIRE, "wavefront");
}
DEV void publish(rsrc_t r, unsigned word_off, int lane) {
    WAITV(0);
    if (lane == 0) __builtin_amdgcn_raw_buffer_store_b32(1u, r, 0, word_off, SC1);
}
DEV unsigned lds_ld(LAS unsigned char* L, int off) { return *(volatile LAS unsigned*)(L + off); }
DEV int s2v(int x) { int v; asm volatile("v_mov_b32 %0, %1" : "=v"(v) : "s"(x)); return v; }
DEV unsigned lds_ldu(LAS unsigned char* L, int off) { return *(volatile LAS unsigned*)(L + s2v(off)); }
DEV void lds_add1(unsigned addr, int lane) {
    const unsigned a = lane == 0 ? addr : (unsigned)(LD_DUMMY + lane * 4);
    asm volatile("ds_add_u32 %0, %1" :: "v"(a), "v"(1u) : "memory");
}
DEV void lds_signal(unsigned addr, int lane) { lds_add1(addr, lane); }
DEV void lds_wait_ge(LAS unsigned char* L, int off, unsigned target, rsrc_t r, int lane) {
    if (__builtin_expect(rfl(lds_ldu(L, off)) < target, 0)) {
#pragma unroll 1
        for (unsigned spins = 0; rfl(lds_ldu(L, off)) < target && spins < (SPIN_CAP << 3); ++spins) if (spins > 1) __builtin_amdgcn_s_sleep(1);
    }
    asm volatile("" ::: "memory");
}
DEV void lds_wait_lazy(LAS unsigned char* L, int off, unsigned need, int lane) {
    if (!__all(lds_ld(L, off) >= need)) {
#pragma unroll 1
        for (unsigned spins = 0; spins < (SPIN_CAP << 3); ++spins) { __builtin_amdgcn_s_sleep(LAZY); if (__all(lds_ld(L, off) >= need)) break; }
    }
    asm volatile("" ::: "memory");
}
DEV void lds_wait_vec(LAS unsigned char* L, int off, unsigned need, rsrc_t r, int lane) {
    if (__builtin_expect(!__all(lds_ld(L, off) >= need), 0)) {
#pragma unroll 1
        for (unsigned spins = 0; !__all(lds_ld(L, off) >= need) && spins < (SPIN_CAP << 3); ++spins) if (spins > TIGHT) __builtin_amdgcn_s_sleep(1);
    }
    asm volatile("" ::: "memory");
}

DEV void lds_set(unsigned addr, unsigned val, int lane) {
    const unsigned a = lane == 0 ? addr : (unsigned)(LD_DUMMY + lane * 4);
    asm volatile("ds_write_b32 %0, %1" :: "v"(a), "v"(val) : "memory");
}
DEV unsigned qmin(unsigned v) {
    v = min(v, (unsigned)__builtin_amdgcn_mov_dpp((int)v, 0xB1, 0xF, 0xF, true));
    return min(v, (unsigned)__builtin_amdgcn_mov_dpp((int)v, 0x4E, 0xF, 0xF, true));
}
DEV unsigned ring_ready(LAS unsigned char* L, unsigned pos, int lane) {
    unsigned n;
#pragma unroll 1
    for (unsigned spins = 0;; ++spins) {
        n = rfl(qmin(lds_ld(L, C_FULL + (lane & 3) * 4)));
        if (n > pos || spins > (SPIN_CAP << 3)) break;
        if (spins > TIGHT) __builtin_amdgcn_s_sleep(1);
    }
    asm volatile("" ::: "memory");
    return n;
}
DEV void stats_issue(rsrc_t r, unsigned st, int tb, int lane, f32x4 (&pr)[8]) {
    const unsigned so = opq(st + tb * 16 * 512);
#pragma unroll
    for (int i = 0; i < 8; ++i) pr[i] = __builtin_bit_cast(f32x4, ld16(r, ((lane >> 5) * 8 * 32 + (lane & 31)) * 16 + i * 512, so));
}
DEV void stats_fold(const f32x4 (&pr)[8], float& la, float& lb) {
    float ms = 0.f;
#pragma unroll
    for (int i = 0; i < 8; ++i) ms += pr[i][0] + pr[i][2];
    const float mean = xsum32(ms) * (1.f / 32.f);
    float q = 0.f;
#pragma unroll
    for (int i = 0; i < 8; ++i) { const float d0 = pr[i][0] - mean, d1 = pr[i][2] - mean; q += pr[i][1] + pr[i][3] + 16.f * (d0 * d0 + d1 * d1); }
    la = __builtin_amdgcn_rsqf(xsum32(q) * (1.f / 512.f) + EPS); lb = -mean * la;
}
DEV f32x2 stats16(u32x4 o) {
    const float q8[8] = {bflo(o.x), bfhi(o.x), bflo(o.y), bfhi(o.y), bflo(o.z), bfhi(o.z), bflo(o.w), bfhi(o.w)};
    const float mean = xsum32(((q8[0] + q8[1]) + (q8[2] + q8[3])) + ((q8[4] + q8[5]) + (q8[6] + q8[7]))) * (1.f / 16.f);
    float q = 0.f;
#pragma unroll
    for (int e = 0; e < 8; ++e) { const float d = q8[e] - mean; q += d * d; }
    return (f32x2){mean, xsum32(q)};
}

template <int C0, int NCH, int NT, int SWAPV, int RB, int AMODE, bool ST, int PD>
DEV unsigned gemm_stage(int wid, unsigned char* lds, f32x16 (&acc)[NT], int wsub, float& la, float& lb,
                        LAS unsigned char* L, unsigned lds0, int layer, rsrc_t r, unsigned so, unsigned st, int tb, int lane) {
    unsigned twait = 0;
    constexpr int NS = 4 * NCH, JS = NT * 1024;
    const unsigned l16 = lane * 16;
    bf16x8 b[RB]; f32x4 pl[8];
#define AOFF(s_) (AMODE == 0 ? (s_) * KB : (((s_) >> 2) * 8 + ((s_) & 3)) * KB)
#pragma unroll
    for (int s = 0; s < RB - (LOADPOS == 0 ? 1 : 0); ++s) if (s < NS) b[s] = ldf(r, l16, so + AOFF(s));
    if (ST) stats_issue(r, st, tb, lane, pl);
    LAS const unsigned char* w0 = L + LD_RING + wsub + l16;
    LAS const unsigned char* w1 = L + opqv(LD_RING + 4 * SLOT + wsub + l16);
    const unsigned fadr = lane == 0 ? lds0 + C_FREE + wid * 4 : (unsigned)(LD_DUMMY + lane * 4);
    const unsigned pos0 = NPOS * layer + C0;
#define WADDR(s_) ((slot_of(C0 + ((s_) >> 2)) == 4 ? w1 : w0 + slot_of(C0 + ((s_) >> 2)) * SLOT) + ((s_) & 3) * JS)
    unsigned ready = ring_ready(L, pos0, lane);
    bf16x8 fa[PD][NT];
#pragma unroll
    for (int q = 0; q < PD; ++q)
#pragma unroll
        for (int t = 0; t < NT; ++t) fa[q][t] = *(LAS const bf16x8*)(WADDR(q) + t * 1024);
    SB();
#pragma unroll
    for (int s = 0; s < NS; ++s) {
        const int sp = s + PD;
        if (sp < NS && (sp & 3) == 0) { if (__builtin_expect(pos0 + (sp >> 2) >= ready, 0)) {
            ready = ring_ready(L, pos0 + (sp >> 2), lane);
        } }
        SB();
#pragma unroll
        for (int t = 0; t < NT; ++t) {
            if (t >= SWAPV) acc[t] = mfma32(b[s % RB], fa[s % PD][t], acc[t]); else acc[t] = mfma32(fa[s % PD][t], b[s % RB], acc[t]);
            if (sp < NS) fa[s % PD][t] = *(LAS const bf16x8*)(WADDR(sp) + t * 1024);
            if (t == NT - 1 && s + RB < NS) b[s % RB] = ldf(r, l16, so + AOFF(s + RB));
            SB();
        }
        if ((s & 3) == 3) { const unsigned rel = (unsigned)s2v((int)(pos0 + (s >> 2) + 1)); asm volatile("ds_write_b32 %0, %1" :: "v"(fadr), "v"(rel) : "memory"); }
        if (ST && s == RB) { stats_fold(pl, la, lb); asm volatile("" : "+v"(la), "+v"(lb)); }
        if (s == 0) STAMPV(layer, C0 == 0 ? 28 : C0 == 8 ? 29 : C0 == 12 ? 30 : 31);
    }
#undef AOFF
#undef WADDR
    return twait;
}
DEV void kh_reduce(f32x16 (&acc)[2], LAS unsigned char* L, unsigned lds0, int wid, int lane, unsigned seq, rsrc_t r) {
    LAS f32x4* d = (LAS f32x4*)(L + LD_XCH + wid * 4096) + lane;
#pragma unroll
    for (int u = 0; u < 4; ++u) d[64 * u] = (f32x4){acc[1][4 * u], acc[1][4 * u + 1], acc[1][4 * u + 2], acc[1][4 * u + 3]};
    lds_signal(lds0 + C_XF + wid * 4, lane);
    lds_wait_ge(L, C_XF + (wid ^ 1) * 4, seq, r, lane);
    LAS const f32x4* e = (LAS const f32x4*)(L + LD_XCH + (wid ^ 1) * 4096) + lane;
#pragma unroll
    for (int u = 0; u < 4; ++u) { const f32x4 v = e[64 * u];
#pragma unroll
        for (int i = 0; i < 4; ++i) acc[0][4 * u + i] += v[i]; }
}
DEV u32x4 pack8(const float (&f)[8]) { return (u32x4){pk2(f[0], f[1]), pk2(f[2], f[3]), pk2(f[4], f[5]), pk2(f[6], f[7])}; }
DEV u32x4 resid_add(const f32x16& a, int half, u32x4 res) {
    const float f[8] = {bflo(res.x) + a[8 * half], bfhi(res.x) + a[8 * half + 1], bflo(res.y) + a[8 * half + 2], bfhi(res.y) + a[8 * half + 3],
                        bflo(res.z) + a[8 * half + 4], bfhi(res.z) + a[8 * half + 5], bflo(res.w) + a[8 * half + 6], bfhi(res.w) + a[8 * half + 7]};
    return pack8(f);
}
DEV void resid_store(u32x4 o0, u32x4 o1, rsrc_t r, unsigned xf, unsigned st, int tb, int s, int lane) {
    st16(r, lane * 16, xf + (tb * 32 + s) * KB, o0); st16(r, lane * 16, xf + (tb * 32 + s + 1) * KB, o1);
    const f32x2 a = stats16(o0), b = stats16(o1);
    if (lane < 32) st16(r, lane * 16, st + (tb * 16 + (s >> 1)) * 512, (u32x4){__float_as_uint(a.x), __float_as_uint(a.y), __float_as_uint(b.x), __float_as_uint(b.y)});
}

DEV void qkv_epi(f32x16 (&acc)[3], float la, float lb, LAS unsigned char* L, int wid, int lane, rsrc_t r, unsigned kvo) {
    const int t32 = lane & 31, h = lane >> 5, kb = 8 + wid;
    LAS const float* csl = (LAS const float*)(L + LD_CSQ);
    LAS f32x2* ab = (LAS f32x2*)(L + LD_AB) + wid * 32;
    if (h == 0) ab[t32] = (f32x2){la, lb};
    const float cv = csl[64 + t32];
#pragma unroll
    for (int a = 0; a < 2; ++a) {
        float v[8];
#pragma unroll
        for (int u = 0; u < 2; ++u) { const f32x4 c = *(LAS const f32x4*)(csl + 32 + 16 * a + 8 * u + 4 * h);
#pragma unroll
            for (int e = 0; e < 4; ++e) v[4 * u + e] = la * acc[1][8 * a + 4 * u + e] + lb * c[e]; }
        const u32x4 o = pack8(v);
        st16(r, lane * 16, kvo + a * KB, o);
        *(LAS u32x4*)(L + LD_K + (kb * 2 + a) * 1024 + lane * 16) = o;
    }
#pragma unroll
    for (int a = 0; a < 2; ++a) {
        float v[8];
#pragma unroll
        for (int u = 0; u < 2; ++u) { const f32x4 s01 = *(LAS const f32x4*)(ab + 16 * a + 8 * u + 4 * h), s23 = *(LAS const f32x4*)(ab + 16 * a + 8 * u + 4 * h + 2);
            v[4 * u + 0] = s01[0] * acc[2][8 * a + 4 * u + 0] + s01[1] * cv; v[4 * u + 1] = s01[2] * acc[2][8 * a + 4 * u + 1] + s01[3] * cv;
            v[4 * u + 2] = s23[0] * acc[2][8 * a + 4 * u + 2] + s23[1] * cv; v[4 * u + 3] = s23[2] * acc[2][8 * a + 4 * u + 3] + s23[3] * cv; }
        const u32x4 o = pack8(v);
        st16(r, lane * 16, kvo + (2 + a) * KB, o);
        *(LAS u32x4*)(L + LD_V + (kb * 2 + a) * 1024 + lane * 16) = o;
    }
    const float qa = la * QSCALE, qb_ = lb * QSCALE;
#pragma unroll
    for (int a = 0; a < 2; ++a) {
        float v[8];
#pragma unroll
        for (int u = 0; u < 2; ++u) { const f32x4 c = *(LAS const f32x4*)(csl + 16 * a + 8 * u + 4 * h);
#pragma unroll
            for (int e = 0; e < 4; ++e) v[4 * u + e] = qa * acc[0][8 * a + 4 * u + e] + qb_ * c[e]; }
        *(LAS u32x4*)(L + LD_Q + (wid * 2 + a) * 1024 + lane * 16) = pack8(v);
    }
}
#define EXP2(x) __builtin_amdgcn_exp2f(x)
DEV void mask_first(f32x16& s, int q32, int h) {
#pragma unroll
    for (int r = 0; r < 16; ++r) { const int key = (r & 3) + 8 * (r >> 2) + 4 * h; if (key <= q32) s[r] = -INFINITY; }
}
DEV void mask_last(f32x16& s, int q32, int h) {
#pragma unroll
    for (int r = 0; r < 16; ++r) { const int key = (r & 3) + 8 * (r >> 2) + 4 * h; if (key > q32) s[r] = -INFINITY; }
}
DEV float max16(const f32x16& s, float m) {
    float a = __builtin_fmaxf(__builtin_fmaxf(s[0], s[1]), m), b = __builtin_fmaxf(__builtin_fmaxf(s[2], s[3]), s[4]);
    a = __builtin_fmaxf(__builtin_fmaxf(a, s[5]), s[6]); b = __builtin_fmaxf(__builtin_fmaxf(b, s[7]), s[8]);
    a = __builtin_fmaxf(__builtin_fmaxf(a, s[9]), s[10]); b = __builtin_fmaxf(__builtin_fmaxf(b, s[11]), s[12]);
    a = __builtin_fmaxf(__builtin_fmaxf(a, s[13]), s[14]);
    return __builtin_fmaxf(__builtin_fmaxf(a, b), s[15]);
}
template <bool FULL9>
DEV void attn_unit(LAS const unsigned char* L, int team, int qb, int hd, int lane, f32x16& o) {
    const int q32 = lane & 31, h = lane >> 5;
    const bf16x8 qf = *(LAS const bf16x8*)(L + LD_Q + (qb * 2 + hd) * 1024 + lane * 16);
    const int first = FULL9 ? 0 : 8 - 4 * team - qb;
    LAS const unsigned char* kp = L + LD_K + (qb * 2 + hd) * 1024 + lane * 16;
    const bool onel = ((lane >> 4) & 1) != hd;
    const unsigned vstr = onel ? 0u : 2048u;
    LAS const unsigned char* vp = onel ? L + LD_ONES : L + LD_V + qb * 2048 + lane * 16;
    LAS const unsigned char* vq = onel ? L + LD_ONES : L + LD_V + qb * 2048 + 1024 + lane * 16;
    bf16x8 kf[9];
#pragma unroll
    for (int i = 0; i < 9; ++i) if (FULL9 || i >= first) kf[i] = *(LAS const bf16x8*)(kp + i * 2048);
    const f32x16 zero = {};
    const int dq = q32 - 4 * h;
    float m = -INFINITY;
    if (FULL9) {
        f32x16 sa = mfma32(kf[0], qf, zero), sb = mfma32(kf[8], qf, zero);
        SB();
#pragma unroll
        for (int r = 0; r < 16; ++r) sa[r] = (r & 3) + 8 * (r >> 2) > dq ? sa[r] : sb[r];
        sb = mfma32(kf[1], qf, zero);
        SB();
        m = max16(sa, m);
        SB();
#pragma unroll
        for (int i = 1; i < 8; ++i) {
            f32x16& cur = (i & 1) ? sb : sa; f32x16& nxt = (i & 1) ? sa : sb;
            if (i + 1 < 8) nxt = mfma32(kf[i + 1], qf, zero);
            SB();
            m = max16(cur, m);
            SB();
        }
    } else {
#pragma unroll
        for (int i = 0; i < 9; ++i) if (i >= first) {
            f32x16 s = mfma32(kf[i], qf, zero);
            if (i == 0) mask_first(s, q32, h);
            if (i == 8) mask_last(s, q32, h);
            m = max16(s, m);
        }
    }
    m = xmax32(m);
    f32x16 nm;
#pragma unroll
    for (int r = 0; r < 16; ++r) nm[r] = -m;
    if (FULL9) {
        f32x16 sa = mfma32(kf[0], qf, nm), sb = mfma32(kf[8], qf, nm);
        const bf16x8 f0 = *(LAS const bf16x8*)(vp), f1 = *(LAS const bf16x8*)(vq), l0 = *(LAS const bf16x8*)(vp + 8 * vstr), l1 = *(LAS const bf16x8*)(vq + 8 * vstr);
        SB();
#pragma unroll
        for (int r = 0; r < 16; ++r) sa[r] = (r & 3) + 8 * (r >> 2) > dq ? sa[r] : sb[r];
        sb = mfma32(kf[1], qf, nm);
        bf16x8 vn0 = *(LAS const bf16x8*)(vp + vstr), vn1 = *(LAS const bf16x8*)(vq + vstr);
        SB();
        {
            u32x4 pf[2], pl[2];
#pragma unroll
            for (int r = 0; r < 16; ++r) sa[r] = EXP2(sa[r]);
            SB();
#pragma unroll
            for (int e = 0; e < 8; ++e) { const int r = 2 * e;
                const unsigned all = pk2(sa[r], sa[r + 1]), fst = pk2((r & 3) + 8 * (r >> 2) > dq ? sa[r] : 0.f, ((r + 1) & 3) + 8 * ((r + 1) >> 2) > dq ? sa[r + 1] : 0.f);
                pf[e >> 2][e & 3] = fst; pl[e >> 2][e & 3] = all ^ fst; }
            SB();
            o = mfma32(f0, __builtin_bit_cast(bf16x8, pf[0]), zero); o = mfma32(f1, __builtin_bit_cast(bf16x8, pf[1]), o);
            o = mfma32(l0, __builtin_bit_cast(bf16x8, pl[0]), o); o = mfma32(l1, __builtin_bit_cast(bf16x8, pl[1]), o);
            SB();
        }
#pragma unroll
        for (int i = 1; i < 8; ++i) {
            f32x16& cur = (i & 1) ? sb : sa; f32x16& nxt = (i & 1) ? sa : sb;
            const bf16x8 v0 = vn0, v1 = vn1;
            if (i + 1 < 8) { vn0 = *(LAS const bf16x8*)(vp + (i + 1) * vstr); vn1 = *(LAS const bf16x8*)(vq + (i + 1) * vstr); nxt = mfma32(kf[i + 1], qf, nm); }
            SB();
            u32x4 p0, p1;
#pragma unroll
            for (int e = 0; e < 4; ++e) p0[e] = pk2(EXP2(cur[2 * e]), EXP2(cur[2 * e + 1]));
            SB();
            o = mfma32(v0, __builtin_bit_cast(bf16x8, p0), o);
#pragma unroll
            for (int e = 0; e < 4; ++e) p1[e] = pk2(EXP2(cur[8 + 2 * e]), EXP2(cur[8 + 2 * e + 1]));
            SB();
            o = mfma32(v1, __builtin_bit_cast(bf16x8, p1), o);
            SB();
        }
    } else {
        float z = 0.f; asm volatile("" : "+v"(z));
#pragma unroll
        for (int r = 0; r < 16; ++r) o[r] = z;
#pragma unroll
        for (int i = 0; i < 9; ++i) if (i >= first) {
            f32x16 s = mfma32(kf[i], qf, nm);
            if (i == 0) mask_first(s, q32, h);
            if (i == 8) mask_last(s, q32, h);
            u32x4 p0, p1;
#pragma unroll
            for (int e = 0; e < 4; ++e) { p0[e] = pk2(EXP2(s[2 * e]), EXP2(s[2 * e + 1])); p1[e] = pk2(EXP2(s[8 + 2 * e]), EXP2(s[8 + 2 * e + 1])); }
            const bf16x8 v0 = *(LAS const bf16x8*)(vp + i * vstr), v1 = *(LAS const bf16x8*)(vq + i * vstr);
            o = mfma32(v0, __builtin_bit_cast(bf16x8, p0), o);
            o = mfma32(v1, __builtin_bit_cast(bf16x8, p1), o);
        }
    }
}

DEV void attention(LAS unsigned char* L, unsigned char* lds, unsigned lds0, int wid, int team, int mem, int qb, int hd, int layer, rsrc_t r, unsigned wl, unsigned fl, int lane_) {
    int lane = opqv(lane_);
    lds_wait_vec(L, C_LOCAL + (lane & 1) * 4, 4u * (layer + 1), r, lane);
    STAMPV(layer, 7);
    f32x16 o;
    if (__builtin_expect(team >= 2, 1)) attn_unit<true>(L, team, qb, hd, lane, o); else attn_unit<false>(L, team, qb, hd, lane, o);
    STAMPV(layer, 8);
    lane = opqv(lane);
    lds_add1(lds0 + C_ATT, lane);
    const float rl = __builtin_amdgcn_rcpf(hd ? o[0] : o[8]);
    float v[8];
#pragma unroll
    for (int e = 0; e < 8; ++e) v[e] = (hd ? o[8 + e] : o[e]) * rl;
    st16(r, lane * 16, wl + L_OF + ((4 * team + qb) * 32 + 2 * mem + hd) * KB, pack8(v));
    publish(r, fl + FL_O + ((4 * team + qb) * 32 + 2 * mem + hd) * WSTR, lane);
    STAMPV(layer, 9);
}

constexpr int NSET = 7;
struct Fetch { unsigned sq, so_, s1, s2; };
template <int C> DEV void loader_fetch(u32x4 (&buf)[NSET][4], const Fetch& f, rsrc_t r, int lane) {
    constexpr int NI = C < 8 ? 3 : 4;
    const unsigned so = opq(C < 8 ? f.sq + C * 12 * KB : C < 12 ? f.so_ + (C - 8) * 16 * KB : C < 20 ? f.s1 + (C - 12) * 16 * KB : f.s2 + (C - 20) * 16 * KB);
#pragma unroll
    for (int i = 0; i < NI; ++i) { buf[C % NSET][i] = ldw(r, lane * 16 + i * 1024, so); SB(); }
}
template <int C> DEV void loader_step(u32x4 (&buf)[NSET][4], const Fetch& f, LAS unsigned char* L, unsigned lds0, int lw, int layer, rsrc_t r, int lane) {
    constexpr int NI = C < 8 ? 3 : 4;
    const int n = NPOS * layer + C;
    if (C < 34) {
        if (C >= 5 || layer) lds_wait_lazy(L, C_FREE + (lane & 3) * 4, (unsigned)(C == 4 ? n - 9 : n - 4), lane);
        __builtin_amdgcn_s_setprio(WPRIO);
        LAS unsigned char* dst = L + LD_RING + (C % NSLOT) * SLOT + lw * NI * 1024 + lane * 16;
#pragma unroll
        for (int i = 0; i < NI; ++i) *(LAS u32x4*)(dst + i * 1024) = buf[C % NSET][i];
    }
    lds_set(lds0 + C_FULL + lw * 4, (unsigned)s2v(n + 1), lane); __builtin_amdgcn_s_setprio(0);
    if (C + NSET < 34) loader_fetch<C + NSET < 34 ? C + NSET : 0>(buf, f, r, lane);
    if (C == 34 && layer == 0) {
        lds_wait_vec(L, C_FREE + (lane & 3) * 4, (unsigned)GATE_Q, r, lane);
        loader_fetch<0>(buf, f, r, lane); loader_fetch<1>(buf, f, r, lane); loader_fetch<2>(buf, f, r, lane); loader_fetch<3>(buf, f, r, lane);
        loader_fetch<4>(buf, f, r, lane); loader_fetch<5>(buf, f, r, lane); loader_fetch<6>(buf, f, r, lane);
    }
    SB();
}
template <int B, int... Cs> DEV void loader_steps_(int, u32x4 (&buf)[NSET][4], const Fetch& f, LAS unsigned char* L, unsigned lds0, int lw, int layer, rsrc_t r, int lane);
template <int... Cs> struct seq {};
template <int B, int N, int... Cs> struct mkseq : mkseq<B, N - 1, B + N - 1, Cs...> {};
template <int B, int... Cs> struct mkseq<B, 0, Cs...> { typedef seq<Cs...> type; };
template <int... Cs> DEV void loader_steps(seq<Cs...>, u32x4 (&buf)[NSET][4], const Fetch& f, LAS unsigned char* L, unsigned lds0, int lw, int layer, rsrc_t r, int lane) {
    (loader_step<Cs>(buf, f, L, lds0, lw, layer, r, lane), ...);
}
template <int... Cs> DEV void loader_prime(seq<Cs...>, u32x4 (&buf)[NSET][4], const Fetch& f, rsrc_t r, int lane) { (loader_fetch<Cs>(buf, f, r, lane), ...); }

#define STAGE_BAR(n) do { lds_signal(lds0 + C_BAR, lane); lds_wait_ge(L, C_BAR, 4u * (3 * layer + (n) - 2), r, lane); } while (0)
__global__ __launch_bounds__(NTHR, 2) void k_main(Params p) {
    extern __shared__ __attribute__((aligned(16))) unsigned char lds[];
    int team, mem; block_map(blockIdx.x, team, mem);
    const int tid = threadIdx.x, wid = rfl(tid >> 6); int lane = tid & 63;
    LAS unsigned char* L = (LAS unsigned char*)lds;
    const unsigned lds0 = (unsigned)(size_t)L;
    const rsrc_t r = __builtin_amdgcn_make_buffer_rsrc(p.ws, 0, WS_END, 0x00020000);
    STAMP_RT(0); STAMP_INIT();
    if (tid < 64) ((LAS unsigned*)(L + LD_CTR))[tid] = 0u;
    if (tid >= 64 && tid < 68) ((LAS unsigned*)(L + LD_ONES))[tid & 3] = 0x3f803f80u;
    __syncthreads();
    STAMPV(0, 0);
    const bool helper = wid >= NCW;
    const int w4 = wid & 3;
    const int cb = mem >> 1, kh = wid & 1;
    const int tb1 = 4 * team + w4;
    const int tb2 = 4 * team + 2 * (w4 >> 1) + (mem & 1);
    if (helper) {
        u32x4 buf[NSET][4]; Fetch f;
        f.sq = WS_WQKV + (mem * 96 + w4 * 3) * KB; f.so_ = WS_WO + (cb * 64 + w4 * 4) * KB; f.s1 = WS_W1 + ((mem < 14 ? mem : 13) * 128 + w4 * 4) * KB; f.s2 = WS_W2 + (cb * 224 + w4 * 4) * KB;
        loader_prime(mkseq<0, NSET>::type(), buf, f, r, lane);
        for (int layer = 0; layer < 2; ++layer) {
            const unsigned wl = opq(WS_LAYER + layer * L_SIZE), fl = opq(WS_FLAGS + layer * FL_LAYER);
            lane = opqv(lane);
            loader_steps(mkseq<0, 13>::type(), buf, f, L, lds0, w4, layer, r, lane);
            STAMPV(layer, 4);
            lds_wait_ge(L, C_ATT, 4u + 8u * layer, r, lane);
            const int gb = 4 * team - 8 + 2 * w4;
            if (gb >= 0) {
                const bool need = lane < 2;
                bool nd = need;
#pragma unroll 1
                for (unsigned spins = 0;; ++spins) {
                    if (nd) nd = __builtin_amdgcn_raw_buffer_load_b32(r, lane * 16 * WSTR, fl + FL_KV + (gb * 16 + mem) * WSTR, SC1) == 0u;
                    if (!__any(nd) || spins > SPIN_CAP) break;
                    __builtin_amdgcn_s_sleep(POLL_SLEEP);
                }
                __builtin_amdgcn_fence(__ATOMIC_ACQUIRE, "wavefront");
                STAMPV(layer, 5);
                u32x4 kv[8];
                const unsigned so = opq(wl + L_KV + (gb * 16 + mem) * 4 * KB);
#pragma unroll
                for (int i = 0; i < 8; ++i) kv[i] = ld16(r, lane * 16, so + (i >> 2) * 64 * KB + (i & 3) * KB);
#pragma unroll
                for (int b = 0; b < 2; ++b)
#pragma unroll
                    for (int i = 0; i < 2; ++i) {
                        *(LAS u32x4*)(L + LD_K + ((2 * w4 + b) * 2 + i) * 1024 + lane * 16) = kv[4 * b + i];
                        *(LAS u32x4*)(L + LD_V + ((2 * w4 + b) * 2 + i) * 1024 + lane * 16) = kv[4 * b + 2 + i];
                    }
            }
            lds_signal(lds0 + C_HALO, lane);
            STAMPV(layer, 6);
            attention(L, lds, lds0, wid, team, mem, w4, 1, layer, r, wl, fl, lane);
            lane = opqv(lane);
            loader_steps(mkseq<13, 22>::type(), buf, f, L, lds0, w4, layer, r, lane);
        }
        STAMP_DUMP();
        return;
    }
    {
        float c[8];
        const bool q = tid < 96; const int col = q ? tid : tid - 128;
        if (q || tid >= 128) {
            const float* src = q ? (const float*)(p.ws + WS_CSQ) + mem * 32 * 96 + col : (const float*)(p.ws + WS_CS1) + (mem < 14 ? mem : 13) * 32 * 128 + col;
            const int st = q ? 96 : 128; float sum = 0.f;
#pragma unroll
            for (int o = 0; o < 4; ++o) {
#pragma unroll
                for (int i = 0; i < 8; ++i) c[i] = src[(8 * o + i) * st];
                sum += ((c[0] + c[1]) + (c[2] + c[3])) + ((c[4] + c[5]) + (c[6] + c[7])); }
            ((LAS float*)(L + (q ? LD_CSQ : LD_CS1)))[col] = sum;
        }
        lds_signal(lds0 + C_ATT, lane);
    }

    for (int layer = 0; layer < 2; ++layer) {
        const unsigned wl = opq(WS_LAYER + layer * L_SIZE), fl = opq(WS_FLAGS + layer * FL_LAYER);
        if (layer) STAMPV(layer, 0);
        {
            lane = opqv(lane);
            float la, lb;
            f32x16 acc[3];
#pragma unroll
            for (int t = 0; t < 3; ++t) acc[t] = (f32x16){};
            if (layer > 0) wait_flags(r, WS_FLAGS + FL_X2 + tb1 * 16 * WSTR, 0xFFFFu, lane);
            STAMPV(layer, 1);
            STAMPW(layer, 24, gemm_stage<0, 8, 3, 2, RB_Q, 0, true, PD_Q>(wid, lds, acc, 0, la, lb, L, lds0, layer, r, opq((layer == 0 ? WS_XF0 : WS_LAYER + L_X2F) + tb1 * 32 * KB),
                                                                         layer == 0 ? WS_ST0 : WS_LAYER + L_ST2, tb1, lane));
            STAMPV(layer, 2);
            lane = opqv(lane);
            lds_wait_ge(L, C_ATT, 4u + 8u * layer, r, lane);
            qkv_epi(acc, la, lb, L, wid, lane, r, wl + L_KV + (tb1 * 16 + mem) * 4 * KB);
            lds_signal(lds0 + C_LOCAL, lane);
            STAMPV(layer, 3);
            publish(r, fl + FL_KV + (tb1 * 16 + mem) * WSTR, lane);
            STAMPV(layer, 4);
        }
        attention(L, lds, lds0, wid, team, mem, w4, 0, layer, r, wl, fl, lane);
        {
            lane = opqv(lane);
            wait_flags(r, fl + FL_O + tb2 * 32 * WSTR, 0xFFFFu << (16 * kh), lane);
            STAMPV(layer, 10);
            f32x16 acc[2]; float la, lb;
            acc[0] = (f32x16){}; acc[1] = (f32x16){};
            const unsigned xo = (layer == 0 ? WS_XF0 : WS_LAYER + L_X2F) + (tb2 * 32 + 4 * cb + 2 * kh) * KB;
            const u32x4 r0 = ld16(r, lane * 16, xo), r1 = ld16(r, lane * 16, xo + KB);
            STAMPW(layer, 25, gemm_stage<8, 4, 2, 99, RB_O, 0, false, PD_2>(wid, lds, acc, kh * 8192, la, lb, L, lds0, layer, r, opq(wl + L_OF + (tb2 * 32 + 16 * kh) * KB), 0, 0, lane));
            STAMPV(layer, 11);
            lane = opqv(lane);
            kh_reduce(acc, L, lds0, wid, lane, 2 * layer + 1, r);
            resid_store(resid_add(acc[0], 0, r0), resid_add(acc[0], 1, r1), r, wl + L_X1F, wl + L_ST1, tb2, 4 * cb + 2 * kh, lane);
            STAMPV(layer, 12);
            publish(r, fl + FL_X1 + (tb2 * 16 + 2 * cb + kh) * WSTR, lane);
            STAGE_BAR(3);
            STAMPV(layer, 13);
        }
        {
            lane = opqv(lane);
            wait_flags(r, fl + FL_X1 + tb1 * 16 * WSTR, 0xFFFFu, lane);
            STAMPV(layer, 14);
            f32x16 acc[4]; float la, lb;
#pragma unroll
            for (int t = 0; t < 4; ++t) acc[t] = (f32x16){};
            STAMPW(layer, 26, gemm_stage<12, 8, 4, 99, RB_F1, 0, true, PD_F1>(wid, lds, acc, 0, la, lb, L, lds0, layer, r, opq(wl + L_X1F + tb1 * 32 * KB), wl + L_ST1, tb1, lane));
            STAMPV(layer, 15);
            lane = opqv(lane); const int h = lane >> 5;
            LAS const float* csl = (LAS const float*)(L + LD_CS1);
            if (mem < 14) {
#pragma unroll
                for (int i = 0; i < 8; ++i) {
                    const f32x4 c0 = *(LAS const f32x4*)(csl + 16 * i + 4 * h), c1 = *(LAS const f32x4*)(csl + 16 * i + 8 + 4 * h);
                    float hv[8], ex[8];
#pragma unroll
                    for (int e = 0; e < 8; ++e) { hv[e] = la * acc[i >> 1][8 * (i & 1) + e] + lb * (e < 4 ? c0[e & 3] : c1[e & 3]); ex[e] = -LOG2E * hv[e]; }
                    SB();
#pragma unroll
                    for (int e = 0; e < 8; ++e) ex[e] = __builtin_amdgcn_exp2f(ex[e]);
                    SB();
#pragma unroll
                    for (int e = 0; e < 8; ++e) ex[e] += 1.f;
                    SB();
#pragma unroll
                    for (int e = 0; e < 8; ++e) ex[e] = __builtin_amdgcn_rcpf(ex[e]);
                    SB();
#pragma unroll
                    for (int e = 0; e < 8; ++e) hv[e] *= ex[e];
                    st16(r, lane * 16, wl + L_HF + (tb1 * 112 + 8 * mem + i) * KB, pack8(hv));
                    SB();
                }
                STAMPV(layer, 16);
                publish(r, fl + FL_H + (tb1 * 16 + mem) * WSTR, lane);
            }
            STAGE_BAR(4);
            STAMPV(layer, 17);
        }
        {
            lane = opqv(lane);
            wait_flags(r, fl + FL_H + tb2 * 16 * WSTR, 0x3FFFu, lane);
            STAMPV(layer, 18);
            f32x16 acc[2]; float la, lb;
            acc[0] = (f32x16){}; acc[1] = (f32x16){};
            const unsigned xo = wl + L_X1F + (tb2 * 32 + 4 * cb + 2 * kh) * KB;
            const u32x4 r0 = ld16(r, lane * 16, xo), r1 = ld16(r, lane * 16, xo + KB);
            STAMPW(layer, 27, gemm_stage<20, 14, 2, 99, RB_F2, 1, false, PD_2>(wid, lds, acc, kh * 8192, la, lb, L, lds0, layer, r, opq(wl + L_HF + (tb2 * 112 + 4 * kh) * KB), 0, 0, lane));
            STAMPV(layer, 19);
            lane = opqv(lane);
            kh_reduce(acc, L, lds0, wid, lane, 2 * layer + 2, r);
            const u32x4 o0 = resid_add(acc[0], 0, r0), o1 = resid_add(acc[0], 1, r1);
            if (layer == 0) {
                resid_store(o0, o1, r, wl + L_X2F, wl + L_ST2, tb2, 4 * cb + 2 * kh, lane);
                STAMPV(layer, 20);
                publish(r, fl + FL_X2 + (tb2 * 16 + 2 * cb + kh) * WSTR, lane);
                STAGE_BAR(5);
            } else {
                unsigned short* dst = p.out + (size_t)(32 * tb2 + (lane & 31)) * D + 64 * cb + 32 * kh + 8 * (lane >> 5);
                { auto s0 = __builtin_amdgcn_permlane32_swap(o0.x, o0.z, false, false), s1 = __builtin_amdgcn_permlane32_swap(o0.y, o0.w, false, false); *(u32x4*)dst = (u32x4){s0[0], s1[0], s0[1], s1[1]}; }
                { auto s0 = __builtin_amdgcn_permlane32_swap(o1.x, o1.z, false, false), s1 = __builtin_amdgcn_permlane32_swap(o1.y, o1.w, false, false); *(u32x4*)(dst + 16) = (u32x4){s0[0], s1[0], s0[1], s1[1]}; }
            }
            STAMPV(layer, 21);
        }
    }
    STAMP_RT(1); STAMP_DUMP();
}

extern "C" void kernel_launch(void* const* d_in, const int* in_sizes, int n_in, void* d_out, int out_size, void* d_ws, size_t ws_size, hipStream_t stream) {
    Params p;
    p.x = (const unsigned short*)d_in[0]; p.wqkv = (const unsigned short*)d_in[1]; p.wo = (const unsigned short*)d_in[2];
    p.w1 = (const unsigned short*)d_in[4]; p.w2 = (const unsigned short*)d_in[5];
    p.out = (unsigned short*)d_out; p.ws = (unsigned char*)d_ws;
    (void)hipFuncSetAttribute((const void*)k_main, hipFuncAttributeMaxDynamicSharedMemorySize, LDS_BYTES);
    k_prep<<<(PREP_W + PREP_X) / 4, 256, 0, stream>>>(p);
    k_main<<<256, NTHR, LDS_BYTES, stream>>>(p);
}
```
